```python
import torch

def _softmax(x, axis=-1):
    return torch.softmax(x, dim=axis)
def _gelu(x):
    return torch.nn.functional.gelu(x, approximate="tanh")
def _silu(x):
    return torch.nn.functional.silu(x)
def _sigmoid(x):
    return torch.sigmoid(x)
def _layernorm(x, axis=-1, eps=1e-5):
    m = x.mean(dim=axis, keepdim=True)
    v = x.var(dim=axis, keepdim=True, unbiased=False)
    return (x - m) / torch.sqrt(v + eps)
def _scatter_add(tgt, idx, src, axis=0):
    return tgt.clone().index_add_(axis, idx, src)
def _segment_sum(src, seg, n):
    out = torch.zeros((n,) + src.shape[1:], dtype=src.dtype)
    return out.index_add_(0, seg, src)
def _pad(x, pw):
    flat = tuple(v for pair in reversed(pw) for v in pair)
    return torch.nn.functional.pad(x, flat)

kernel_name = 'recurrent_scan__compressed_kv_cla__mixer_s_mlp__prompt'

def setup_inputs():

    _w = (lambda a: (lambda t: t.to(torch.bfloat16) if t.is_floating_point() else t)(torch.as_tensor(a)))
    def _A_setup():
        B, H, Sq, Sk, D, M = 1, 32, 2048, 2048, 128, 512
        Dc = 4 * D
        return {
            "x": _w((lambda *s: torch.randn(*s, dtype=torch.bfloat16) * 0.05)(B, Sq, M)),
            "c_kv": _w((lambda *s: torch.randn(*s, dtype=torch.bfloat16) * 0.2)(B, Sk, Dc)),
            "w_q": _w((lambda *s: torch.randn(*s, dtype=torch.bfloat16) * 3.2)(M, H, D) * (M ** -0.5)),
            "w_uk": _w((lambda *s: torch.randn(*s, dtype=torch.bfloat16) * 3.2)(Dc, H, D) * (Dc ** -0.5)),
            "w_uv": _w((lambda *s: torch.randn(*s, dtype=torch.bfloat16) * 0.2)(Dc, H, D) * (Dc ** -0.5)),
            "w_o": _w((lambda *s: torch.randn(*s, dtype=torch.bfloat16) * 0.2)(H, D, M) * ((H * D) ** -0.5)),
        }
    _A_in = _A_setup()

    def _B_setup():
        B, S, D, F = 1, 2048, 512, 1376
        return {
            "x":  _w((lambda *s: torch.randn(*s, dtype=torch.bfloat16) * 0.05)(B, S, D)),
            "w1": _w((lambda *s: torch.randn(*s, dtype=torch.bfloat16) * 3.2)(D, D) * (D ** -0.5)),
            "w2": _w((lambda *s: torch.randn(*s, dtype=torch.bfloat16) * 3.2)(D, D) * (D ** -0.5)),
            "w3": _w((lambda *s: torch.randn(*s, dtype=torch.bfloat16) * 3.2)(D, F) * (D ** -0.5)),
            "w4": _w((lambda *s: torch.randn(*s, dtype=torch.bfloat16) * 1.6)(F, D) * (F ** -0.5)),
        }
    _B_in = _B_setup()

    return {
        "A_x": _A_in["x"],
        "A_c_kv": _A_in["c_kv"],
        "A_w_q": _A_in["w_q"],
        "A_w_uk": _A_in["w_uk"],
        "A_w_uv": _A_in["w_uv"],
        "A_w_o": _A_in["w_o"],
        "B_x": _B_in["x"],
        "B_w1": _B_in["w1"],
        "B_w2": _B_in["w2"],
        "B_w3": _B_in["w3"],
        "B_w4": _B_in["w4"],
    }

def reference(A_x, A_c_kv, A_w_q, A_w_uk, A_w_uv, A_w_o, B_x, B_w1, B_w2, B_w3, B_w4):
    def _A_ref(x, c_kv, w_q, w_uk, w_uv, w_o):
        B, Sq, M = x.shape[0], x.shape[1], x.shape[2]
        Sk = c_kv.shape[1]
        H, D = w_q.shape[1], w_q.shape[2]
        q = torch.einsum("bqm,mhd->bhqd", x, w_q)
        k = torch.einsum("bkc,chd->bhkd", c_kv, w_uk)
        v = torch.einsum("bkc,chd->bhkd", c_kv, w_uv)
        scores = torch.einsum("bhqd,bhkd->bhqk", q, k) * (D ** -0.5)
        off = Sk - Sq
        qi = torch.arange(Sq)[:, None]
        kj = torch.arange(Sk)[None, :]
        cmask = kj <= (qi + off)
        scores = torch.where(cmask[None, None], scores, -1e9)
        p = _softmax(scores, axis=-1)
        ctx = torch.einsum("bhqk,bhkd->bhqd", p, v)
        return torch.einsum("bhqd,hdm->bqm", ctx, w_o)

    def _B_ref(x, w1, w2, w3, w4):
        B, S, D = x.shape
        h = _gelu(_layernorm(x, axis=-1) @ w1)
        c = D // 4
        g0 = _pad(h[:, :, 0*c:1*c], ((0, 0), (1, 0), (0, 0)))[:, :S, :]
        g1 = _pad(h[:, :, 1*c:2*c], ((0, 0), (0, 1), (0, 0)))[:, 1:, :]
        g2 = _pad(h[:, :, 2*c:3*c], ((0, 0), (2, 0), (0, 0)))[:, :S, :]
        g3 = _pad(h[:, :, 3*c:   ], ((0, 0), (0, 2), (0, 0)))[:, 2:, :]
        hs = torch.concatenate([g0, g1, g2, g3], axis=-1) @ w2
        x1 = x + hs
        h2 = _gelu(_layernorm(x1, axis=-1) @ w3) @ w4
        return x1 + h2

    x = A_x
    xn = _layernorm(x, axis=-1)
    s = _A_ref(xn, A_c_kv, A_w_q, A_w_uk, A_w_uv, A_w_o)
    g = _silu(_B_ref(xn, B_w1, B_w2, B_w3, B_w4))
    return x + s * g
```

```cpp
#include <hip/hip_runtime.h>
#include <hip/hip_bf16.h>
typedef unsigned short u16;
__device__ __forceinline__ float b2f(u16 v){ return __uint_as_float(((unsigned)v)<<16); }
__device__ __forceinline__ u16 f2b(float f){ unsigned u=__float_as_uint(f); u += 0x7fffu + ((u>>16)&1u); return (u16)(u>>16); }
constexpr int S=2048, M=512, H=32, D=128, HD=4096, F=1376;

__global__ void ln2_kernel(const u16* x, u16* xn, u16* a1){
  int row=blockIdx.x*4+(threadIdx.x>>6), lane=threadIdx.x&63;
  float v[8]; float s=0;
  for(int j=0;j<8;j++){ v[j]=b2f(x[row*M+lane*8+j]); s+=v[j]; }
  for(int o=32;o>=1;o>>=1) s+=__shfl_xor(s,o);
  float m=s/M, q=0; for(int j=0;j<8;j++){ v[j]-=m; q+=v[j]*v[j]; }
  for(int o=32;o>=1;o>>=1) q+=__shfl_xor(q,o);
  float r=1.f/sqrtf(q/M+1e-5f);
  s=0; for(int j=0;j<8;j++){ u16 b=f2b(v[j]*r); xn[row*M+lane*8+j]=b; v[j]=b2f(b); s+=v[j]; }
  for(int o=32;o>=1;o>>=1) s+=__shfl_xor(s,o);
  m=s/M; q=0; for(int j=0;j<8;j++){ v[j]-=m; q+=v[j]*v[j]; }
  for(int o=32;o>=1;o>>=1) q+=__shfl_xor(q,o);
  r=1.f/sqrtf(q/M+1e-5f);
  for(int j=0;j<8;j++) a1[row*M+lane*8+j]=f2b(v[j]*r);
}
__global__ void ln1_kernel(const u16* x, u16* y){
  int row=blockIdx.x*4+(threadIdx.x>>6), lane=threadIdx.x&63;
  float v[8]; float s=0;
  for(int j=0;j<8;j++){ v[j]=b2f(x[row*M+lane*8+j]); s+=v[j]; }
  for(int o=32;o>=1;o>>=1) s+=__shfl_xor(s,o);
  float m=s/M, q=0; for(int j=0;j<8;j++){ v[j]-=m; q+=v[j]*v[j]; }
  for(int o=32;o>=1;o>>=1) q+=__shfl_xor(q,o);
  float r=1.f/sqrtf(q/M+1e-5f);
  for(int j=0;j<8;j++) y[row*M+lane*8+j]=f2b(v[j]*r);
}
__device__ __forceinline__ float gelu_t(float v){ float u=0.7978845608028654f*(v+0.044715f*v*v*v); return 0.5f*v*(1.f+tanhf(u)); }
template<int EPI,int SHIFT>
__global__ void gemm_naive(const u16* A,int lda,const u16* W,int N,int K,u16* C,const u16* res){
  __shared__ float As[16][65]; __shared__ float Ws[16][65];
  int tx=threadIdx.x&15, ty=threadIdx.x>>4; int r0=blockIdx.y*64, c0=blockIdx.x*64;
  float acc[4][4]={};
  for(int k0=0;k0<K;k0+=16){
    for(int i=threadIdx.x;i<64*16;i+=256){ int r=i>>4,k=i&15; int row=r0+r; float v;
      if(SHIFT){ int g=(k0+k)/128; int sh=(g==0)?1:(g==1)?-1:(g==2)?2:-2; int sr=row-sh; v=(sr>=0&&sr<S)?b2f(A[(size_t)sr*lda+k0+k]):0.f; }
      else v=b2f(A[(size_t)row*lda+k0+k]);
      As[k][r]=v; }
    for(int i=threadIdx.x;i<64*16;i+=256){ int k=i>>6,c=i&63; Ws[k][c]=(c0+c<N)?b2f(W[(size_t)(k0+k)*N+c0+c]):0.f; }
    __syncthreads();
    for(int k=0;k<16;k++){ float a[4],b[4]; for(int i=0;i<4;i++){a[i]=As[k][ty*4+i]; b[i]=Ws[k][tx*4+i];}
      for(int i=0;i<4;i++)for(int j=0;j<4;j++)acc[i][j]+=a[i]*b[j]; }
    __syncthreads();
  }
  for(int i=0;i<4;i++)for(int j=0;j<4;j++){ int r=r0+ty*4+i,c=c0+tx*4+j; if(c<N){ float v=acc[i][j];
    v=b2f(f2b(v)); if(EPI==1) v=gelu_t(v); if(res) v=b2f(res[(size_t)r*N+c])+v; C[(size_t)r*N+c]=f2b(v);} }
}
__global__ void attn_naive(const u16* Q,const u16* K,const u16* V,u16* O){
  __shared__ float p[2048]; __shared__ float q[128]; __shared__ float red[256];
  int h=blockIdx.y, r=blockIdx.x; int t=threadIdx.x;
  if(t<128) q[t]=b2f(Q[(size_t)r*HD+h*D+t]);
  __syncthreads();
  float mx=-1e30f;
  for(int k=t;k<=r;k+=256){ float s=0; const u16* kp=K+(size_t)k*HD+h*D; for(int d=0;d<128;d++) s+=q[d]*b2f(kp[d]); s*=0.08838834764831845f; p[k]=s; mx=fmaxf(mx,s);}
  red[t]=mx; __syncthreads(); for(int o=128;o>=1;o>>=1){ if(t<o) red[t]=fmaxf(red[t],red[t+o]); __syncthreads(); }
  mx=red[0]; __syncthreads();
  float sm=0; for(int k=t;k<=r;k+=256){ float e=__expf(p[k]-mx); p[k]=e; sm+=e; }
  red[t]=sm; __syncthreads(); for(int o=128;o>=1;o>>=1){ if(t<o) red[t]+=red[t+o]; __syncthreads(); }
  sm=red[0];
  if(t<128){ float a=0; for(int k=0;k<=r;k++) a+=p[k]*b2f(V[(size_t)k*HD+h*D+t]); O[(size_t)r*HD+h*D+t]=f2b(a/sm); }
}
__global__ void final_kernel(const u16* x,const u16* s,const u16* b,u16* out){
  int i=blockIdx.x*256+threadIdx.x; float bv=b2f(b[i]); float g=bv/(1.f+__expf(-bv)); g=b2f(f2b(g)); float t=b2f(f2b(b2f(s[i])*g)); out[i]=f2b(b2f(x[i])+t);
}
extern "C" void kernel_launch(void* const* d_in,const int* in_sizes,int n_in,void* d_out,int out_size,void* d_ws,size_t ws_size,hipStream_t stream){
  const u16* x=(const u16*)d_in[0]; const u16* ckv=(const u16*)d_in[1]; const u16* wq=(const u16*)d_in[2]; const u16* wuk=(const u16*)d_in[3];
  const u16* wuv=(const u16*)d_in[4]; const u16* wo=(const u16*)d_in[5]; const u16* w1=(const u16*)d_in[7]; const u16* w2=(const u16*)d_in[8];
  const u16* w3=(const u16*)d_in[9]; const u16* w4=(const u16*)d_in[10];
  u16* ws=(u16*)d_ws; size_t o=0; auto al=[&](size_t n){u16* p=ws+o; o+=n; return p;};
  u16* xn=al(S*M); u16* a1=al(S*M); u16* Q=al((size_t)S*HD); u16* K=al((size_t)S*HD); u16* V=al((size_t)S*HD); u16* ctx=al((size_t)S*HD);
  u16* sA=al(S*M); u16* h=al(S*M); u16* x1=al(S*M); u16* a2=al(S*M); u16* h3=al(S*F); u16* bo=al(S*M);
  ln2_kernel<<<S/4,256,0,stream>>>(x,xn,a1);
  gemm_naive<0,0><<<dim3(HD/64,S/64),256,0,stream>>>(xn,M,wq,HD,M,Q,nullptr);
  gemm_naive<0,0><<<dim3(HD/64,S/64),256,0,stream>>>(ckv,M,wuk,HD,M,K,nullptr);
  gemm_naive<0,0><<<dim3(HD/64,S/64),256,0,stream>>>(ckv,M,wuv,HD,M,V,nullptr);
  attn_naive<<<dim3(S,H),256,0,stream>>>(Q,K,V,ctx);
  gemm_naive<0,0><<<dim3(M/64,S/64),256,0,stream>>>(ctx,HD,wo,M,HD,sA,nullptr);
  gemm_naive<1,0><<<dim3(M/64,S/64),256,0,stream>>>(a1,M,w1,M,M,h,nullptr);
  gemm_naive<0,1><<<dim3(M/64,S/64),256,0,stream>>>(h,M,w2,M,M,x1,xn);
  ln1_kernel<<<S/4,256,0,stream>>>(x1,a2);
  gemm_naive<1,0><<<dim3((F+63)/64,S/64),256,0,stream>>>(a2,M,w3,F,M,h3,nullptr);
  gemm_naive<0,0><<<dim3(M/64,S/64),256,0,stream>>>(h3,F,w4,M,F,bo,x1);
  final_kernel<<<S*M/256,256,0,stream>>>(x,sA,bo,(u16*)d_out);
}
```

```cpp
#include <hip/hip_runtime.h>
#include <hip/hip_bf16.h>
typedef unsigned short u16;
typedef short bf16x8 __attribute__((ext_vector_type(8)));
typedef short s16x4 __attribute__((ext_vector_type(4)));
typedef float f32x4 __attribute__((ext_vector_type(4)));
typedef float f32x2 __attribute__((ext_vector_type(2)));
typedef __bf16 bf16x2_t __attribute__((ext_vector_type(2)));
typedef unsigned u32x4 __attribute__((ext_vector_type(4)));
#define LAS __attribute__((address_space(3)))
#define DI __device__ __forceinline__

constexpr int S_ = 2048, DM = 512, NH = 32, HD = 128, NQ = 4096, FF = 1376;
constexpr size_t MB = 1u << 20;
constexpr size_t WS_XNQ = 20 * MB;
constexpr size_t WS_KV = 32 * MB;
constexpr size_t WS_CTX = 64 * MB;

DI unsigned cvtpk(float lo, float hi) { f32x2 v = {lo, hi}; bf16x2_t b = __builtin_convertvector(v, bf16x2_t); return __builtin_bit_cast(unsigned, b); }
DI bf16x8 pack2(const f32x4& a, const f32x4& b, float sc) {
  u32x4 w = {cvtpk(a[0] * sc, a[1] * sc), cvtpk(a[2] * sc, a[3] * sc), cvtpk(b[0] * sc, b[1] * sc), cvtpk(b[2] * sc, b[3] * sc)};
  return __builtin_bit_cast(bf16x8, w);
}
#define MFMA(a, b, c) __builtin_amdgcn_mfma_f32_16x16x32_bf16(a, b, c, 0, 0, 0)
typedef short v4i16_t __attribute__((ext_vector_type(4)));
DI s16x4 vtr(const LAS char* p) { return __builtin_bit_cast(s16x4, __builtin_amdgcn_ds_read_tr16_b64_v4i16((LAS v4i16_t*)p)); }
DI float shx(float v, int o, int lane) { return __int_as_float(__builtin_amdgcn_ds_bpermute((lane ^ o) << 2, __float_as_int(v))); }

template <bool NAT> DI int wswz(int row) { return NAT ? ((row & 3) | ((row >> 1) & 4)) : (row & 7); }
template <bool NAT> DI bf16x8 wfrag(const LAS char* lds, int nb, int lane) {
  const int g = lane >> 4, q = (lane >> 2) & 3, p = lane & 3;
  const int r0 = (NAT ? 8 * g : 4 * g) + q, r1 = r0 + (NAT ? 4 : 16);
  s16x4 a = vtr(lds + r0 * 256 + ((nb ^ wswz<NAT>(r0)) * 32) + p * 8), b = vtr(lds + r1 * 256 + ((nb ^ wswz<NAT>(r1)) * 32) + p * 8);
  return (bf16x8){a[0], a[1], a[2], a[3], b[0], b[1], b[2], b[3]};
}
template <bool NAT> DI void wstage(LAS char* lds, const u16* __restrict__ W, int ldw, int k0, int n0, int nlim, int tid) {
  for (int i = 0; i < 2; i++) { const int idx = tid + 256 * i, kr = idx >> 4, c = idx & 15;
    u32x4 v = (u32x4){0, 0, 0, 0}; if (n0 + c * 8 < nlim) v = *(const u32x4*)(W + (size_t)(k0 + kr) * ldw + n0 + c * 8);
    *(LAS u32x4*)(lds + kr * 256 + ((c ^ (wswz<NAT>(kr) << 1)) * 16)) = v; }
}

#define MF "v_mfma_f32_16x16x32_bf16 "
#define TR "ds_read_b64_tr_b16 "
#define DR "ds_read_b128 "
#define BL "buffer_load_dwordx4 "
#define BS "buffer_store_dwordx4 "
#define CV "v_cvt_pk_bf16_f32 "
#define AR "v_accvgpr_read_b32 "
#define WL "s_waitcnt lgkmcnt"
#define WV "s_waitcnt vmcnt"
#define EX "v_exp_f32 "
#define SA "s_add_u32 "
#define VM "v_mul_f32 "
#define VV "v_mov_b32 "
#define CLOBBERS "v0", "v1", "v2", "v3", "v4", "v5", "v6", "v7", "v8", "v9", "v10", "v11", "v12", "v13", "v14", "v15", "v16", "v17", "v18", "v19", "v20", "v21", "v22", "v23", "v24", "v25", "v26", "v27", "v28", "v29", "v30", "v31", "v32", "v33", "v34", "v35", "v36", "v37", "v38", "v39", "v40", "v41", "v42", "v43", "v44", "v45", "v46", "v47", "v48", "v49", "v50", "v51", "v52", "v53", "v54", "v55", "v56", "v57", "v58", "v59", "v60", "v61", "v62", "v63", "v64", "v65", "v66", "v67", "v68", "v69", "v70", "v71", "v72", "v73", "v74", "v75", "v76", "v77", "v78", "v79", "v80", "v81", "v82", "v83", "v84", "v85", "v86", "v87", "v88", "v89", "v90", "v91", "v92", "v93", "v94", "v95", "v96", "v97", "v98", "v99", "v100", "v101", "v102", "v103", "v104", "v105", "v106", "v107", "v108", "v109", "v110", "v111", "v112", "v113", "v114", "v115", "v116", "v117", "v118", "v119", "v120", "v121", "v122", "v123", "v124", "v125", "v126", "v127", "v128", "v129", "v130", "v131", "v132", "v133", "v134", "v135", "v136", "v137", "v138", "v139", "v140", "v141", "v142", "v143", "v144", "v145", "v146", "v147", "v148", "v149", "v150", "v151", "v152", "v153", "v154", "v155", "v156", "v157", "v158", "v159", "v160", "v161", "v162", "v163", "v164", "v165", "v166", "v167", "v168", "v169", "v170", "v171", "v172", "v173", "v174", "v175", "v176", "v177", "v178", "v179", "v180", "v181", "v182", "v183", "v184", "v185", "v186", "v187", "v188", "v189", "v190", "v191", "v192", "v193", "v194", "v195", "v196", "v197", "v198", "v199", "v200", "v201", "v202", "v203", "v204", "v205", "v206", "v207", "v208", "v209", "v210", "v211", "v212", "v213", "v214", "v215", "v216", "v217", "v218", "v219", "v220", "v221", "v222", "v223", "v224", "v225", "v226", "v227", "v228", "v229", "v230", "v231", "v232", "v233", "v234", "v235", "v236", "v237", "v238", "v239", "v240", "v241", "v242", "v243", "v244", "v245", "v246", "v247", "v248", "v249", "v250", "v251", "v252", "v253", "v254", "v255", "a0", "a1", "a2", "a3", "a4", "a5", "a6", "a7", "a8", "a9", "a10", "a11", "a12", "a13", "a14", "a15", "a16", "a17", "a18", "a19", "a20", "a21", "a22", "a23", "a24", "a25", "a26", "a27", "a28", "a29", "a30", "a31", "a32", "a33", "a34", "a35", "a36", "a37", "a38", "a39", "a40", "a41", "a42", "a43", "a44", "a45", "a46", "a47", "a48", "a49", "a50", "a51", "a52", "a53", "a54", "a55", "a56", "a57", "a58", "a59", "a60", "a61", "a62", "a63", "a64", "a65", "a66", "a67", "a68", "a69", "a70", "a71", "a72", "a73", "a74", "a75", "a76", "a77", "a78", "a79", "a80", "a81", "a82", "a83", "a84", "a85", "a86", "a87", "a88", "a89", "a90", "a91", "a92", "a93", "a94", "a95", "a96", "a97", "a98", "a99", "a100", "a101", "a102", "a103", "a104", "a105", "a106", "a107", "a108", "a109", "a110", "a111", "a112", "a113", "a114", "a115", "a116", "a117", "a118", "a119", "a120", "a121", "a122", "a123", "a124", "a125", "a126", "a127", "a128", "a129", "a130", "a131", "a132", "a133", "a134", "a135", "a136", "a137", "a138", "a139", "a140", "a141", "a142", "a143", "a144", "a145", "a146", "a147", "a148", "a149", "a150", "a151", "a152", "a153", "a154", "a155", "a156", "a157", "a158", "a159", "a160", "a161", "a162", "a163", "a164", "a165", "a166", "a167", "a168", "a169", "a170", "a171", "a172", "a173", "a174", "a175", "a176", "a177", "a178", "a179", "a180", "a181", "a182", "a183", "a184", "a185", "a186", "a187", "a188", "a189", "a190", "a191", "a192", "a193", "a194", "a195", "a196", "a197", "a198", "a199", "a200", "a201", "a202", "a203", "a204", "a205", "a206", "a207", "a208", "a209", "a210", "a211", "a212", "a213", "a214", "a215", "a216", "a217", "a218", "a219", "a220", "a221", "a222", "a223", "a224", "a225", "a226", "a227", "a228", "a229", "a230", "a231", "a232", "a233", "a234", "a235", "a236", "a237", "a238", "a239", "a240", "a241", "a242", "a243", "a244", "a245", "a246", "a247", "a248", "a249", "a250", "a251", "a252", "a253", "a254", "a255", "s16", "s17", "s18", "s19", "s20", "s21", "s22", "s23", "s24", "s25", "s26", "s27", "s28", "s29", "s30", "s31", "s34", "s35", "s36", "s37", "s38", "s39", "s40", "s41", "s42", "s43", "s44", "s45", "s46", "s47", "s48", "s49", "s50", "s51", "s52", "s53", "s54", "s55", "s56", "s57", "s58", "s59", "s60", "s61", "s62", "s63", "s64", "s65", "s66", "s67", "s68", "s69", "s70", "s71", "s72", "s73", "s74", "s75", "s76", "s77", "s78", "s79", "s80", "s81", "s82", "s83", "s84", "s85", "s86", "s87", "s88", "s89", "s90", "s91", "s92", "s93", "s94", "s95", "s96", "s97", "s98", "s99", "vcc", "scc", "memory"
DI unsigned lane_id() { unsigned l; asm volatile("v_mbcnt_lo_u32_b32 %0, -1, 0\n\tv_mbcnt_hi_u32_b32 %0, -1, %0" : "=v"(l)); return l; }

DI void attn_slow_item(int h, int blk, int tid, const u16* __restrict__ wq, char* __restrict__ ws, LAS char* lds) {
  const int lane = tid & 63, w = tid >> 6, r = lane & 15, g = lane >> 4;
  f32x4 qa[8][2] = {};
  for (int k0 = 0; k0 < DM; k0 += 32) {
    __syncthreads(); wstage<false>(lds, wq, NQ, k0, h * 128, NQ, tid); __syncthreads();
    const int ks = k0 >> 5;
    for (int qb = 0; qb < 2; qb++) { const bf16x8 af = *(const bf16x8*)(ws + WS_XNQ + ((size_t)(8 * blk + 2 * w + qb) * 16 + ks) * 1024 + lane * 16);
      for (int db = 0; db < 8; db++) qa[db][qb] = MFMA(wfrag<false>(lds, db, lane), af, qa[db][qb]); }
  }
  const char* kb_ = ws + WS_KV + (size_t)h * MB; const char* vb = kb_ + 524288;
  for (int qb = 0; qb < 2; qb++) {
    const int rho = 8 * blk + 2 * w + qb;
    bf16x8 qf[4]; for (int d4 = 0; d4 < 4; d4++) qf[d4] = pack2(qa[2 * d4][qb], qa[2 * d4 + 1][qb], 1.f);
    f32x4 o[8] = {}; float m = -1e30f, l = 0.f;
    for (int u = 0; u <= (rho >> 1); u++) {
      f32x4 sc[2] = {};
      for (int kb = 0; kb < 2; kb++) { for (int d4 = 0; d4 < 4; d4++) sc[kb] = MFMA(*(const bf16x8*)(kb_ + (size_t)u * 8192 + (kb * 4 + d4) * 1024 + lane * 16), qf[d4], sc[kb]);
        for (int e = 0; e < 4; e++) if (32 * u + 16 * kb + 4 * g + e > 16 * rho + r) sc[kb][e] = -__builtin_inff(); }
      float mx = fmaxf(fmaxf(fmaxf(sc[0][0], sc[0][1]), fmaxf(sc[0][2], sc[0][3])), fmaxf(fmaxf(sc[1][0], sc[1][1]), fmaxf(sc[1][2], sc[1][3])));
      mx = fmaxf(mx, shx(mx, 16, lane)); mx = fmaxf(mx, shx(mx, 32, lane));
      const float mn = fmaxf(m, mx), al = __builtin_amdgcn_exp2f(m - mn); m = mn;
      float ps = 0; for (int kb = 0; kb < 2; kb++) for (int e = 0; e < 4; e++) { sc[kb][e] = __builtin_amdgcn_exp2f(sc[kb][e] - mn); ps += sc[kb][e]; }
      l = l * al + ps;
      const bf16x8 pf = pack2(sc[0], sc[1], 1.f);
      for (int db = 0; db < 8; db++) { o[db] *= al; o[db] = MFMA(*(const bf16x8*)(vb + (size_t)u * 8192 + db * 1024 + lane * 16), pf, o[db]); }
    }
    l += shx(l, 16, lane); l += shx(l, 32, lane); const float il = 1.f / l;
    for (int d4 = 0; d4 < 4; d4++) *(bf16x8*)(ws + WS_CTX + ((size_t)rho * 128 + h * 4 + d4) * 1024 + lane * 16) = pack2(o[2 * d4], o[2 * d4 + 1], il);
  }
}

__global__ __launch_bounds__(256, 1) void mega_kernel(const u16* __restrict__ x_, const u16* __restrict__ ckv_, const u16* __restrict__ wq_, const u16* __restrict__ wuk_,
    const u16* __restrict__ wuv_, const u16* __restrict__ wo_, const u16* __restrict__ w1_, const u16* __restrict__ w2_, const u16* __restrict__ w3_, const u16* __restrict__ w4_,
    u16* __restrict__ out_, char* __restrict__ ws_) {
  extern __shared__ __attribute__((aligned(16))) char dyn_lds[];
  const unsigned w = __builtin_amdgcn_readfirstlane(threadIdx.x >> 6), b = blockIdx.x;
  typedef const __attribute__((address_space(4))) unsigned long long* kptr_t;
  kptr_t ka = (kptr_t)__builtin_amdgcn_kernarg_segment_ptr();
#define GAS __attribute__((address_space(1)))
#define KARG(T, i) ((T)(GAS T)(size_t)ka[i])
  unsigned st;
  asm volatile(
"s_mov_b32 s36,%[w]\n""s_mov_b32 s96,%[b]\n""s_mov_b64 s[34:35],%[ka]\n""s_load_dwordx8 s[60:67],s[34:35],0\n""s_load_dwordx8 s[68:75],s[34:35],32\n""s_load_dwordx8 s[76:83],s[34:35],64\n" WL"(0)\n"
"s_mov_b64 s[52:53],s[82:83]\n""s_mov_b64 s[92:93],s[82:83]\n""s_mov_b32 s94,65536\n""s_mov_b32 s95,0x20000\n""s_mov_b64 s[28:29],s[60:61]\n""s_mov_b64 s[84:85],s[76:77]\n"
"s_mov_b64 s[86:87],s[62:63]\n""s_mov_b64 s[88:89],s[66:67]\n""s_mov_b64 s[90:91],s[68:69]\n""v_writelane_b32 v254,s60,0\n""v_writelane_b32 v254,s61,1\n""v_writelane_b32 v254,s62,2\n"
"v_writelane_b32 v254,s63,3\n""v_writelane_b32 v254,s64,4\n""v_writelane_b32 v254,s65,5\n""v_writelane_b32 v254,s66,6\n""v_writelane_b32 v254,s67,7\n""v_writelane_b32 v254,s68,8\n"
"v_writelane_b32 v254,s69,9\n""v_writelane_b32 v254,s70,10\n""v_writelane_b32 v254,s71,11\n""v_writelane_b32 v254,s72,12\n""v_writelane_b32 v254,s73,13\n""v_writelane_b32 v254,s74,14\n"
"v_writelane_b32 v254,s75,15\n""v_writelane_b32 v254,s76,16\n""v_writelane_b32 v254,s77,17\n""v_writelane_b32 v254,s78,18\n""v_writelane_b32 v254,s79,19\n""v_writelane_b32 v254,s80,20\n"
"v_writelane_b32 v254,s81,21\n""v_writelane_b32 v254,s82,22\n""v_writelane_b32 v254,s83,23\n""s_mov_b32 s54,0\n""s_and_b32 s37,s96,31\n""s_lshr_b32 s47,s96,5\n""s_mov_b64 s[68:69],s[84:85]\n"
"v_mbcnt_lo_u32_b32 v32,-1,0\n""v_mbcnt_hi_u32_b32 v32,-1,v32\n""v_lshrrev_b32 v33,2,v32\n""v_and_b32 v34,3,v32\n""v_lshlrev_b32 v33,6,v33\n""v_lshl_add_u32 v33,v34,3,v33\n""s_lshl_b32 s50,s96,3\n"
"s_lshl_b32 s51,s36,1\n" SA"s50,s50,s51\n""s_lshl_b32 s50,s50,10\n""s_mov_b32 s30,0x200000\n""s_mov_b32 s31,0x20000\n""s_mov_b32 s70,1409024\n""s_mov_b32 s71,0x20000\n" VV"v62,0\n"
"buffer_load_dwordx2 v[60:61],v62,s[92:95],0 offen sc1\n""buffer_load_dwordx2 v[36:37],v33,s[28:31],s50 offen offset:0\n""buffer_load_dwordx2 v[38:39],v33,s[28:31],s50 offen offset:32\n"
"buffer_load_dwordx2 v[40:41],v33,s[28:31],s50 offen offset:1024\n""buffer_load_dwordx2 v[42:43],v33,s[28:31],s50 offen offset:1056\n""s_cmp_ge_u32 s96,86\n""s_cbranch_scc1 Lpcs0_%=\n"
"v_lshrrev_b32 v34,1,v32\n""v_mul_u32_u24 v34,2752,v34\n""v_and_b32 v35,1,v32\n""v_lshl_add_u32 v34,v35,4,v34\n""s_mul_i32 s50,s36,352256\n""s_lshl_b32 s51,s96,5\n" SA"s50,s50,s51\n"
 BL"v[44:47],v34,s[68:71],s50 offen\n" SA"s50,s50,88064\n" BL"v[48:51],v34,s[68:71],s50 offen\n" SA"s50,s50,88064\n" BL"v[52:55],v34,s[68:71],s50 offen\n" SA"s50,s50,88064\n"
 BL"v[56:59],v34,s[68:71],s50 offen\n""Lpcs0_%=:\n""s_getpc_b64 s[60:61]\n"".Lmk_pc0:\n""s_lshr_b32 s57,s96,3\n""s_mul_i32 s57,s57,4224\n""v_mbcnt_lo_u32_b32 v30,-1,0\n"
"v_mbcnt_hi_u32_b32 v30,-1,v30\n""v_min_u32 v30,32,v30\n""v_lshlrev_b32 v30,7,v30\n""v_add_u32 v30,s57,v30\n""s_mov_b32 s57,.Lmk_final-.Lmk_pc0-8\n""v_min_u32 v30,s57,v30\n""v_and_b32 v30,-4,v30\n"
"global_load_dword v31,v30,s[60:61]\n""s_cmp_lt_u32 s36,2\n""s_cselect_b32 s16,s88,s90\n""s_cselect_b32 s17,s89,s91\n""s_mov_b32 s18,0x400000\n""s_mov_b32 s19,0x20000\n""s_mov_b64 s[20:21],s[86:87]\n"
"s_mov_b32 s22,0x200000\n""s_mov_b32 s23,0x20000\n""s_lshl_b32 s50,s37,20\n" SA"s50,s50,33554432\n" SA"s24,s52,s50\n""s_addc_u32 s25,s53,0\n""s_mov_b32 s26,0x100000\n""s_mov_b32 s27,0x20000\n"
"s_lshl_b32 s40,s47,18\n""s_lshl_b32 s50,s36,16\n" SA"s40,s40,s50\n""s_lshl_b32 s42,s36,13\n""s_and_b32 s48,s36,1\n""s_lshr_b32 s49,s36,1\n""s_lshl_b32 s41,s48,17\n""s_lshl_b32 s50,s37,8\n"
 SA"s41,s41,s50\n""s_lshl_b32 s43,s49,13\n""s_lshl_b32 s50,s48,12\n" SA"s43,s43,s50\n""s_lshl_b32 s44,s47,16\n""s_lshl_b32 s50,s49,15\n" SA"s44,s44,s50\n""s_lshl_b32 s50,s48,19\n" SA"s44,s44,s50\n"
"v_mbcnt_lo_u32_b32 v20,-1,0\n""v_mbcnt_hi_u32_b32 v20,-1,v20\n""v_lshlrev_b32 v0,4,v20\n""v_lshrrev_b32 v21,4,v20\n""v_lshrrev_b32 v22,3,v20\n""v_and_b32 v23,7,v20\n""v_add_u32 v24,0,v21\n"
"v_xor_b32 v24,v24,v23\n""v_lshlrev_b32 v24,4,v24\n""v_lshl_add_u32 v2,v22,10,v24\n""v_add_u32 v24,4,v21\n""v_xor_b32 v24,v24,v23\n""v_lshlrev_b32 v24,4,v24\n""v_lshl_add_u32 v3,v22,10,v24\n"
"v_and_b32 v23,15,v20\n""v_or_b32 v24,0,v21\n""v_lshlrev_b32 v24,1,v24\n""v_xor_b32 v24,v24,v23\n""v_lshlrev_b32 v24,4,v24\n""v_lshl_add_u32 v4,v21,13,v24\n""v_or_b32 v24,4,v21\n"
"v_lshlrev_b32 v24,1,v24\n""v_xor_b32 v24,v24,v23\n""v_lshlrev_b32 v24,4,v24\n""v_lshl_add_u32 v5,v21,13,v24\n""v_and_b32 v23,15,v20\n""s_lshl_b32 s50,s49,14\n" SA"s50,s50,65536\n"
"v_lshlrev_b32 v24,7,v23\n""v_add_u32 v24,s50,v24\n""v_lshrrev_b32 v25,1,v23\n""v_add_u32 v26,0,v21\n""v_xor_b32 v26,v26,v25\n""v_lshl_add_u32 v6,v26,4,v24\n""v_add_u32 v8,0x10000,v6\n"
"v_add_u32 v26,4,v21\n""v_xor_b32 v26,v26,v25\n""v_lshl_add_u32 v7,v26,4,v24\n""v_add_u32 v9,0x10000,v7\n""v_bfe_u32 v23,v20,2,2\n""v_lshl_add_u32 v24,v21,3,v23\n""v_lshlrev_b32 v24,8,v24\n"
"v_and_b32 v25,3,v20\n""v_lshl_add_u32 v24,v25,3,v24\n""s_lshl_b32 s50,s48,13\n""v_add_u32 v24,s50,v24\n""v_and_b32 v25,1,v21\n""v_lshl_or_b32 v25,v25,2,v23\n""v_xor_b32 v26,0,v25\n"
"v_lshl_add_u32 v10,v26,5,v24\n""v_xor_b32 v26,1,v25\n""v_lshl_add_u32 v11,v26,5,v24\n""v_xor_b32 v26,2,v25\n""v_lshl_add_u32 v12,v26,5,v24\n""v_xor_b32 v26,3,v25\n""v_lshl_add_u32 v13,v26,5,v24\n"
"v_xor_b32 v26,4,v25\n""v_lshl_add_u32 v14,v26,5,v24\n""v_xor_b32 v26,5,v25\n""v_lshl_add_u32 v15,v26,5,v24\n""v_xor_b32 v26,6,v25\n""v_lshl_add_u32 v16,v26,5,v24\n""v_xor_b32 v26,7,v25\n"
"v_lshl_add_u32 v17,v26,5,v24\n" SA"m0,s43,0\n" SA"s50,s41,0\n" BL"v4,s[16:19],s50 offen lds\n" SA"m0,s43,1024\n" SA"s50,s41,32768\n" BL"v4,s[16:19],s50 offen lds\n" SA"m0,s43,2048\n"
 SA"s50,s41,65536\n" BL"v5,s[16:19],s50 offen lds\n" SA"m0,s43,3072\n" SA"s50,s41,98304\n" BL"v5,s[16:19],s50 offen lds\n" SA"m0,s42,65536\n" SA"s50,s40,0\n" BL"v2,s[20:23],s50 offen lds\n"
 SA"m0,s42,66560\n" SA"s50,s40,8192\n" BL"v3,s[20:23],s50 offen lds\n" SA"m0,s42,67584\n" SA"s50,s40,16384\n" BL"v2,s[20:23],s50 offen lds\n" SA"m0,s42,68608\n" SA"s50,s40,24576\n"
 BL"v3,s[20:23],s50 offen lds\n" SA"m0,s42,69632\n" SA"s50,s40,32768\n" BL"v2,s[20:23],s50 offen lds\n" SA"m0,s42,70656\n" SA"s50,s40,40960\n" BL"v3,s[20:23],s50 offen lds\n" SA"m0,s42,71680\n"
 SA"s50,s40,49152\n" BL"v2,s[20:23],s50 offen lds\n" SA"m0,s42,72704\n" SA"s50,s40,57344\n" BL"v3,s[20:23],s50 offen lds\n" SA"m0,s43,16384\n" SA"s50,s41,262144\n" BL"v4,s[16:19],s50 offen lds\n"
 SA"m0,s43,17408\n" SA"s50,s41,294912\n" BL"v4,s[16:19],s50 offen lds\n" SA"m0,s43,18432\n" SA"s50,s41,327680\n" BL"v5,s[16:19],s50 offen lds\n" SA"m0,s43,19456\n" SA"s50,s41,360448\n"
 BL"v5,s[16:19],s50 offen lds\n" SA"m0,s42,98304\n" SA"s50,s40,128\n" BL"v2,s[20:23],s50 offen lds\n" SA"m0,s42,99328\n" SA"s50,s40,8320\n" BL"v3,s[20:23],s50 offen lds\n" SA"m0,s42,100352\n"
 SA"s50,s40,16512\n" BL"v2,s[20:23],s50 offen lds\n" SA"m0,s42,101376\n" SA"s50,s40,24704\n" BL"v3,s[20:23],s50 offen lds\n" SA"m0,s43,32768\n" SA"s50,s41,524288\n" BL"v4,s[16:19],s50 offen lds\n"
 SA"m0,s43,33792\n" SA"s50,s41,557056\n" BL"v4,s[16:19],s50 offen lds\n" SA"m0,s43,34816\n" SA"s50,s41,589824\n" BL"v5,s[16:19],s50 offen lds\n" SA"m0,s43,35840\n" SA"s50,s41,622592\n"
 BL"v5,s[16:19],s50 offen lds\n" SA"m0,s42,102400\n" SA"s50,s40,32896\n" BL"v2,s[20:23],s50 offen lds\n" SA"m0,s42,103424\n" SA"s50,s40,41088\n" BL"v3,s[20:23],s50 offen lds\n" SA"m0,s42,104448\n"
 SA"s50,s40,49280\n" BL"v2,s[20:23],s50 offen lds\n" SA"m0,s42,105472\n" SA"s50,s40,57472\n" BL"v3,s[20:23],s50 offen lds\n" WV"(29)\n""v_readfirstlane_b32 s98,v60\n""v_readfirstlane_b32 s99,v61\n"
"s_nop 0\n" SA"s98,s98,1\n""s_addc_u32 s99,s99,0\n""v_lshlrev_b32 v64,16,v36\n""v_and_b32 v65,0xffff0000,v36\n""v_lshlrev_b32 v66,16,v37\n""v_and_b32 v67,0xffff0000,v37\n""v_lshlrev_b32 v68,16,v38\n"
"v_and_b32 v69,0xffff0000,v38\n""v_lshlrev_b32 v70,16,v39\n""v_and_b32 v71,0xffff0000,v39\n""v_lshlrev_b32 v80,16,v40\n""v_and_b32 v81,0xffff0000,v40\n""v_lshlrev_b32 v82,16,v41\n"
"v_and_b32 v83,0xffff0000,v41\n""v_lshlrev_b32 v84,16,v42\n""v_and_b32 v85,0xffff0000,v42\n""v_lshlrev_b32 v86,16,v43\n""v_and_b32 v87,0xffff0000,v43\n""s_lshl_b32 s50,s36,5\n" SA"s50,s50,131072\n"
"v_lshlrev_b32 v35,7,v32\n""v_add_u32 v35,s50,v35\n""ds_write_b128 v35,v[36:39] offset:16384\n""ds_write_b128 v35,v[40:43] offset:16400\n""v_add_f32 v96,v64,v65\n""v_add_f32 v97,v80,v81\n"
"v_add_f32 v96,v96,v66\n""v_add_f32 v97,v97,v82\n""v_add_f32 v96,v96,v67\n""v_add_f32 v97,v97,v83\n""v_add_f32 v96,v96,v68\n""v_add_f32 v97,v97,v84\n""v_add_f32 v96,v96,v69\n""v_add_f32 v97,v97,v85\n"
"v_add_f32 v96,v96,v70\n""v_add_f32 v97,v97,v86\n""v_add_f32 v96,v96,v71\n""v_add_f32 v97,v97,v87\n""s_nop 0\n""v_add_f32_dpp v96,v96,v96 row_ror:8 row_mask:0xf bank_mask:0xf\n"
"v_add_f32_dpp v97,v97,v97 row_ror:8 row_mask:0xf bank_mask:0xf\n""s_nop 1\n""v_add_f32_dpp v96,v96,v96 row_ror:4 row_mask:0xf bank_mask:0xf\n"
"v_add_f32_dpp v97,v97,v97 row_ror:4 row_mask:0xf bank_mask:0xf\n""s_nop 1\n""v_add_f32_dpp v96,v96,v96 row_ror:2 row_mask:0xf bank_mask:0xf\n"
"v_add_f32_dpp v97,v97,v97 row_ror:2 row_mask:0xf bank_mask:0xf\n""s_nop 1\n""v_add_f32_dpp v96,v96,v96 row_ror:1 row_mask:0xf bank_mask:0xf\n"
"v_add_f32_dpp v97,v97,v97 row_ror:1 row_mask:0xf bank_mask:0xf\n""s_nop 1\n" VV"v98,v96\n" VV"v99,v97\n""s_nop 1\n""v_permlane16_swap_b32 v96,v98\n""v_permlane16_swap_b32 v97,v99\n""s_nop 1\n"
"v_add_f32 v96,v96,v98\n""v_add_f32 v97,v97,v99\n""s_nop 0\n" VV"v98,v96\n" VV"v99,v97\n""s_nop 1\n""v_permlane32_swap_b32 v96,v98\n""v_permlane32_swap_b32 v97,v99\n""s_nop 1\n"
"v_add_f32 v96,v96,v98\n""v_add_f32 v97,v97,v99\n""s_nop 0\n" VM"v96,0x3b000000,v96\n" VM"v97,0x3b000000,v97\n""v_sub_f32 v64,v64,v96\n""v_sub_f32 v80,v80,v97\n""v_sub_f32 v65,v65,v96\n"
"v_sub_f32 v81,v81,v97\n""v_sub_f32 v66,v66,v96\n""v_sub_f32 v82,v82,v97\n""v_sub_f32 v67,v67,v96\n""v_sub_f32 v83,v83,v97\n""v_sub_f32 v68,v68,v96\n""v_sub_f32 v84,v84,v97\n""v_sub_f32 v69,v69,v96\n"
"v_sub_f32 v85,v85,v97\n""v_sub_f32 v70,v70,v96\n""v_sub_f32 v86,v86,v97\n""v_sub_f32 v71,v71,v96\n""v_sub_f32 v87,v87,v97\n" VM"v96,v64,v64\n" VM"v97,v80,v80\n""v_fmac_f32 v96,v65,v65\n"
"v_fmac_f32 v97,v81,v81\n""v_fmac_f32 v96,v66,v66\n""v_fmac_f32 v97,v82,v82\n""v_fmac_f32 v96,v67,v67\n""v_fmac_f32 v97,v83,v83\n""v_fmac_f32 v96,v68,v68\n""v_fmac_f32 v97,v84,v84\n"
"v_fmac_f32 v96,v69,v69\n""v_fmac_f32 v97,v85,v85\n""v_fmac_f32 v96,v70,v70\n""v_fmac_f32 v97,v86,v86\n""v_fmac_f32 v96,v71,v71\n""v_fmac_f32 v97,v87,v87\n""s_nop 0\n"
"v_add_f32_dpp v96,v96,v96 row_ror:8 row_mask:0xf bank_mask:0xf\n""v_add_f32_dpp v97,v97,v97 row_ror:8 row_mask:0xf bank_mask:0xf\n""s_nop 1\n"
"v_add_f32_dpp v96,v96,v96 row_ror:4 row_mask:0xf bank_mask:0xf\n""v_add_f32_dpp v97,v97,v97 row_ror:4 row_mask:0xf bank_mask:0xf\n""s_nop 1\n"
"v_add_f32_dpp v96,v96,v96 row_ror:2 row_mask:0xf bank_mask:0xf\n""v_add_f32_dpp v97,v97,v97 row_ror:2 row_mask:0xf bank_mask:0xf\n""s_nop 1\n"
"v_add_f32_dpp v96,v96,v96 row_ror:1 row_mask:0xf bank_mask:0xf\n""v_add_f32_dpp v97,v97,v97 row_ror:1 row_mask:0xf bank_mask:0xf\n""s_nop 1\n" VV"v98,v96\n" VV"v99,v97\n""s_nop 1\n"
"v_permlane16_swap_b32 v96,v98\n""v_permlane16_swap_b32 v97,v99\n""s_nop 1\n""v_add_f32 v96,v96,v98\n""v_add_f32 v97,v97,v99\n""s_nop 0\n" VV"v98,v96\n" VV"v99,v97\n""s_nop 1\n"
"v_permlane32_swap_b32 v96,v98\n""v_permlane32_swap_b32 v97,v99\n""s_nop 1\n""v_add_f32 v96,v96,v98\n""v_add_f32 v97,v97,v99\n""s_nop 0\n" VV"v98,0x3727c5ac\n" VV"v99,0x3727c5ac\n"
"v_fmac_f32 v98,0x3b000000,v96\n""v_fmac_f32 v99,0x3b000000,v97\n""v_rsq_f32 v98,v98\n""v_rsq_f32 v99,v99\n""s_nop 0\n" VM"v100,0x3e0293ee,v98\n" VM"v101,0x3e0293ee,v99\n" VM"v104,v64,v100\n"
 VM"v105,v65,v100\n" VM"v106,v66,v100\n" VM"v107,v67,v100\n" VM"v108,v68,v100\n" VM"v109,v69,v100\n" VM"v110,v70,v100\n" VM"v111,v71,v100\n" CV"v120,v104,v105\n" CV"v121,v106,v107\n"
 CV"v122,v108,v109\n" CV"v123,v110,v111\n""ds_write_b128 v35,v[120:123] offset:24576\n" VM"v112,v80,v101\n" VM"v113,v81,v101\n" VM"v114,v82,v101\n" VM"v115,v83,v101\n" VM"v116,v84,v101\n"
 VM"v117,v85,v101\n" VM"v118,v86,v101\n" VM"v119,v87,v101\n" CV"v124,v112,v113\n" CV"v125,v114,v115\n" CV"v126,v116,v117\n" CV"v127,v118,v119\n""ds_write_b128 v35,v[124:127] offset:24592\n"
 VM"v64,v64,v98\n" VM"v65,v65,v98\n" VM"v66,v66,v98\n" VM"v67,v67,v98\n" VM"v68,v68,v98\n" VM"v69,v69,v98\n" VM"v70,v70,v98\n" VM"v71,v71,v98\n" CV"v72,v64,v65\n" CV"v73,v66,v67\n" CV"v74,v68,v69\n"
 CV"v75,v70,v71\n""ds_write_b128 v35,v[72:75] offset:0\n""v_lshlrev_b32 v64,16,v72\n""v_and_b32 v65,0xffff0000,v72\n""v_lshlrev_b32 v66,16,v73\n""v_and_b32 v67,0xffff0000,v73\n"
"v_lshlrev_b32 v68,16,v74\n""v_and_b32 v69,0xffff0000,v74\n""v_lshlrev_b32 v70,16,v75\n""v_and_b32 v71,0xffff0000,v75\n" VM"v80,v80,v99\n" VM"v81,v81,v99\n" VM"v82,v82,v99\n" VM"v83,v83,v99\n"
 VM"v84,v84,v99\n" VM"v85,v85,v99\n" VM"v86,v86,v99\n" VM"v87,v87,v99\n" CV"v88,v80,v81\n" CV"v89,v82,v83\n" CV"v90,v84,v85\n" CV"v91,v86,v87\n""ds_write_b128 v35,v[88:91] offset:16\n"
"v_lshlrev_b32 v80,16,v88\n""v_and_b32 v81,0xffff0000,v88\n""v_lshlrev_b32 v82,16,v89\n""v_and_b32 v83,0xffff0000,v89\n""v_lshlrev_b32 v84,16,v90\n""v_and_b32 v85,0xffff0000,v90\n"
"v_lshlrev_b32 v86,16,v91\n""v_and_b32 v87,0xffff0000,v91\n""v_add_f32 v96,v64,v65\n""v_add_f32 v97,v80,v81\n""v_add_f32 v96,v96,v66\n""v_add_f32 v97,v97,v82\n""v_add_f32 v96,v96,v67\n"
"v_add_f32 v97,v97,v83\n""v_add_f32 v96,v96,v68\n""v_add_f32 v97,v97,v84\n""v_add_f32 v96,v96,v69\n""v_add_f32 v97,v97,v85\n""v_add_f32 v96,v96,v70\n""v_add_f32 v97,v97,v86\n""v_add_f32 v96,v96,v71\n"
"v_add_f32 v97,v97,v87\n""s_nop 0\n""v_add_f32_dpp v96,v96,v96 row_ror:8 row_mask:0xf bank_mask:0xf\n""v_add_f32_dpp v97,v97,v97 row_ror:8 row_mask:0xf bank_mask:0xf\n""s_nop 1\n"
"v_add_f32_dpp v96,v96,v96 row_ror:4 row_mask:0xf bank_mask:0xf\n""v_add_f32_dpp v97,v97,v97 row_ror:4 row_mask:0xf bank_mask:0xf\n""s_nop 1\n"
"v_add_f32_dpp v96,v96,v96 row_ror:2 row_mask:0xf bank_mask:0xf\n""v_add_f32_dpp v97,v97,v97 row_ror:2 row_mask:0xf bank_mask:0xf\n""s_nop 1\n"
"v_add_f32_dpp v96,v96,v96 row_ror:1 row_mask:0xf bank_mask:0xf\n""v_add_f32_dpp v97,v97,v97 row_ror:1 row_mask:0xf bank_mask:0xf\n""s_nop 1\n" VV"v98,v96\n" VV"v99,v97\n""s_nop 1\n"
"v_permlane16_swap_b32 v96,v98\n""v_permlane16_swap_b32 v97,v99\n""s_nop 1\n""v_add_f32 v96,v96,v98\n""v_add_f32 v97,v97,v99\n""s_nop 0\n" VV"v98,v96\n" VV"v99,v97\n""s_nop 1\n"
"v_permlane32_swap_b32 v96,v98\n""v_permlane32_swap_b32 v97,v99\n""s_nop 1\n""v_add_f32 v96,v96,v98\n""v_add_f32 v97,v97,v99\n""s_nop 0\n" VM"v96,0x3b000000,v96\n" VM"v97,0x3b000000,v97\n"
"v_sub_f32 v64,v64,v96\n""v_sub_f32 v80,v80,v97\n""v_sub_f32 v65,v65,v96\n""v_sub_f32 v81,v81,v97\n""v_sub_f32 v66,v66,v96\n""v_sub_f32 v82,v82,v97\n""v_sub_f32 v67,v67,v96\n""v_sub_f32 v83,v83,v97\n"
"v_sub_f32 v68,v68,v96\n""v_sub_f32 v84,v84,v97\n""v_sub_f32 v69,v69,v96\n""v_sub_f32 v85,v85,v97\n""v_sub_f32 v70,v70,v96\n""v_sub_f32 v86,v86,v97\n""v_sub_f32 v71,v71,v96\n""v_sub_f32 v87,v87,v97\n"
 VM"v96,v64,v64\n" VM"v97,v80,v80\n""v_fmac_f32 v96,v65,v65\n""v_fmac_f32 v97,v81,v81\n""v_fmac_f32 v96,v66,v66\n""v_fmac_f32 v97,v82,v82\n""v_fmac_f32 v96,v67,v67\n""v_fmac_f32 v97,v83,v83\n"
"v_fmac_f32 v96,v68,v68\n""v_fmac_f32 v97,v84,v84\n""v_fmac_f32 v96,v69,v69\n""v_fmac_f32 v97,v85,v85\n""v_fmac_f32 v96,v70,v70\n""v_fmac_f32 v97,v86,v86\n""v_fmac_f32 v96,v71,v71\n"
"v_fmac_f32 v97,v87,v87\n""s_nop 0\n""v_add_f32_dpp v96,v96,v96 row_ror:8 row_mask:0xf bank_mask:0xf\n""v_add_f32_dpp v97,v97,v97 row_ror:8 row_mask:0xf bank_mask:0xf\n""s_nop 1\n"
"v_add_f32_dpp v96,v96,v96 row_ror:4 row_mask:0xf bank_mask:0xf\n""v_add_f32_dpp v97,v97,v97 row_ror:4 row_mask:0xf bank_mask:0xf\n""s_nop 1\n"
"v_add_f32_dpp v96,v96,v96 row_ror:2 row_mask:0xf bank_mask:0xf\n""v_add_f32_dpp v97,v97,v97 row_ror:2 row_mask:0xf bank_mask:0xf\n""s_nop 1\n"
"v_add_f32_dpp v96,v96,v96 row_ror:1 row_mask:0xf bank_mask:0xf\n""v_add_f32_dpp v97,v97,v97 row_ror:1 row_mask:0xf bank_mask:0xf\n""s_nop 1\n" VV"v98,v96\n" VV"v99,v97\n""s_nop 1\n"
"v_permlane16_swap_b32 v96,v98\n""v_permlane16_swap_b32 v97,v99\n""s_nop 1\n""v_add_f32 v96,v96,v98\n""v_add_f32 v97,v97,v99\n""s_nop 0\n" VV"v98,v96\n" VV"v99,v97\n""s_nop 1\n"
"v_permlane32_swap_b32 v96,v98\n""v_permlane32_swap_b32 v97,v99\n""s_nop 1\n""v_add_f32 v96,v96,v98\n""v_add_f32 v97,v97,v99\n""s_nop 0\n" VV"v98,0x3727c5ac\n" VV"v99,0x3727c5ac\n"
"v_fmac_f32 v98,0x3b000000,v96\n""v_fmac_f32 v99,0x3b000000,v97\n""v_rsq_f32 v98,v98\n""v_rsq_f32 v99,v99\n""s_nop 0\n" VM"v64,v64,v98\n" VM"v65,v65,v98\n" VM"v66,v66,v98\n" VM"v67,v67,v98\n"
 VM"v68,v68,v98\n" VM"v69,v69,v98\n" VM"v70,v70,v98\n" VM"v71,v71,v98\n" CV"v72,v64,v65\n" CV"v73,v66,v67\n" CV"v74,v68,v69\n" CV"v75,v70,v71\n""ds_write_b128 v35,v[72:75] offset:8192\n"
 VM"v80,v80,v99\n" VM"v81,v81,v99\n" VM"v82,v82,v99\n" VM"v83,v83,v99\n" VM"v84,v84,v99\n" VM"v85,v85,v99\n" VM"v86,v86,v99\n" VM"v87,v87,v99\n" CV"v88,v80,v81\n" CV"v89,v82,v83\n" CV"v90,v84,v85\n"
 CV"v91,v86,v87\n""ds_write_b128 v35,v[88:91] offset:8208\n""s_cmp_ge_u32 s96,86\n""s_cbranch_scc1 Lpcs1_%=\n""v_lshlrev_b32 v100,16,v44\n""v_and_b32 v101,0xffff0000,v44\n"
"v_lshlrev_b32 v102,16,v45\n""v_and_b32 v103,0xffff0000,v45\n""v_lshlrev_b32 v104,16,v46\n""v_and_b32 v105,0xffff0000,v46\n""v_lshlrev_b32 v106,16,v47\n""v_and_b32 v107,0xffff0000,v47\n"
"v_lshlrev_b32 v34,16,v48\n""v_and_b32 v33,0xffff0000,v48\n""v_add_f32 v100,v100,v34\n""v_add_f32 v101,v101,v33\n""v_lshlrev_b32 v34,16,v49\n""v_and_b32 v33,0xffff0000,v49\n"
"v_add_f32 v102,v102,v34\n""v_add_f32 v103,v103,v33\n""v_lshlrev_b32 v34,16,v50\n""v_and_b32 v33,0xffff0000,v50\n""v_add_f32 v104,v104,v34\n""v_add_f32 v105,v105,v33\n""v_lshlrev_b32 v34,16,v51\n"
"v_and_b32 v33,0xffff0000,v51\n""v_add_f32 v106,v106,v34\n""v_add_f32 v107,v107,v33\n""v_lshlrev_b32 v34,16,v52\n""v_and_b32 v33,0xffff0000,v52\n""v_add_f32 v100,v100,v34\n""v_add_f32 v101,v101,v33\n"
"v_lshlrev_b32 v34,16,v53\n""v_and_b32 v33,0xffff0000,v53\n""v_add_f32 v102,v102,v34\n""v_add_f32 v103,v103,v33\n""v_lshlrev_b32 v34,16,v54\n""v_and_b32 v33,0xffff0000,v54\n"
"v_add_f32 v104,v104,v34\n""v_add_f32 v105,v105,v33\n""v_lshlrev_b32 v34,16,v55\n""v_and_b32 v33,0xffff0000,v55\n""v_add_f32 v106,v106,v34\n""v_add_f32 v107,v107,v33\n""v_lshlrev_b32 v34,16,v56\n"
"v_and_b32 v33,0xffff0000,v56\n""v_add_f32 v100,v100,v34\n""v_add_f32 v101,v101,v33\n""v_lshlrev_b32 v34,16,v57\n""v_and_b32 v33,0xffff0000,v57\n""v_add_f32 v102,v102,v34\n""v_add_f32 v103,v103,v33\n"
"v_lshlrev_b32 v34,16,v58\n""v_and_b32 v33,0xffff0000,v58\n""v_add_f32 v104,v104,v34\n""v_add_f32 v105,v105,v33\n""v_lshlrev_b32 v34,16,v59\n""v_and_b32 v33,0xffff0000,v59\n"
"v_add_f32 v106,v106,v34\n""v_add_f32 v107,v107,v33\n""v_add_f32_dpp v100,v100,v100 row_ror:8 row_mask:0xf bank_mask:0xf\n""v_add_f32_dpp v101,v101,v101 row_ror:8 row_mask:0xf bank_mask:0xf\n"
"v_add_f32_dpp v102,v102,v102 row_ror:8 row_mask:0xf bank_mask:0xf\n""v_add_f32_dpp v103,v103,v103 row_ror:8 row_mask:0xf bank_mask:0xf\n"
"v_add_f32_dpp v104,v104,v104 row_ror:8 row_mask:0xf bank_mask:0xf\n""v_add_f32_dpp v105,v105,v105 row_ror:8 row_mask:0xf bank_mask:0xf\n"
"v_add_f32_dpp v106,v106,v106 row_ror:8 row_mask:0xf bank_mask:0xf\n""v_add_f32_dpp v107,v107,v107 row_ror:8 row_mask:0xf bank_mask:0xf\n"
"v_add_f32_dpp v100,v100,v100 row_ror:4 row_mask:0xf bank_mask:0xf\n""v_add_f32_dpp v101,v101,v101 row_ror:4 row_mask:0xf bank_mask:0xf\n"
"v_add_f32_dpp v102,v102,v102 row_ror:4 row_mask:0xf bank_mask:0xf\n""v_add_f32_dpp v103,v103,v103 row_ror:4 row_mask:0xf bank_mask:0xf\n"
"v_add_f32_dpp v104,v104,v104 row_ror:4 row_mask:0xf bank_mask:0xf\n""v_add_f32_dpp v105,v105,v105 row_ror:4 row_mask:0xf bank_mask:0xf\n"
"v_add_f32_dpp v106,v106,v106 row_ror:4 row_mask:0xf bank_mask:0xf\n""v_add_f32_dpp v107,v107,v107 row_ror:4 row_mask:0xf bank_mask:0xf\n"
"v_add_f32_dpp v100,v100,v100 row_ror:2 row_mask:0xf bank_mask:0xf\n""v_add_f32_dpp v101,v101,v101 row_ror:2 row_mask:0xf bank_mask:0xf\n"
"v_add_f32_dpp v102,v102,v102 row_ror:2 row_mask:0xf bank_mask:0xf\n""v_add_f32_dpp v103,v103,v103 row_ror:2 row_mask:0xf bank_mask:0xf\n"
"v_add_f32_dpp v104,v104,v104 row_ror:2 row_mask:0xf bank_mask:0xf\n""v_add_f32_dpp v105,v105,v105 row_ror:2 row_mask:0xf bank_mask:0xf\n"
"v_add_f32_dpp v106,v106,v106 row_ror:2 row_mask:0xf bank_mask:0xf\n""v_add_f32_dpp v107,v107,v107 row_ror:2 row_mask:0xf bank_mask:0xf\n" VV"v108,v100\n" VV"v109,v101\n" VV"v110,v102\n"
 VV"v111,v103\n" VV"v112,v104\n" VV"v113,v105\n" VV"v114,v106\n" VV"v115,v107\n""v_permlane16_swap_b32 v100,v108\n""v_permlane16_swap_b32 v101,v109\n""v_permlane16_swap_b32 v102,v110\n"
"v_permlane16_swap_b32 v103,v111\n""v_permlane16_swap_b32 v104,v112\n""v_permlane16_swap_b32 v105,v113\n""v_permlane16_swap_b32 v106,v114\n""v_permlane16_swap_b32 v107,v115\n"
"v_add_f32 v100,v100,v108\n""v_add_f32 v101,v101,v109\n""v_add_f32 v102,v102,v110\n""v_add_f32 v103,v103,v111\n""v_add_f32 v104,v104,v112\n""v_add_f32 v105,v105,v113\n""v_add_f32 v106,v106,v114\n"
"v_add_f32 v107,v107,v115\n" VV"v108,v100\n" VV"v109,v101\n" VV"v110,v102\n" VV"v111,v103\n" VV"v112,v104\n" VV"v113,v105\n" VV"v114,v106\n" VV"v115,v107\n""v_permlane32_swap_b32 v100,v108\n"
"v_permlane32_swap_b32 v101,v109\n""v_permlane32_swap_b32 v102,v110\n""v_permlane32_swap_b32 v103,v111\n""v_permlane32_swap_b32 v104,v112\n""v_permlane32_swap_b32 v105,v113\n"
"v_permlane32_swap_b32 v106,v114\n""v_permlane32_swap_b32 v107,v115\n""v_add_f32 v100,v100,v108\n""v_add_f32 v101,v101,v109\n""v_add_f32 v102,v102,v110\n""v_add_f32 v103,v103,v111\n"
"v_add_f32 v104,v104,v112\n""v_add_f32 v105,v105,v113\n""v_add_f32 v106,v106,v114\n""v_add_f32 v107,v107,v115\n""s_lshl_b32 s50,s36,6\n" SA"s50,s50,49152\n""v_and_b32 v33,1,v32\n"
"v_lshlrev_b32 v33,5,v33\n""v_add_u32 v33,s50,v33\n""ds_write_b128 v33,v[100:103]\n""ds_write_b128 v33,v[104:107] offset:16\n""Lpcs1_%=:\n" WL"(0)\n""s_barrier\n" SA"s64,s52,0\n"
"s_addc_u32 s65,s53,0\n""s_mov_b32 s66,33554432\n""s_mov_b32 s67,0x20000\n""s_mov_b32 s50,1048576\n""s_cmp_eq_u32 s36,1\n""s_cselect_b32 s50,3145728,s50\n""s_cmp_eq_u32 s36,2\n"
"s_cselect_b32 s50,5242880,s50\n""s_cmp_eq_u32 s36,3\n""s_cselect_b32 s50,20971520,s50\n""s_lshr_b32 s51,s96,1\n""s_lshl_b32 s51,s51,14\n" SA"s50,s50,s51\n""s_and_b32 s51,s96,1\n"
"s_lshl_b32 s51,s51,7\n" SA"s50,s50,s51\n""v_lshrrev_b32 v33,3,v32\n""v_lshlrev_b32 v33,8,v33\n""v_and_b32 v34,7,v32\n""v_lshl_add_u32 v33,v34,4,v33\n""s_lshl_b32 s51,s36,13\n" SA"s51,s51,131072\n"
"v_lshlrev_b32 v34,4,v32\n""v_add_u32 v34,s51,v34\n" DR"v[64:67],v34 offset:0\n" DR"v[68:71],v34 offset:1024\n" DR"v[72:75],v34 offset:2048\n" DR"v[76:79],v34 offset:3072\n"
 DR"v[80:83],v34 offset:4096\n" DR"v[84:87],v34 offset:5120\n" DR"v[88:91],v34 offset:6144\n" DR"v[92:95],v34 offset:7168\n" WL"(7)\n" BS"v[64:67],v33,s[64:67],s50 offen offset:0 sc1\n" WL"(6)\n"
 BS"v[68:71],v33,s[64:67],s50 offen offset:2048 sc1\n" SA"s50,s50,4096\n" WL"(5)\n" BS"v[72:75],v33,s[64:67],s50 offen offset:0 sc1\n" WL"(4)\n" BS"v[76:79],v33,s[64:67],s50 offen offset:2048 sc1\n"
 SA"s50,s50,4096\n" WL"(3)\n" BS"v[80:83],v33,s[64:67],s50 offen offset:0 sc1\n" WL"(2)\n" BS"v[84:87],v33,s[64:67],s50 offen offset:2048 sc1\n" SA"s50,s50,4096\n" WL"(1)\n"
 BS"v[88:91],v33,s[64:67],s50 offen offset:0 sc1\n" WL"(0)\n" BS"v[92:95],v33,s[64:67],s50 offen offset:2048 sc1\n" SA"s50,s50,4096\n""s_cmp_ge_u32 s96,86\n""s_cbranch_scc1 Lpcs2_%=\n"
"s_cmp_lg_u32 s36,0\n""s_cbranch_scc1 Lpcs2_%=\n""v_and_b32 v33,15,v32\n""v_lshlrev_b32 v33,2,v33\n""v_add_u32 v34,49152,v33\n""ds_read_b32 v100,v34 offset:0\n""ds_read_b32 v101,v34 offset:64\n"
"ds_read_b32 v102,v34 offset:128\n""ds_read_b32 v103,v34 offset:192\n" WL"(0)\n""v_add_f32 v100,v100,v101\n""v_add_f32 v102,v102,v103\n""v_add_f32 v100,v100,v102\n""s_lshl_b32 s50,s96,6\n"
 SA"s50,s50,24117248\n""s_mov_b64 exec,0xffff\n""buffer_store_dword v100,v33,s[64:67],s50 offen sc1\n""s_mov_b64 exec,-1\n""Lpcs2_%=:\n""s_cmp_lg_u32 s96,0\n""s_cbranch_scc1 Lpz_%=\n"
"s_lshl_b32 s50,s36,6\n""v_add_u32 v33,s50,v32\n""v_lshrrev_b32 v34,2,v33\n""v_mul_u32_u24 v34,2056,v34\n""v_and_b32 v33,3,v33\n""v_cmp_lt_u32 vcc,1,v33\n""v_add_u32 v35,2048,v33\n""s_nop 0\n"
"v_cndmask_b32 v33,v33,v35,vcc\n""v_add_u32 v33,v33,v34\n""v_lshlrev_b32 v33,4,v33\n" VV"v100,0\n" VV"v101,0\n" VV"v102,0\n" VV"v103,0\n""s_mov_b32 s50,11534336\n"
 BS"v[100:103],v33,s[64:67],s50 offen sc1\n""Lpz_%=:\n""s_cmp_eq_u32 s48,1\n""s_cbranch_scc1 Lkvv_%=\n" WV"(26)\n""s_barrier\n" DR"v[32:35],v6 offset:0\n" DR"v[36:39],v6 offset:2048\n"
 DR"v[40:43],v6 offset:4096\n" DR"v[44:47],v6 offset:6144\n" DR"v[48:51],v6 offset:8192\n" DR"v[52:55],v6 offset:10240\n" DR"v[56:59],v6 offset:12288\n" DR"v[60:63],v6 offset:14336\n"
 TR"v[64:65],v10 offset:0\n" TR"v[66:67],v10 offset:1024\n" TR"v[68:69],v11 offset:0\n" TR"v[70:71],v11 offset:1024\n" TR"v[72:73],v12 offset:0\n" TR"v[74:75],v12 offset:1024\n"
 TR"v[76:77],v13 offset:0\n" WL"(8)\n" TR"v[78:79],v13 offset:1024\n" TR"v[80:81],v14 offset:0\n" TR"v[82:83],v14 offset:1024\n" TR"v[84:85],v15 offset:0\n" TR"v[86:87],v15 offset:1024\n"
 TR"v[88:89],v16 offset:0\n" TR"v[90:91],v16 offset:1024\n" WL"(8)\n" TR"v[92:93],v17 offset:0\n" TR"v[94:95],v17 offset:1024\n" MF"a[0:3],v[64:67],v[32:35],0\n" WV"(22) lgkmcnt(0)\n""s_barrier\n"
 MF"a[4:7],v[64:67],v[36:39],0\n" MF"a[8:11],v[64:67],v[40:43],0\n" DR"v[96:99],v7 offset:0\n" MF"a[12:15],v[64:67],v[44:47],0\n" MF"a[16:19],v[64:67],v[48:51],0\n" DR"v[100:103],v7 offset:2048\n"
 MF"a[20:23],v[64:67],v[52:55],0\n" SA"m0,s43,49152\n" SA"s50,s41,786432\n" BL"v4,s[16:19],s50 offen lds\n" MF"a[24:27],v[64:67],v[56:59],0\n" DR"v[104:107],v7 offset:4096\n"
 MF"a[28:31],v[64:67],v[60:63],0\n" MF"a[32:35],v[68:71],v[32:35],0\n" DR"v[108:111],v7 offset:6144\n" MF"a[36:39],v[68:71],v[36:39],0\n" MF"a[40:43],v[68:71],v[40:43],0\n"
 DR"v[112:115],v7 offset:8192\n" MF"a[44:47],v[68:71],v[44:47],0\n" MF"a[48:51],v[68:71],v[48:51],0\n" DR"v[116:119],v7 offset:10240\n" SA"m0,s43,50176\n" SA"s50,s41,819200\n"
 BL"v4,s[16:19],s50 offen lds\n" MF"a[52:55],v[68:71],v[52:55],0\n" MF"a[56:59],v[68:71],v[56:59],0\n" DR"v[120:123],v7 offset:12288\n" MF"a[60:63],v[68:71],v[60:63],0\n"
 MF"a[64:67],v[72:75],v[32:35],0\n" DR"v[124:127],v7 offset:14336\n" MF"a[68:71],v[72:75],v[36:39],0\n" MF"a[72:75],v[72:75],v[40:43],0\n" MF"a[76:79],v[72:75],v[44:47],0\n"
 TR"v[128:129],v10 offset:16384\n" TR"v[130:131],v10 offset:17408\n" SA"m0,s43,51200\n" SA"s50,s41,851968\n" BL"v5,s[16:19],s50 offen lds\n" MF"a[80:83],v[72:75],v[48:51],0\n"
 MF"a[84:87],v[72:75],v[52:55],0\n" MF"a[88:91],v[72:75],v[56:59],0\n" MF"a[92:95],v[72:75],v[60:63],0\n" MF"a[96:99],v[76:79],v[32:35],0\n" TR"v[132:133],v11 offset:16384\n"
 TR"v[134:135],v11 offset:17408\n" MF"a[100:103],v[76:79],v[36:39],0\n" MF"a[104:107],v[76:79],v[40:43],0\n" SA"m0,s43,52224\n" SA"s50,s41,884736\n" BL"v5,s[16:19],s50 offen lds\n"
 MF"a[108:111],v[76:79],v[44:47],0\n" MF"a[112:115],v[76:79],v[48:51],0\n" MF"a[116:119],v[76:79],v[52:55],0\n" TR"v[136:137],v12 offset:16384\n" TR"v[138:139],v12 offset:17408\n"
 MF"a[120:123],v[76:79],v[56:59],0\n" MF"a[124:127],v[76:79],v[60:63],0\n" MF"a[128:131],v[80:83],v[32:35],0\n" MF"a[132:135],v[80:83],v[36:39],0\n" SA"m0,s42,131072\n" SA"s50,s40,256\n"
 BL"v2,s[20:23],s50 offen lds\n" MF"a[136:139],v[80:83],v[40:43],0\n" TR"v[140:141],v13 offset:16384\n" WL"(8)\n" TR"v[142:143],v13 offset:17408\n" MF"a[140:143],v[80:83],v[44:47],0\n"
 MF"a[144:147],v[80:83],v[48:51],0\n" MF"a[148:151],v[80:83],v[52:55],0\n" MF"a[152:155],v[80:83],v[56:59],0\n" MF"a[156:159],v[80:83],v[60:63],0\n" TR"v[144:145],v14 offset:16384\n"
 TR"v[146:147],v14 offset:17408\n" MF"a[160:163],v[84:87],v[32:35],0\n" SA"m0,s42,132096\n" SA"s50,s40,8448\n" BL"v3,s[20:23],s50 offen lds\n" MF"a[164:167],v[84:87],v[36:39],0\n"
 MF"a[168:171],v[84:87],v[40:43],0\n" MF"a[172:175],v[84:87],v[44:47],0\n" MF"a[176:179],v[84:87],v[48:51],0\n" TR"v[148:149],v15 offset:16384\n" TR"v[150:151],v15 offset:17408\n"
 MF"a[180:183],v[84:87],v[52:55],0\n" MF"a[184:187],v[84:87],v[56:59],0\n" MF"a[188:191],v[84:87],v[60:63],0\n" SA"m0,s42,133120\n" SA"s50,s40,16640\n" BL"v2,s[20:23],s50 offen lds\n"
 MF"a[192:195],v[88:91],v[32:35],0\n" MF"a[196:199],v[88:91],v[36:39],0\n" TR"v[152:153],v16 offset:16384\n" TR"v[154:155],v16 offset:17408\n" MF"a[200:203],v[88:91],v[40:43],0\n"
 MF"a[204:207],v[88:91],v[44:47],0\n" MF"a[208:211],v[88:91],v[48:51],0\n" MF"a[212:215],v[88:91],v[52:55],0\n" MF"a[216:219],v[88:91],v[56:59],0\n" WL"(8)\n" TR"v[156:157],v17 offset:16384\n"
 TR"v[158:159],v17 offset:17408\n" SA"m0,s42,134144\n" SA"s50,s40,24832\n" BL"v3,s[20:23],s50 offen lds\n" MF"a[220:223],v[88:91],v[60:63],0\n" MF"a[224:227],v[92:95],v[32:35],0\n"
 MF"a[228:231],v[92:95],v[36:39],0\n" MF"a[232:235],v[92:95],v[40:43],0\n" MF"a[236:239],v[92:95],v[44:47],0\n" MF"a[240:243],v[92:95],v[48:51],0\n" MF"a[244:247],v[92:95],v[52:55],0\n"
 MF"a[248:251],v[92:95],v[56:59],0\n" MF"a[252:255],v[92:95],v[60:63],0\n" MF"a[0:3],v[128:131],v[96:99],a[0:3]\n" WV"(18) lgkmcnt(0)\n""s_barrier\n" MF"a[4:7],v[128:131],v[100:103],a[4:7]\n"
 MF"a[8:11],v[128:131],v[104:107],a[8:11]\n" DR"v[32:35],v6 offset:32768\n" MF"a[12:15],v[128:131],v[108:111],a[12:15]\n" MF"a[16:19],v[128:131],v[112:115],a[16:19]\n" DR"v[36:39],v6 offset:34816\n"
 MF"a[20:23],v[128:131],v[116:119],a[20:23]\n" SA"m0,s43,0\n" SA"s50,s41,1048576\n" BL"v4,s[16:19],s50 offen lds\n" MF"a[24:27],v[128:131],v[120:123],a[24:27]\n" DR"v[40:43],v6 offset:36864\n"
 MF"a[28:31],v[128:131],v[124:127],a[28:31]\n" MF"a[32:35],v[132:135],v[96:99],a[32:35]\n" DR"v[44:47],v6 offset:38912\n" MF"a[36:39],v[132:135],v[100:103],a[36:39]\n"
 MF"a[40:43],v[132:135],v[104:107],a[40:43]\n" DR"v[48:51],v6 offset:40960\n" MF"a[44:47],v[132:135],v[108:111],a[44:47]\n" MF"a[48:51],v[132:135],v[112:115],a[48:51]\n" DR"v[52:55],v6 offset:43008\n"
 SA"m0,s43,1024\n" SA"s50,s41,1081344\n" BL"v4,s[16:19],s50 offen lds\n" MF"a[52:55],v[132:135],v[116:119],a[52:55]\n" MF"a[56:59],v[132:135],v[120:123],a[56:59]\n" DR"v[56:59],v6 offset:45056\n"
 MF"a[60:63],v[132:135],v[124:127],a[60:63]\n" MF"a[64:67],v[136:139],v[96:99],a[64:67]\n" DR"v[60:63],v6 offset:47104\n" MF"a[68:71],v[136:139],v[100:103],a[68:71]\n"
 MF"a[72:75],v[136:139],v[104:107],a[72:75]\n" MF"a[76:79],v[136:139],v[108:111],a[76:79]\n" TR"v[64:65],v10 offset:32768\n" TR"v[66:67],v10 offset:33792\n" SA"m0,s43,2048\n" SA"s50,s41,1114112\n"
 BL"v5,s[16:19],s50 offen lds\n" MF"a[80:83],v[136:139],v[112:115],a[80:83]\n" MF"a[84:87],v[136:139],v[116:119],a[84:87]\n" MF"a[88:91],v[136:139],v[120:123],a[88:91]\n"
 MF"a[92:95],v[136:139],v[124:127],a[92:95]\n" MF"a[96:99],v[140:143],v[96:99],a[96:99]\n" TR"v[68:69],v11 offset:32768\n" TR"v[70:71],v11 offset:33792\n"
 MF"a[100:103],v[140:143],v[100:103],a[100:103]\n" MF"a[104:107],v[140:143],v[104:107],a[104:107]\n" SA"m0,s43,3072\n" SA"s50,s41,1146880\n" BL"v5,s[16:19],s50 offen lds\n"
 MF"a[108:111],v[140:143],v[108:111],a[108:111]\n" MF"a[112:115],v[140:143],v[112:115],a[112:115]\n" MF"a[116:119],v[140:143],v[116:119],a[116:119]\n" TR"v[72:73],v12 offset:32768\n"
 TR"v[74:75],v12 offset:33792\n" MF"a[120:123],v[140:143],v[120:123],a[120:123]\n" MF"a[124:127],v[140:143],v[124:127],a[124:127]\n" MF"a[128:131],v[144:147],v[96:99],a[128:131]\n"
 MF"a[132:135],v[144:147],v[100:103],a[132:135]\n" SA"m0,s42,135168\n" SA"s50,s40,33024\n" BL"v2,s[20:23],s50 offen lds\n" MF"a[136:139],v[144:147],v[104:107],a[136:139]\n"
 TR"v[76:77],v13 offset:32768\n" WL"(8)\n" TR"v[78:79],v13 offset:33792\n" MF"a[140:143],v[144:147],v[108:111],a[140:143]\n" MF"a[144:147],v[144:147],v[112:115],a[144:147]\n"
 MF"a[148:151],v[144:147],v[116:119],a[148:151]\n" MF"a[152:155],v[144:147],v[120:123],a[152:155]\n" MF"a[156:159],v[144:147],v[124:127],a[156:159]\n" TR"v[80:81],v14 offset:32768\n"
 TR"v[82:83],v14 offset:33792\n" MF"a[160:163],v[148:151],v[96:99],a[160:163]\n" SA"m0,s42,136192\n" SA"s50,s40,41216\n" BL"v3,s[20:23],s50 offen lds\n"
 MF"a[164:167],v[148:151],v[100:103],a[164:167]\n" MF"a[168:171],v[148:151],v[104:107],a[168:171]\n" MF"a[172:175],v[148:151],v[108:111],a[172:175]\n" MF"a[176:179],v[148:151],v[112:115],a[176:179]\n"
 TR"v[84:85],v15 offset:32768\n" TR"v[86:87],v15 offset:33792\n" MF"a[180:183],v[148:151],v[116:119],a[180:183]\n" MF"a[184:187],v[148:151],v[120:123],a[184:187]\n"
 MF"a[188:191],v[148:151],v[124:127],a[188:191]\n" SA"m0,s42,137216\n" SA"s50,s40,49408\n" BL"v2,s[20:23],s50 offen lds\n" MF"a[192:195],v[152:155],v[96:99],a[192:195]\n"
 MF"a[196:199],v[152:155],v[100:103],a[196:199]\n" TR"v[88:89],v16 offset:32768\n" TR"v[90:91],v16 offset:33792\n" MF"a[200:203],v[152:155],v[104:107],a[200:203]\n"
 MF"a[204:207],v[152:155],v[108:111],a[204:207]\n" MF"a[208:211],v[152:155],v[112:115],a[208:211]\n" MF"a[212:215],v[152:155],v[116:119],a[212:215]\n" MF"a[216:219],v[152:155],v[120:123],a[216:219]\n"
 WL"(8)\n" TR"v[92:93],v17 offset:32768\n" TR"v[94:95],v17 offset:33792\n" SA"m0,s42,138240\n" SA"s50,s40,57600\n" BL"v3,s[20:23],s50 offen lds\n" MF"a[220:223],v[152:155],v[124:127],a[220:223]\n"
 MF"a[224:227],v[156:159],v[96:99],a[224:227]\n" MF"a[228:231],v[156:159],v[100:103],a[228:231]\n" MF"a[232:235],v[156:159],v[104:107],a[232:235]\n" MF"a[236:239],v[156:159],v[108:111],a[236:239]\n"
 MF"a[240:243],v[156:159],v[112:115],a[240:243]\n" MF"a[244:247],v[156:159],v[116:119],a[244:247]\n" MF"a[248:251],v[156:159],v[120:123],a[248:251]\n" MF"a[252:255],v[156:159],v[124:127],a[252:255]\n"
 MF"a[0:3],v[64:67],v[32:35],a[0:3]\n" WV"(12) lgkmcnt(0)\n""s_barrier\n" MF"a[4:7],v[64:67],v[36:39],a[4:7]\n" MF"a[8:11],v[64:67],v[40:43],a[8:11]\n" DR"v[96:99],v7 offset:32768\n"
 MF"a[12:15],v[64:67],v[44:47],a[12:15]\n" MF"a[16:19],v[64:67],v[48:51],a[16:19]\n" DR"v[100:103],v7 offset:34816\n" MF"a[20:23],v[64:67],v[52:55],a[20:23]\n" SA"m0,s43,16384\n" SA"s50,s41,1310720\n"
 BL"v4,s[16:19],s50 offen lds\n" MF"a[24:27],v[64:67],v[56:59],a[24:27]\n" DR"v[104:107],v7 offset:36864\n" MF"a[28:31],v[64:67],v[60:63],a[28:31]\n" MF"a[32:35],v[68:71],v[32:35],a[32:35]\n"
 DR"v[108:111],v7 offset:38912\n" MF"a[36:39],v[68:71],v[36:39],a[36:39]\n" MF"a[40:43],v[68:71],v[40:43],a[40:43]\n" DR"v[112:115],v7 offset:40960\n" MF"a[44:47],v[68:71],v[44:47],a[44:47]\n"
 MF"a[48:51],v[68:71],v[48:51],a[48:51]\n" DR"v[116:119],v7 offset:43008\n" SA"m0,s43,17408\n" SA"s50,s41,1343488\n" BL"v4,s[16:19],s50 offen lds\n" MF"a[52:55],v[68:71],v[52:55],a[52:55]\n"
 MF"a[56:59],v[68:71],v[56:59],a[56:59]\n" DR"v[120:123],v7 offset:45056\n" MF"a[60:63],v[68:71],v[60:63],a[60:63]\n" MF"a[64:67],v[72:75],v[32:35],a[64:67]\n" DR"v[124:127],v7 offset:47104\n"
 MF"a[68:71],v[72:75],v[36:39],a[68:71]\n" MF"a[72:75],v[72:75],v[40:43],a[72:75]\n" MF"a[76:79],v[72:75],v[44:47],a[76:79]\n" TR"v[128:129],v10 offset:49152\n" TR"v[130:131],v10 offset:50176\n"
 SA"m0,s43,18432\n" SA"s50,s41,1376256\n" BL"v5,s[16:19],s50 offen lds\n" MF"a[80:83],v[72:75],v[48:51],a[80:83]\n" MF"a[84:87],v[72:75],v[52:55],a[84:87]\n" MF"a[88:91],v[72:75],v[56:59],a[88:91]\n"
 MF"a[92:95],v[72:75],v[60:63],a[92:95]\n" MF"a[96:99],v[76:79],v[32:35],a[96:99]\n" TR"v[132:133],v11 offset:49152\n" TR"v[134:135],v11 offset:50176\n" MF"a[100:103],v[76:79],v[36:39],a[100:103]\n"
 MF"a[104:107],v[76:79],v[40:43],a[104:107]\n" SA"m0,s43,19456\n" SA"s50,s41,1409024\n" BL"v5,s[16:19],s50 offen lds\n" MF"a[108:111],v[76:79],v[44:47],a[108:111]\n"
 MF"a[112:115],v[76:79],v[48:51],a[112:115]\n" MF"a[116:119],v[76:79],v[52:55],a[116:119]\n" TR"v[136:137],v12 offset:49152\n" TR"v[138:139],v12 offset:50176\n"
 MF"a[120:123],v[76:79],v[56:59],a[120:123]\n" MF"a[124:127],v[76:79],v[60:63],a[124:127]\n" MF"a[128:131],v[80:83],v[32:35],a[128:131]\n" MF"a[132:135],v[80:83],v[36:39],a[132:135]\n"
 SA"m0,s42,65536\n" SA"s50,s40,384\n" BL"v2,s[20:23],s50 offen lds\n" MF"a[136:139],v[80:83],v[40:43],a[136:139]\n" TR"v[140:141],v13 offset:49152\n" WL"(8)\n" TR"v[142:143],v13 offset:50176\n"
 MF"a[140:143],v[80:83],v[44:47],a[140:143]\n" MF"a[144:147],v[80:83],v[48:51],a[144:147]\n" MF"a[148:151],v[80:83],v[52:55],a[148:151]\n" MF"a[152:155],v[80:83],v[56:59],a[152:155]\n"
 MF"a[156:159],v[80:83],v[60:63],a[156:159]\n" TR"v[144:145],v14 offset:49152\n" TR"v[146:147],v14 offset:50176\n" MF"a[160:163],v[84:87],v[32:35],a[160:163]\n" SA"m0,s42,66560\n" SA"s50,s40,8576\n"
 BL"v3,s[20:23],s50 offen lds\n" MF"a[164:167],v[84:87],v[36:39],a[164:167]\n" MF"a[168:171],v[84:87],v[40:43],a[168:171]\n" MF"a[172:175],v[84:87],v[44:47],a[172:175]\n"
 MF"a[176:179],v[84:87],v[48:51],a[176:179]\n" TR"v[148:149],v15 offset:49152\n" TR"v[150:151],v15 offset:50176\n" MF"a[180:183],v[84:87],v[52:55],a[180:183]\n"
 MF"a[184:187],v[84:87],v[56:59],a[184:187]\n" MF"a[188:191],v[84:87],v[60:63],a[188:191]\n" SA"m0,s42,67584\n" SA"s50,s40,16768\n" BL"v2,s[20:23],s50 offen lds\n"
 MF"a[192:195],v[88:91],v[32:35],a[192:195]\n" MF"a[196:199],v[88:91],v[36:39],a[196:199]\n" TR"v[152:153],v16 offset:49152\n" TR"v[154:155],v16 offset:50176\n"
 MF"a[200:203],v[88:91],v[40:43],a[200:203]\n" MF"a[204:207],v[88:91],v[44:47],a[204:207]\n" MF"a[208:211],v[88:91],v[48:51],a[208:211]\n" MF"a[212:215],v[88:91],v[52:55],a[212:215]\n"
 MF"a[216:219],v[88:91],v[56:59],a[216:219]\n" WL"(8)\n" TR"v[156:157],v17 offset:49152\n" TR"v[158:159],v17 offset:50176\n" SA"m0,s42,68608\n" SA"s50,s40,24960\n" BL"v3,s[20:23],s50 offen lds\n"
 MF"a[220:223],v[88:91],v[60:63],a[220:223]\n" MF"a[224:227],v[92:95],v[32:35],a[224:227]\n" MF"a[228:231],v[92:95],v[36:39],a[228:231]\n" MF"a[232:235],v[92:95],v[40:43],a[232:235]\n"
 MF"a[236:239],v[92:95],v[44:47],a[236:239]\n" MF"a[240:243],v[92:95],v[48:51],a[240:243]\n" MF"a[244:247],v[92:95],v[52:55],a[244:247]\n" MF"a[248:251],v[92:95],v[56:59],a[248:251]\n"
 MF"a[252:255],v[92:95],v[60:63],a[252:255]\n" MF"a[0:3],v[128:131],v[96:99],a[0:3]\n" WV"(8) lgkmcnt(0)\n""s_barrier\n""s_lshl_b32 s57,s96,3\n" SA"s57,s57,4096\n" VV"v20,s98\n" VV"v21,s99\n"
 VV"v22,0\n""buffer_store_dwordx2 v[20:21],v22,s[92:95],s57 offen sc1\n" MF"a[4:7],v[128:131],v[100:103],a[4:7]\n" MF"a[8:11],v[128:131],v[104:107],a[8:11]\n" DR"v[32:35],v8 offset:0\n"
 MF"a[12:15],v[128:131],v[108:111],a[12:15]\n" MF"a[16:19],v[128:131],v[112:115],a[16:19]\n" DR"v[36:39],v8 offset:2048\n" MF"a[20:23],v[128:131],v[116:119],a[20:23]\n" SA"m0,s43,32768\n"
 SA"s50,s41,1572864\n" BL"v4,s[16:19],s50 offen lds\n" MF"a[24:27],v[128:131],v[120:123],a[24:27]\n" DR"v[40:43],v8 offset:4096\n" MF"a[28:31],v[128:131],v[124:127],a[28:31]\n"
 MF"a[32:35],v[132:135],v[96:99],a[32:35]\n" DR"v[44:47],v8 offset:6144\n" MF"a[36:39],v[132:135],v[100:103],a[36:39]\n" MF"a[40:43],v[132:135],v[104:107],a[40:43]\n" DR"v[48:51],v8 offset:8192\n"
 MF"a[44:47],v[132:135],v[108:111],a[44:47]\n" MF"a[48:51],v[132:135],v[112:115],a[48:51]\n" DR"v[52:55],v8 offset:10240\n" SA"m0,s43,33792\n" SA"s50,s41,1605632\n" BL"v4,s[16:19],s50 offen lds\n"
 MF"a[52:55],v[132:135],v[116:119],a[52:55]\n" MF"a[56:59],v[132:135],v[120:123],a[56:59]\n" DR"v[56:59],v8 offset:12288\n" MF"a[60:63],v[132:135],v[124:127],a[60:63]\n"
 MF"a[64:67],v[136:139],v[96:99],a[64:67]\n" DR"v[60:63],v8 offset:14336\n" MF"a[68:71],v[136:139],v[100:103],a[68:71]\n" MF"a[72:75],v[136:139],v[104:107],a[72:75]\n"
 MF"a[76:79],v[136:139],v[108:111],a[76:79]\n" TR"v[64:65],v10 offset:0\n" TR"v[66:67],v10 offset:1024\n" SA"m0,s43,34816\n" SA"s50,s41,1638400\n" BL"v5,s[16:19],s50 offen lds\n"
 MF"a[80:83],v[136:139],v[112:115],a[80:83]\n" MF"a[84:87],v[136:139],v[116:119],a[84:87]\n" MF"a[88:91],v[136:139],v[120:123],a[88:91]\n" MF"a[92:95],v[136:139],v[124:127],a[92:95]\n"
 MF"a[96:99],v[140:143],v[96:99],a[96:99]\n" TR"v[68:69],v11 offset:0\n" TR"v[70:71],v11 offset:1024\n" MF"a[100:103],v[140:143],v[100:103],a[100:103]\n"
 MF"a[104:107],v[140:143],v[104:107],a[104:107]\n" SA"m0,s43,35840\n" SA"s50,s41,1671168\n" BL"v5,s[16:19],s50 offen lds\n" MF"a[108:111],v[140:143],v[108:111],a[108:111]\n"
 MF"a[112:115],v[140:143],v[112:115],a[112:115]\n" MF"a[116:119],v[140:143],v[116:119],a[116:119]\n" TR"v[72:73],v12 offset:0\n" TR"v[74:75],v12 offset:1024\n"
 MF"a[120:123],v[140:143],v[120:123],a[120:123]\n" MF"a[124:127],v[140:143],v[124:127],a[124:127]\n" MF"a[128:131],v[144:147],v[96:99],a[128:131]\n" MF"a[132:135],v[144:147],v[100:103],a[132:135]\n"
 SA"m0,s42,69632\n" SA"s50,s40,33152\n" BL"v2,s[20:23],s50 offen lds\n" MF"a[136:139],v[144:147],v[104:107],a[136:139]\n" TR"v[76:77],v13 offset:0\n" WL"(8)\n" TR"v[78:79],v13 offset:1024\n"
 MF"a[140:143],v[144:147],v[108:111],a[140:143]\n" MF"a[144:147],v[144:147],v[112:115],a[144:147]\n" MF"a[148:151],v[144:147],v[116:119],a[148:151]\n" MF"a[152:155],v[144:147],v[120:123],a[152:155]\n"
 MF"a[156:159],v[144:147],v[124:127],a[156:159]\n" TR"v[80:81],v14 offset:0\n" TR"v[82:83],v14 offset:1024\n" MF"a[160:163],v[148:151],v[96:99],a[160:163]\n" SA"m0,s42,70656\n" SA"s50,s40,41344\n"
 BL"v3,s[20:23],s50 offen lds\n" MF"a[164:167],v[148:151],v[100:103],a[164:167]\n" MF"a[168:171],v[148:151],v[104:107],a[168:171]\n" MF"a[172:175],v[148:151],v[108:111],a[172:175]\n"
 MF"a[176:179],v[148:151],v[112:115],a[176:179]\n" TR"v[84:85],v15 offset:0\n" TR"v[86:87],v15 offset:1024\n" MF"a[180:183],v[148:151],v[116:119],a[180:183]\n"
 MF"a[184:187],v[148:151],v[120:123],a[184:187]\n" MF"a[188:191],v[148:151],v[124:127],a[188:191]\n" SA"m0,s42,71680\n" SA"s50,s40,49536\n" BL"v2,s[20:23],s50 offen lds\n"
 MF"a[192:195],v[152:155],v[96:99],a[192:195]\n" MF"a[196:199],v[152:155],v[100:103],a[196:199]\n" TR"v[88:89],v16 offset:0\n" TR"v[90:91],v16 offset:1024\n"
 MF"a[200:203],v[152:155],v[104:107],a[200:203]\n" MF"a[204:207],v[152:155],v[108:111],a[204:207]\n" MF"a[208:211],v[152:155],v[112:115],a[208:211]\n" MF"a[212:215],v[152:155],v[116:119],a[212:215]\n"
 MF"a[216:219],v[152:155],v[120:123],a[216:219]\n" WL"(8)\n" TR"v[92:93],v17 offset:0\n" TR"v[94:95],v17 offset:1024\n" SA"m0,s42,72704\n" SA"s50,s40,57728\n" BL"v3,s[20:23],s50 offen lds\n"
 MF"a[220:223],v[152:155],v[124:127],a[220:223]\n" MF"a[224:227],v[156:159],v[96:99],a[224:227]\n" MF"a[228:231],v[156:159],v[100:103],a[228:231]\n" MF"a[232:235],v[156:159],v[104:107],a[232:235]\n"
 MF"a[236:239],v[156:159],v[108:111],a[236:239]\n" MF"a[240:243],v[156:159],v[112:115],a[240:243]\n" MF"a[244:247],v[156:159],v[116:119],a[244:247]\n" MF"a[248:251],v[156:159],v[120:123],a[248:251]\n"
 MF"a[252:255],v[156:159],v[124:127],a[252:255]\n" MF"a[0:3],v[64:67],v[32:35],a[0:3]\n" WV"(13) lgkmcnt(0)\n""s_barrier\n" MF"a[4:7],v[64:67],v[36:39],a[4:7]\n"
 MF"a[8:11],v[64:67],v[40:43],a[8:11]\n" DR"v[96:99],v9 offset:0\n" MF"a[12:15],v[64:67],v[44:47],a[12:15]\n" MF"a[16:19],v[64:67],v[48:51],a[16:19]\n" DR"v[100:103],v9 offset:2048\n"
 MF"a[20:23],v[64:67],v[52:55],a[20:23]\n" SA"m0,s43,49152\n" SA"s50,s41,1835008\n" BL"v4,s[16:19],s50 offen lds\n" MF"a[24:27],v[64:67],v[56:59],a[24:27]\n" DR"v[104:107],v9 offset:4096\n"
 MF"a[28:31],v[64:67],v[60:63],a[28:31]\n" MF"a[32:35],v[68:71],v[32:35],a[32:35]\n" DR"v[108:111],v9 offset:6144\n" MF"a[36:39],v[68:71],v[36:39],a[36:39]\n" MF"a[40:43],v[68:71],v[40:43],a[40:43]\n"
 DR"v[112:115],v9 offset:8192\n" MF"a[44:47],v[68:71],v[44:47],a[44:47]\n" MF"a[48:51],v[68:71],v[48:51],a[48:51]\n" DR"v[116:119],v9 offset:10240\n" SA"m0,s43,50176\n" SA"s50,s41,1867776\n"
 BL"v4,s[16:19],s50 offen lds\n" MF"a[52:55],v[68:71],v[52:55],a[52:55]\n" MF"a[56:59],v[68:71],v[56:59],a[56:59]\n" DR"v[120:123],v9 offset:12288\n" MF"a[60:63],v[68:71],v[60:63],a[60:63]\n"
 MF"a[64:67],v[72:75],v[32:35],a[64:67]\n" DR"v[124:127],v9 offset:14336\n" MF"a[68:71],v[72:75],v[36:39],a[68:71]\n" MF"a[72:75],v[72:75],v[40:43],a[72:75]\n"
 MF"a[76:79],v[72:75],v[44:47],a[76:79]\n" TR"v[128:129],v10 offset:16384\n" TR"v[130:131],v10 offset:17408\n" SA"m0,s43,51200\n" SA"s50,s41,1900544\n" BL"v5,s[16:19],s50 offen lds\n"
 MF"a[80:83],v[72:75],v[48:51],a[80:83]\n" MF"a[84:87],v[72:75],v[52:55],a[84:87]\n" MF"a[88:91],v[72:75],v[56:59],a[88:91]\n" MF"a[92:95],v[72:75],v[60:63],a[92:95]\n"
 MF"a[96:99],v[76:79],v[32:35],a[96:99]\n" TR"v[132:133],v11 offset:16384\n" TR"v[134:135],v11 offset:17408\n" MF"a[100:103],v[76:79],v[36:39],a[100:103]\n"
 MF"a[104:107],v[76:79],v[40:43],a[104:107]\n" SA"m0,s43,52224\n" SA"s50,s41,1933312\n" BL"v5,s[16:19],s50 offen lds\n" MF"a[108:111],v[76:79],v[44:47],a[108:111]\n"
 MF"a[112:115],v[76:79],v[48:51],a[112:115]\n" MF"a[116:119],v[76:79],v[52:55],a[116:119]\n" TR"v[136:137],v12 offset:16384\n" TR"v[138:139],v12 offset:17408\n"
 MF"a[120:123],v[76:79],v[56:59],a[120:123]\n" MF"a[124:127],v[76:79],v[60:63],a[124:127]\n" MF"a[128:131],v[80:83],v[32:35],a[128:131]\n" MF"a[132:135],v[80:83],v[36:39],a[132:135]\n"
 SA"m0,s42,98304\n" SA"s50,s40,512\n" BL"v2,s[20:23],s50 offen lds\n" MF"a[136:139],v[80:83],v[40:43],a[136:139]\n" TR"v[140:141],v13 offset:16384\n" WL"(8)\n" TR"v[142:143],v13 offset:17408\n"
 MF"a[140:143],v[80:83],v[44:47],a[140:143]\n" MF"a[144:147],v[80:83],v[48:51],a[144:147]\n" MF"a[148:151],v[80:83],v[52:55],a[148:151]\n" MF"a[152:155],v[80:83],v[56:59],a[152:155]\n"
 MF"a[156:159],v[80:83],v[60:63],a[156:159]\n" TR"v[144:145],v14 offset:16384\n" TR"v[146:147],v14 offset:17408\n" MF"a[160:163],v[84:87],v[32:35],a[160:163]\n" SA"m0,s42,99328\n" SA"s50,s40,8704\n"
 BL"v3,s[20:23],s50 offen lds\n" MF"a[164:167],v[84:87],v[36:39],a[164:167]\n" MF"a[168:171],v[84:87],v[40:43],a[168:171]\n" MF"a[172:175],v[84:87],v[44:47],a[172:175]\n"
 MF"a[176:179],v[84:87],v[48:51],a[176:179]\n" TR"v[148:149],v15 offset:16384\n" TR"v[150:151],v15 offset:17408\n" MF"a[180:183],v[84:87],v[52:55],a[180:183]\n"
 MF"a[184:187],v[84:87],v[56:59],a[184:187]\n" MF"a[188:191],v[84:87],v[60:63],a[188:191]\n" SA"m0,s42,100352\n" SA"s50,s40,16896\n" BL"v2,s[20:23],s50 offen lds\n"
 MF"a[192:195],v[88:91],v[32:35],a[192:195]\n" MF"a[196:199],v[88:91],v[36:39],a[196:199]\n" TR"v[152:153],v16 offset:16384\n" TR"v[154:155],v16 offset:17408\n"
 MF"a[200:203],v[88:91],v[40:43],a[200:203]\n" MF"a[204:207],v[88:91],v[44:47],a[204:207]\n" MF"a[208:211],v[88:91],v[48:51],a[208:211]\n" MF"a[212:215],v[88:91],v[52:55],a[212:215]\n"
 MF"a[216:219],v[88:91],v[56:59],a[216:219]\n" WL"(8)\n" TR"v[156:157],v17 offset:16384\n" TR"v[158:159],v17 offset:17408\n" SA"m0,s42,101376\n" SA"s50,s40,25088\n" BL"v3,s[20:23],s50 offen lds\n"
 MF"a[220:223],v[88:91],v[60:63],a[220:223]\n" MF"a[224:227],v[92:95],v[32:35],a[224:227]\n" MF"a[228:231],v[92:95],v[36:39],a[228:231]\n" MF"a[232:235],v[92:95],v[40:43],a[232:235]\n"
 MF"a[236:239],v[92:95],v[44:47],a[236:239]\n" MF"a[240:243],v[92:95],v[48:51],a[240:243]\n" MF"a[244:247],v[92:95],v[52:55],a[244:247]\n" MF"a[248:251],v[92:95],v[56:59],a[248:251]\n"
 MF"a[252:255],v[92:95],v[60:63],a[252:255]\n" MF"a[0:3],v[128:131],v[96:99],a[0:3]\n" WV"(8) lgkmcnt(0)\n""s_barrier\n""v_readlane_b32 s72,v254,4\n""v_readlane_b32 s73,v254,5\n"
"s_mov_b32 s74,4194304\n""s_mov_b32 s75,0x20000\n""v_mbcnt_lo_u32_b32 v28,-1,0\n""v_mbcnt_hi_u32_b32 v28,-1,v28\n""v_lshlrev_b32 v28,13,v28\n""s_lshr_b32 s57,s96,5\n""s_lshl_b32 s57,s57,19\n"
"s_and_b32 s58,s96,31\n""s_lshl_b32 s58,s58,8\n" SA"s57,s57,s58\n""s_and_b32 s58,s36,1\n""s_lshl_b32 s58,s58,7\n" SA"s57,s57,s58\n""buffer_load_dword v29,v28,s[72:75],s57 offen\n"
 MF"a[4:7],v[128:131],v[100:103],a[4:7]\n" MF"a[8:11],v[128:131],v[104:107],a[8:11]\n" DR"v[32:35],v6 offset:0\n" MF"a[12:15],v[128:131],v[108:111],a[12:15]\n"
 MF"a[16:19],v[128:131],v[112:115],a[16:19]\n" DR"v[36:39],v6 offset:2048\n" MF"a[20:23],v[128:131],v[116:119],a[20:23]\n" SA"m0,s43,0\n" SA"s50,s41,2097152\n" BL"v4,s[16:19],s50 offen lds\n"
 MF"a[24:27],v[128:131],v[120:123],a[24:27]\n" DR"v[40:43],v6 offset:4096\n" MF"a[28:31],v[128:131],v[124:127],a[28:31]\n" MF"a[32:35],v[132:135],v[96:99],a[32:35]\n" DR"v[44:47],v6 offset:6144\n"
 MF"a[36:39],v[132:135],v[100:103],a[36:39]\n" MF"a[40:43],v[132:135],v[104:107],a[40:43]\n" DR"v[48:51],v6 offset:8192\n" MF"a[44:47],v[132:135],v[108:111],a[44:47]\n"
 MF"a[48:51],v[132:135],v[112:115],a[48:51]\n" DR"v[52:55],v6 offset:10240\n" SA"m0,s43,1024\n" SA"s50,s41,2129920\n" BL"v4,s[16:19],s50 offen lds\n" MF"a[52:55],v[132:135],v[116:119],a[52:55]\n"
 MF"a[56:59],v[132:135],v[120:123],a[56:59]\n" DR"v[56:59],v6 offset:12288\n" MF"a[60:63],v[132:135],v[124:127],a[60:63]\n" MF"a[64:67],v[136:139],v[96:99],a[64:67]\n" DR"v[60:63],v6 offset:14336\n"
 MF"a[68:71],v[136:139],v[100:103],a[68:71]\n" MF"a[72:75],v[136:139],v[104:107],a[72:75]\n" MF"a[76:79],v[136:139],v[108:111],a[76:79]\n" TR"v[64:65],v10 offset:32768\n"
 TR"v[66:67],v10 offset:33792\n" SA"m0,s43,2048\n" SA"s50,s41,2162688\n" BL"v5,s[16:19],s50 offen lds\n" MF"a[80:83],v[136:139],v[112:115],a[80:83]\n" MF"a[84:87],v[136:139],v[116:119],a[84:87]\n"
 MF"a[88:91],v[136:139],v[120:123],a[88:91]\n" MF"a[92:95],v[136:139],v[124:127],a[92:95]\n" MF"a[96:99],v[140:143],v[96:99],a[96:99]\n" TR"v[68:69],v11 offset:32768\n" TR"v[70:71],v11 offset:33792\n"
 MF"a[100:103],v[140:143],v[100:103],a[100:103]\n" MF"a[104:107],v[140:143],v[104:107],a[104:107]\n" SA"m0,s43,3072\n" SA"s50,s41,2195456\n" BL"v5,s[16:19],s50 offen lds\n"
 MF"a[108:111],v[140:143],v[108:111],a[108:111]\n" MF"a[112:115],v[140:143],v[112:115],a[112:115]\n" MF"a[116:119],v[140:143],v[116:119],a[116:119]\n" TR"v[72:73],v12 offset:32768\n"
 TR"v[74:75],v12 offset:33792\n" MF"a[120:123],v[140:143],v[120:123],a[120:123]\n" MF"a[124:127],v[140:143],v[124:127],a[124:127]\n" MF"a[128:131],v[144:147],v[96:99],a[128:131]\n"
 MF"a[132:135],v[144:147],v[100:103],a[132:135]\n" SA"m0,s42,102400\n" SA"s50,s40,33280\n" BL"v2,s[20:23],s50 offen lds\n" MF"a[136:139],v[144:147],v[104:107],a[136:139]\n"
 TR"v[76:77],v13 offset:32768\n" WL"(8)\n" TR"v[78:79],v13 offset:33792\n" MF"a[140:143],v[144:147],v[108:111],a[140:143]\n" MF"a[144:147],v[144:147],v[112:115],a[144:147]\n"
 MF"a[148:151],v[144:147],v[116:119],a[148:151]\n" MF"a[152:155],v[144:147],v[120:123],a[152:155]\n" MF"a[156:159],v[144:147],v[124:127],a[156:159]\n" TR"v[80:81],v14 offset:32768\n"
 TR"v[82:83],v14 offset:33792\n" MF"a[160:163],v[148:151],v[96:99],a[160:163]\n" SA"m0,s42,103424\n" SA"s50,s40,41472\n" BL"v3,s[20:23],s50 offen lds\n"
 MF"a[164:167],v[148:151],v[100:103],a[164:167]\n" MF"a[168:171],v[148:151],v[104:107],a[168:171]\n" MF"a[172:175],v[148:151],v[108:111],a[172:175]\n" MF"a[176:179],v[148:151],v[112:115],a[176:179]\n"
 TR"v[84:85],v15 offset:32768\n" TR"v[86:87],v15 offset:33792\n" MF"a[180:183],v[148:151],v[116:119],a[180:183]\n" MF"a[184:187],v[148:151],v[120:123],a[184:187]\n"
 MF"a[188:191],v[148:151],v[124:127],a[188:191]\n" SA"m0,s42,104448\n" SA"s50,s40,49664\n" BL"v2,s[20:23],s50 offen lds\n" MF"a[192:195],v[152:155],v[96:99],a[192:195]\n"
 MF"a[196:199],v[152:155],v[100:103],a[196:199]\n" TR"v[88:89],v16 offset:32768\n" TR"v[90:91],v16 offset:33792\n" MF"a[200:203],v[152:155],v[104:107],a[200:203]\n"
 MF"a[204:207],v[152:155],v[108:111],a[204:207]\n" MF"a[208:211],v[152:155],v[112:115],a[208:211]\n" MF"a[212:215],v[152:155],v[116:119],a[212:215]\n" MF"a[216:219],v[152:155],v[120:123],a[216:219]\n"
 WL"(8)\n" TR"v[92:93],v17 offset:32768\n" TR"v[94:95],v17 offset:33792\n" SA"m0,s42,105472\n" SA"s50,s40,57856\n" BL"v3,s[20:23],s50 offen lds\n" MF"a[220:223],v[152:155],v[124:127],a[220:223]\n"
 MF"a[224:227],v[156:159],v[96:99],a[224:227]\n" MF"a[228:231],v[156:159],v[100:103],a[228:231]\n" MF"a[232:235],v[156:159],v[104:107],a[232:235]\n" MF"a[236:239],v[156:159],v[108:111],a[236:239]\n"
 MF"a[240:243],v[156:159],v[112:115],a[240:243]\n" MF"a[244:247],v[156:159],v[116:119],a[244:247]\n" MF"a[248:251],v[156:159],v[120:123],a[248:251]\n" MF"a[252:255],v[156:159],v[124:127],a[252:255]\n"
 MF"a[0:3],v[64:67],v[32:35],a[0:3]\n" WV"(13) lgkmcnt(0)\n""s_barrier\n""s_bitcmp1_b32 s36,0\n""s_cselect_b32 s57,14,12\n""v_readlane_b32 s76,v254,s57\n" SA"s57,s57,1\n"
"v_readlane_b32 s77,v254,s57\n""s_mov_b32 s78,0x80000\n""s_mov_b32 s79,0x20000\n""s_lshl_b32 s57,s96,11\n""v_mbcnt_lo_u32_b32 v28,-1,0\n""v_mbcnt_hi_u32_b32 v28,-1,v28\n""v_min_u32 v28,15,v28\n"
"v_mul_u32_u24 v28,128,v28\n""buffer_load_dword v29,v28,s[76:79],s57 offen\n" MF"a[4:7],v[64:67],v[36:39],a[4:7]\n" MF"a[8:11],v[64:67],v[40:43],a[8:11]\n" DR"v[96:99],v7 offset:0\n"
 MF"a[12:15],v[64:67],v[44:47],a[12:15]\n" MF"a[16:19],v[64:67],v[48:51],a[16:19]\n" DR"v[100:103],v7 offset:2048\n" MF"a[20:23],v[64:67],v[52:55],a[20:23]\n" SA"m0,s43,16384\n" SA"s50,s41,2359296\n"
 BL"v4,s[16:19],s50 offen lds\n" MF"a[24:27],v[64:67],v[56:59],a[24:27]\n" DR"v[104:107],v7 offset:4096\n" MF"a[28:31],v[64:67],v[60:63],a[28:31]\n" MF"a[32:35],v[68:71],v[32:35],a[32:35]\n"
 DR"v[108:111],v7 offset:6144\n" MF"a[36:39],v[68:71],v[36:39],a[36:39]\n" MF"a[40:43],v[68:71],v[40:43],a[40:43]\n" DR"v[112:115],v7 offset:8192\n" MF"a[44:47],v[68:71],v[44:47],a[44:47]\n"
 MF"a[48:51],v[68:71],v[48:51],a[48:51]\n" DR"v[116:119],v7 offset:10240\n" SA"m0,s43,17408\n" SA"s50,s41,2392064\n" BL"v4,s[16:19],s50 offen lds\n" MF"a[52:55],v[68:71],v[52:55],a[52:55]\n"
 MF"a[56:59],v[68:71],v[56:59],a[56:59]\n" DR"v[120:123],v7 offset:12288\n" MF"a[60:63],v[68:71],v[60:63],a[60:63]\n" MF"a[64:67],v[72:75],v[32:35],a[64:67]\n" DR"v[124:127],v7 offset:14336\n"
 MF"a[68:71],v[72:75],v[36:39],a[68:71]\n" MF"a[72:75],v[72:75],v[40:43],a[72:75]\n" MF"a[76:79],v[72:75],v[44:47],a[76:79]\n" TR"v[128:129],v10 offset:49152\n" TR"v[130:131],v10 offset:50176\n"
 SA"m0,s43,18432\n" SA"s50,s41,2424832\n" BL"v5,s[16:19],s50 offen lds\n" MF"a[80:83],v[72:75],v[48:51],a[80:83]\n" MF"a[84:87],v[72:75],v[52:55],a[84:87]\n" MF"a[88:91],v[72:75],v[56:59],a[88:91]\n"
 MF"a[92:95],v[72:75],v[60:63],a[92:95]\n" MF"a[96:99],v[76:79],v[32:35],a[96:99]\n" TR"v[132:133],v11 offset:49152\n" TR"v[134:135],v11 offset:50176\n" MF"a[100:103],v[76:79],v[36:39],a[100:103]\n"
 MF"a[104:107],v[76:79],v[40:43],a[104:107]\n" SA"m0,s43,19456\n" SA"s50,s41,2457600\n" BL"v5,s[16:19],s50 offen lds\n" MF"a[108:111],v[76:79],v[44:47],a[108:111]\n"
 MF"a[112:115],v[76:79],v[48:51],a[112:115]\n" MF"a[116:119],v[76:79],v[52:55],a[116:119]\n" TR"v[136:137],v12 offset:49152\n" TR"v[138:139],v12 offset:50176\n"
 MF"a[120:123],v[76:79],v[56:59],a[120:123]\n" MF"a[124:127],v[76:79],v[60:63],a[124:127]\n" MF"a[128:131],v[80:83],v[32:35],a[128:131]\n" MF"a[132:135],v[80:83],v[36:39],a[132:135]\n"
 SA"m0,s42,131072\n" SA"s50,s40,640\n" BL"v2,s[20:23],s50 offen lds\n" MF"a[136:139],v[80:83],v[40:43],a[136:139]\n" TR"v[140:141],v13 offset:49152\n" WL"(8)\n" TR"v[142:143],v13 offset:50176\n"
 MF"a[140:143],v[80:83],v[44:47],a[140:143]\n" MF"a[144:147],v[80:83],v[48:51],a[144:147]\n" MF"a[148:151],v[80:83],v[52:55],a[148:151]\n" MF"a[152:155],v[80:83],v[56:59],a[152:155]\n"
 MF"a[156:159],v[80:83],v[60:63],a[156:159]\n" TR"v[144:145],v14 offset:49152\n" TR"v[146:147],v14 offset:50176\n" MF"a[160:163],v[84:87],v[32:35],a[160:163]\n" SA"m0,s42,132096\n" SA"s50,s40,8832\n"
 BL"v3,s[20:23],s50 offen lds\n" MF"a[164:167],v[84:87],v[36:39],a[164:167]\n" MF"a[168:171],v[84:87],v[40:43],a[168:171]\n" MF"a[172:175],v[84:87],v[44:47],a[172:175]\n"
 MF"a[176:179],v[84:87],v[48:51],a[176:179]\n" TR"v[148:149],v15 offset:49152\n" TR"v[150:151],v15 offset:50176\n" MF"a[180:183],v[84:87],v[52:55],a[180:183]\n"
 MF"a[184:187],v[84:87],v[56:59],a[184:187]\n" MF"a[188:191],v[84:87],v[60:63],a[188:191]\n" SA"m0,s42,133120\n" SA"s50,s40,17024\n" BL"v2,s[20:23],s50 offen lds\n"
 MF"a[192:195],v[88:91],v[32:35],a[192:195]\n" MF"a[196:199],v[88:91],v[36:39],a[196:199]\n" TR"v[152:153],v16 offset:49152\n" TR"v[154:155],v16 offset:50176\n"
 MF"a[200:203],v[88:91],v[40:43],a[200:203]\n" MF"a[204:207],v[88:91],v[44:47],a[204:207]\n" MF"a[208:211],v[88:91],v[48:51],a[208:211]\n" MF"a[212:215],v[88:91],v[52:55],a[212:215]\n"
 MF"a[216:219],v[88:91],v[56:59],a[216:219]\n" WL"(8)\n" TR"v[156:157],v17 offset:49152\n" TR"v[158:159],v17 offset:50176\n" SA"m0,s42,134144\n" SA"s50,s40,25216\n" BL"v3,s[20:23],s50 offen lds\n"
 MF"a[220:223],v[88:91],v[60:63],a[220:223]\n" MF"a[224:227],v[92:95],v[32:35],a[224:227]\n" MF"a[228:231],v[92:95],v[36:39],a[228:231]\n" MF"a[232:235],v[92:95],v[40:43],a[232:235]\n"
 MF"a[236:239],v[92:95],v[44:47],a[236:239]\n" MF"a[240:243],v[92:95],v[48:51],a[240:243]\n" MF"a[244:247],v[92:95],v[52:55],a[244:247]\n" MF"a[248:251],v[92:95],v[56:59],a[248:251]\n"
 MF"a[252:255],v[92:95],v[60:63],a[252:255]\n" MF"a[0:3],v[128:131],v[96:99],a[0:3]\n" WV"(9) lgkmcnt(0)\n""s_barrier\n" MF"a[4:7],v[128:131],v[100:103],a[4:7]\n"
 MF"a[8:11],v[128:131],v[104:107],a[8:11]\n" DR"v[32:35],v6 offset:32768\n" MF"a[12:15],v[128:131],v[108:111],a[12:15]\n" MF"a[16:19],v[128:131],v[112:115],a[16:19]\n" DR"v[36:39],v6 offset:34816\n"
 MF"a[20:23],v[128:131],v[116:119],a[20:23]\n" SA"m0,s43,32768\n" SA"s50,s41,2621440\n" BL"v4,s[16:19],s50 offen lds\n" MF"a[24:27],v[128:131],v[120:123],a[24:27]\n" DR"v[40:43],v6 offset:36864\n"
 MF"a[28:31],v[128:131],v[124:127],a[28:31]\n" MF"a[32:35],v[132:135],v[96:99],a[32:35]\n" DR"v[44:47],v6 offset:38912\n" MF"a[36:39],v[132:135],v[100:103],a[36:39]\n"
 MF"a[40:43],v[132:135],v[104:107],a[40:43]\n" DR"v[48:51],v6 offset:40960\n" MF"a[44:47],v[132:135],v[108:111],a[44:47]\n" MF"a[48:51],v[132:135],v[112:115],a[48:51]\n" DR"v[52:55],v6 offset:43008\n"
 SA"m0,s43,33792\n" SA"s50,s41,2654208\n" BL"v4,s[16:19],s50 offen lds\n" MF"a[52:55],v[132:135],v[116:119],a[52:55]\n" MF"a[56:59],v[132:135],v[120:123],a[56:59]\n" DR"v[56:59],v6 offset:45056\n"
 MF"a[60:63],v[132:135],v[124:127],a[60:63]\n" MF"a[64:67],v[136:139],v[96:99],a[64:67]\n" DR"v[60:63],v6 offset:47104\n" MF"a[68:71],v[136:139],v[100:103],a[68:71]\n"
 MF"a[72:75],v[136:139],v[104:107],a[72:75]\n" MF"a[76:79],v[136:139],v[108:111],a[76:79]\n" TR"v[64:65],v10 offset:0\n" TR"v[66:67],v10 offset:1024\n" SA"m0,s43,34816\n" SA"s50,s41,2686976\n"
 BL"v5,s[16:19],s50 offen lds\n" MF"a[80:83],v[136:139],v[112:115],a[80:83]\n" MF"a[84:87],v[136:139],v[116:119],a[84:87]\n" MF"a[88:91],v[136:139],v[120:123],a[88:91]\n"
 MF"a[92:95],v[136:139],v[124:127],a[92:95]\n" MF"a[96:99],v[140:143],v[96:99],a[96:99]\n" TR"v[68:69],v11 offset:0\n" TR"v[70:71],v11 offset:1024\n" MF"a[100:103],v[140:143],v[100:103],a[100:103]\n"
 MF"a[104:107],v[140:143],v[104:107],a[104:107]\n" SA"m0,s43,35840\n" SA"s50,s41,2719744\n" BL"v5,s[16:19],s50 offen lds\n" MF"a[108:111],v[140:143],v[108:111],a[108:111]\n"
 MF"a[112:115],v[140:143],v[112:115],a[112:115]\n" MF"a[116:119],v[140:143],v[116:119],a[116:119]\n" TR"v[72:73],v12 offset:0\n" TR"v[74:75],v12 offset:1024\n"
 MF"a[120:123],v[140:143],v[120:123],a[120:123]\n" MF"a[124:127],v[140:143],v[124:127],a[124:127]\n" MF"a[128:131],v[144:147],v[96:99],a[128:131]\n" MF"a[132:135],v[144:147],v[100:103],a[132:135]\n"
 SA"m0,s42,135168\n" SA"s50,s40,33408\n" BL"v2,s[20:23],s50 offen lds\n" MF"a[136:139],v[144:147],v[104:107],a[136:139]\n" TR"v[76:77],v13 offset:0\n" WL"(8)\n" TR"v[78:79],v13 offset:1024\n"
 MF"a[140:143],v[144:147],v[108:111],a[140:143]\n" MF"a[144:147],v[144:147],v[112:115],a[144:147]\n" MF"a[148:151],v[144:147],v[116:119],a[148:151]\n" MF"a[152:155],v[144:147],v[120:123],a[152:155]\n"
 MF"a[156:159],v[144:147],v[124:127],a[156:159]\n" TR"v[80:81],v14 offset:0\n" TR"v[82:83],v14 offset:1024\n" MF"a[160:163],v[148:151],v[96:99],a[160:163]\n" SA"m0,s42,136192\n" SA"s50,s40,41600\n"
 BL"v3,s[20:23],s50 offen lds\n" MF"a[164:167],v[148:151],v[100:103],a[164:167]\n" MF"a[168:171],v[148:151],v[104:107],a[168:171]\n" MF"a[172:175],v[148:151],v[108:111],a[172:175]\n"
 MF"a[176:179],v[148:151],v[112:115],a[176:179]\n" TR"v[84:85],v15 offset:0\n" TR"v[86:87],v15 offset:1024\n" MF"a[180:183],v[148:151],v[116:119],a[180:183]\n"
 MF"a[184:187],v[148:151],v[120:123],a[184:187]\n" MF"a[188:191],v[148:151],v[124:127],a[188:191]\n" SA"m0,s42,137216\n" SA"s50,s40,49792\n" BL"v2,s[20:23],s50 offen lds\n"
 MF"a[192:195],v[152:155],v[96:99],a[192:195]\n" MF"a[196:199],v[152:155],v[100:103],a[196:199]\n" TR"v[88:89],v16 offset:0\n" TR"v[90:91],v16 offset:1024\n"
 MF"a[200:203],v[152:155],v[104:107],a[200:203]\n" MF"a[204:207],v[152:155],v[108:111],a[204:207]\n" MF"a[208:211],v[152:155],v[112:115],a[208:211]\n" MF"a[212:215],v[152:155],v[116:119],a[212:215]\n"
 MF"a[216:219],v[152:155],v[120:123],a[216:219]\n" WL"(8)\n" TR"v[92:93],v17 offset:0\n" TR"v[94:95],v17 offset:1024\n" SA"m0,s42,138240\n" SA"s50,s40,57984\n" BL"v3,s[20:23],s50 offen lds\n"
 MF"a[220:223],v[152:155],v[124:127],a[220:223]\n" MF"a[224:227],v[156:159],v[96:99],a[224:227]\n" MF"a[228:231],v[156:159],v[100:103],a[228:231]\n" MF"a[232:235],v[156:159],v[104:107],a[232:235]\n"
 MF"a[236:239],v[156:159],v[108:111],a[236:239]\n" MF"a[240:243],v[156:159],v[112:115],a[240:243]\n" MF"a[244:247],v[156:159],v[116:119],a[244:247]\n" MF"a[248:251],v[156:159],v[120:123],a[248:251]\n"
 MF"a[252:255],v[156:159],v[124:127],a[252:255]\n" MF"a[0:3],v[64:67],v[32:35],a[0:3]\n" WV"(12) lgkmcnt(0)\n""s_barrier\n""v_mbcnt_lo_u32_b32 v23,-1,0\n""v_mbcnt_hi_u32_b32 v23,-1,v23\n"
"v_min_u32 v23,39,v23\n""s_lshr_b32 s60,s96,5\n""s_and_b32 s61,s96,7\n""s_lshl_b32 s61,s61,2\n""s_bfe_u32 s57,s96,0x20003\n" SA"s61,s61,s57\n""s_lshl_b32 s62,s61,2\n" SA"s62,s62,s36\n"
"s_lshl_b32 s57,s61,6\n" SA"s57,s57,4096\n" VV"v20,s57\n""s_lshr_b32 s57,s96,5\n""s_lshl_b32 s57,s57,7\n" SA"s57,s57,4096\n""s_sub_u32 s57,s57,64\n" VV"v21,s57\n""v_cmp_le_u32 vcc,8,v23\n""s_nop 1\n"
"v_cndmask_b32 v20,v20,v21,vcc\n""s_lshr_b32 s57,s96,5\n""s_sub_u32 s57,15,s57\n""s_lshl_b32 s57,s57,7\n" SA"s57,s57,4096\n""s_sub_u32 s57,s57,192\n" VV"v21,s57\n""v_cmp_le_u32 vcc,24,v23\n"
"s_nop 1\n""v_cndmask_b32 v20,v20,v21,vcc\n""v_lshl_add_u32 v20,v23,3,v20\n""buffer_load_dwordx2 v[24:25],v20,s[92:95],0 offen sc1\n" MF"a[4:7],v[64:67],v[36:39],a[4:7]\n"
 MF"a[8:11],v[64:67],v[40:43],a[8:11]\n" DR"v[96:99],v7 offset:32768\n" MF"a[12:15],v[64:67],v[44:47],a[12:15]\n" MF"a[16:19],v[64:67],v[48:51],a[16:19]\n" DR"v[100:103],v7 offset:34816\n"
 MF"a[20:23],v[64:67],v[52:55],a[20:23]\n" SA"m0,s43,49152\n" SA"s50,s41,2883584\n" BL"v4,s[16:19],s50 offen lds\n" MF"a[24:27],v[64:67],v[56:59],a[24:27]\n" DR"v[104:107],v7 offset:36864\n"
 MF"a[28:31],v[64:67],v[60:63],a[28:31]\n" MF"a[32:35],v[68:71],v[32:35],a[32:35]\n" DR"v[108:111],v7 offset:38912\n" MF"a[36:39],v[68:71],v[36:39],a[36:39]\n"
 MF"a[40:43],v[68:71],v[40:43],a[40:43]\n" DR"v[112:115],v7 offset:40960\n" MF"a[44:47],v[68:71],v[44:47],a[44:47]\n" MF"a[48:51],v[68:71],v[48:51],a[48:51]\n" DR"v[116:119],v7 offset:43008\n"
 SA"m0,s43,50176\n" SA"s50,s41,2916352\n" BL"v4,s[16:19],s50 offen lds\n" MF"a[52:55],v[68:71],v[52:55],a[52:55]\n" MF"a[56:59],v[68:71],v[56:59],a[56:59]\n" DR"v[120:123],v7 offset:45056\n"
 MF"a[60:63],v[68:71],v[60:63],a[60:63]\n" MF"a[64:67],v[72:75],v[32:35],a[64:67]\n" DR"v[124:127],v7 offset:47104\n" MF"a[68:71],v[72:75],v[36:39],a[68:71]\n"
 MF"a[72:75],v[72:75],v[40:43],a[72:75]\n" MF"a[76:79],v[72:75],v[44:47],a[76:79]\n" TR"v[128:129],v10 offset:16384\n" TR"v[130:131],v10 offset:17408\n" SA"m0,s43,51200\n" SA"s50,s41,2949120\n"
 BL"v5,s[16:19],s50 offen lds\n" MF"a[80:83],v[72:75],v[48:51],a[80:83]\n" MF"a[84:87],v[72:75],v[52:55],a[84:87]\n" MF"a[88:91],v[72:75],v[56:59],a[88:91]\n" MF"a[92:95],v[72:75],v[60:63],a[92:95]\n"
 MF"a[96:99],v[76:79],v[32:35],a[96:99]\n" TR"v[132:133],v11 offset:16384\n" TR"v[134:135],v11 offset:17408\n" MF"a[100:103],v[76:79],v[36:39],a[100:103]\n"
 MF"a[104:107],v[76:79],v[40:43],a[104:107]\n" SA"m0,s43,52224\n" SA"s50,s41,2981888\n" BL"v5,s[16:19],s50 offen lds\n" MF"a[108:111],v[76:79],v[44:47],a[108:111]\n"
 MF"a[112:115],v[76:79],v[48:51],a[112:115]\n" MF"a[116:119],v[76:79],v[52:55],a[116:119]\n" TR"v[136:137],v12 offset:16384\n" TR"v[138:139],v12 offset:17408\n"
 MF"a[120:123],v[76:79],v[56:59],a[120:123]\n" MF"a[124:127],v[76:79],v[60:63],a[124:127]\n" MF"a[128:131],v[80:83],v[32:35],a[128:131]\n" MF"a[132:135],v[80:83],v[36:39],a[132:135]\n"
 SA"m0,s42,65536\n" SA"s50,s40,768\n" BL"v2,s[20:23],s50 offen lds\n" MF"a[136:139],v[80:83],v[40:43],a[136:139]\n" TR"v[140:141],v13 offset:16384\n" WL"(8)\n" TR"v[142:143],v13 offset:17408\n"
 MF"a[140:143],v[80:83],v[44:47],a[140:143]\n" MF"a[144:147],v[80:83],v[48:51],a[144:147]\n" MF"a[148:151],v[80:83],v[52:55],a[148:151]\n" MF"a[152:155],v[80:83],v[56:59],a[152:155]\n"
 MF"a[156:159],v[80:83],v[60:63],a[156:159]\n" TR"v[144:145],v14 offset:16384\n" TR"v[146:147],v14 offset:17408\n" MF"a[160:163],v[84:87],v[32:35],a[160:163]\n" SA"m0,s42,66560\n" SA"s50,s40,8960\n"
 BL"v3,s[20:23],s50 offen lds\n" MF"a[164:167],v[84:87],v[36:39],a[164:167]\n" MF"a[168:171],v[84:87],v[40:43],a[168:171]\n" MF"a[172:175],v[84:87],v[44:47],a[172:175]\n"
 MF"a[176:179],v[84:87],v[48:51],a[176:179]\n" TR"v[148:149],v15 offset:16384\n" TR"v[150:151],v15 offset:17408\n" MF"a[180:183],v[84:87],v[52:55],a[180:183]\n"
 MF"a[184:187],v[84:87],v[56:59],a[184:187]\n" MF"a[188:191],v[84:87],v[60:63],a[188:191]\n" SA"m0,s42,67584\n" SA"s50,s40,17152\n" BL"v2,s[20:23],s50 offen lds\n"
 MF"a[192:195],v[88:91],v[32:35],a[192:195]\n" MF"a[196:199],v[88:91],v[36:39],a[196:199]\n" TR"v[152:153],v16 offset:16384\n" TR"v[154:155],v16 offset:17408\n"
 MF"a[200:203],v[88:91],v[40:43],a[200:203]\n" MF"a[204:207],v[88:91],v[44:47],a[204:207]\n" MF"a[208:211],v[88:91],v[48:51],a[208:211]\n" MF"a[212:215],v[88:91],v[52:55],a[212:215]\n"
 MF"a[216:219],v[88:91],v[56:59],a[216:219]\n" WL"(8)\n" TR"v[156:157],v17 offset:16384\n" TR"v[158:159],v17 offset:17408\n" SA"m0,s42,68608\n" SA"s50,s40,25344\n" BL"v3,s[20:23],s50 offen lds\n"
 MF"a[220:223],v[88:91],v[60:63],a[220:223]\n" MF"a[224:227],v[92:95],v[32:35],a[224:227]\n" MF"a[228:231],v[92:95],v[36:39],a[228:231]\n" MF"a[232:235],v[92:95],v[40:43],a[232:235]\n"
 MF"a[236:239],v[92:95],v[44:47],a[236:239]\n" MF"a[240:243],v[92:95],v[48:51],a[240:243]\n" MF"a[244:247],v[92:95],v[52:55],a[244:247]\n" MF"a[248:251],v[92:95],v[56:59],a[248:251]\n"
 MF"a[252:255],v[92:95],v[60:63],a[252:255]\n" MF"a[0:3],v[128:131],v[96:99],a[0:3]\n" WV"(9) lgkmcnt(0)\n""s_barrier\n" MF"a[4:7],v[128:131],v[100:103],a[4:7]\n"
 MF"a[8:11],v[128:131],v[104:107],a[8:11]\n" DR"v[32:35],v8 offset:0\n" MF"a[12:15],v[128:131],v[108:111],a[12:15]\n" MF"a[16:19],v[128:131],v[112:115],a[16:19]\n" DR"v[36:39],v8 offset:2048\n"
 MF"a[20:23],v[128:131],v[116:119],a[20:23]\n" SA"m0,s43,0\n" SA"s50,s41,3145728\n" BL"v4,s[16:19],s50 offen lds\n" MF"a[24:27],v[128:131],v[120:123],a[24:27]\n" DR"v[40:43],v8 offset:4096\n"
 MF"a[28:31],v[128:131],v[124:127],a[28:31]\n" MF"a[32:35],v[132:135],v[96:99],a[32:35]\n" DR"v[44:47],v8 offset:6144\n" MF"a[36:39],v[132:135],v[100:103],a[36:39]\n"
 MF"a[40:43],v[132:135],v[104:107],a[40:43]\n" DR"v[48:51],v8 offset:8192\n" MF"a[44:47],v[132:135],v[108:111],a[44:47]\n" MF"a[48:51],v[132:135],v[112:115],a[48:51]\n" DR"v[52:55],v8 offset:10240\n"
 SA"m0,s43,1024\n" SA"s50,s41,3178496\n" BL"v4,s[16:19],s50 offen lds\n" MF"a[52:55],v[132:135],v[116:119],a[52:55]\n" MF"a[56:59],v[132:135],v[120:123],a[56:59]\n" DR"v[56:59],v8 offset:12288\n"
 MF"a[60:63],v[132:135],v[124:127],a[60:63]\n" MF"a[64:67],v[136:139],v[96:99],a[64:67]\n" DR"v[60:63],v8 offset:14336\n" MF"a[68:71],v[136:139],v[100:103],a[68:71]\n"
 MF"a[72:75],v[136:139],v[104:107],a[72:75]\n" MF"a[76:79],v[136:139],v[108:111],a[76:79]\n" TR"v[64:65],v10 offset:32768\n" TR"v[66:67],v10 offset:33792\n" SA"m0,s43,2048\n" SA"s50,s41,3211264\n"
 BL"v5,s[16:19],s50 offen lds\n" MF"a[80:83],v[136:139],v[112:115],a[80:83]\n" MF"a[84:87],v[136:139],v[116:119],a[84:87]\n" MF"a[88:91],v[136:139],v[120:123],a[88:91]\n"
 MF"a[92:95],v[136:139],v[124:127],a[92:95]\n" MF"a[96:99],v[140:143],v[96:99],a[96:99]\n" TR"v[68:69],v11 offset:32768\n" TR"v[70:71],v11 offset:33792\n"
 MF"a[100:103],v[140:143],v[100:103],a[100:103]\n" MF"a[104:107],v[140:143],v[104:107],a[104:107]\n" SA"m0,s43,3072\n" SA"s50,s41,3244032\n" BL"v5,s[16:19],s50 offen lds\n"
 MF"a[108:111],v[140:143],v[108:111],a[108:111]\n" MF"a[112:115],v[140:143],v[112:115],a[112:115]\n" MF"a[116:119],v[140:143],v[116:119],a[116:119]\n" TR"v[72:73],v12 offset:32768\n"
 TR"v[74:75],v12 offset:33792\n" MF"a[120:123],v[140:143],v[120:123],a[120:123]\n" MF"a[124:127],v[140:143],v[124:127],a[124:127]\n" MF"a[128:131],v[144:147],v[96:99],a[128:131]\n"
 MF"a[132:135],v[144:147],v[100:103],a[132:135]\n" SA"m0,s42,69632\n" SA"s50,s40,33536\n" BL"v2,s[20:23],s50 offen lds\n" MF"a[136:139],v[144:147],v[104:107],a[136:139]\n"
 TR"v[76:77],v13 offset:32768\n" WL"(8)\n" TR"v[78:79],v13 offset:33792\n" MF"a[140:143],v[144:147],v[108:111],a[140:143]\n" MF"a[144:147],v[144:147],v[112:115],a[144:147]\n"
 MF"a[148:151],v[144:147],v[116:119],a[148:151]\n" MF"a[152:155],v[144:147],v[120:123],a[152:155]\n" MF"a[156:159],v[144:147],v[124:127],a[156:159]\n" TR"v[80:81],v14 offset:32768\n"
 TR"v[82:83],v14 offset:33792\n" MF"a[160:163],v[148:151],v[96:99],a[160:163]\n" SA"m0,s42,70656\n" SA"s50,s40,41728\n" BL"v3,s[20:23],s50 offen lds\n"
 MF"a[164:167],v[148:151],v[100:103],a[164:167]\n" MF"a[168:171],v[148:151],v[104:107],a[168:171]\n" MF"a[172:175],v[148:151],v[108:111],a[172:175]\n" MF"a[176:179],v[148:151],v[112:115],a[176:179]\n"
 TR"v[84:85],v15 offset:32768\n" TR"v[86:87],v15 offset:33792\n" MF"a[180:183],v[148:151],v[116:119],a[180:183]\n" MF"a[184:187],v[148:151],v[120:123],a[184:187]\n"
 MF"a[188:191],v[148:151],v[124:127],a[188:191]\n" SA"m0,s42,71680\n" SA"s50,s40,49920\n" BL"v2,s[20:23],s50 offen lds\n" MF"a[192:195],v[152:155],v[96:99],a[192:195]\n"
 MF"a[196:199],v[152:155],v[100:103],a[196:199]\n" TR"v[88:89],v16 offset:32768\n" TR"v[90:91],v16 offset:33792\n" MF"a[200:203],v[152:155],v[104:107],a[200:203]\n"
 MF"a[204:207],v[152:155],v[108:111],a[204:207]\n" MF"a[208:211],v[152:155],v[112:115],a[208:211]\n" MF"a[212:215],v[152:155],v[116:119],a[212:215]\n" MF"a[216:219],v[152:155],v[120:123],a[216:219]\n"
 WL"(8)\n" TR"v[92:93],v17 offset:32768\n" TR"v[94:95],v17 offset:33792\n" SA"m0,s42,72704\n" SA"s50,s40,58112\n" BL"v3,s[20:23],s50 offen lds\n" MF"a[220:223],v[152:155],v[124:127],a[220:223]\n"
 MF"a[224:227],v[156:159],v[96:99],a[224:227]\n" MF"a[228:231],v[156:159],v[100:103],a[228:231]\n" MF"a[232:235],v[156:159],v[104:107],a[232:235]\n" MF"a[236:239],v[156:159],v[108:111],a[236:239]\n"
 MF"a[240:243],v[156:159],v[112:115],a[240:243]\n" MF"a[244:247],v[156:159],v[116:119],a[244:247]\n" MF"a[248:251],v[156:159],v[120:123],a[248:251]\n" MF"a[252:255],v[156:159],v[124:127],a[252:255]\n"
 MF"a[0:3],v[64:67],v[32:35],a[0:3]\n" WV"(12) lgkmcnt(0)\n""s_barrier\n" MF"a[4:7],v[64:67],v[36:39],a[4:7]\n" MF"a[8:11],v[64:67],v[40:43],a[8:11]\n" DR"v[96:99],v9 offset:0\n"
 MF"a[12:15],v[64:67],v[44:47],a[12:15]\n" MF"a[16:19],v[64:67],v[48:51],a[16:19]\n" DR"v[100:103],v9 offset:2048\n" MF"a[20:23],v[64:67],v[52:55],a[20:23]\n" SA"m0,s43,16384\n" SA"s50,s41,3407872\n"
 BL"v4,s[16:19],s50 offen lds\n" MF"a[24:27],v[64:67],v[56:59],a[24:27]\n" DR"v[104:107],v9 offset:4096\n" MF"a[28:31],v[64:67],v[60:63],a[28:31]\n" MF"a[32:35],v[68:71],v[32:35],a[32:35]\n"
 DR"v[108:111],v9 offset:6144\n" MF"a[36:39],v[68:71],v[36:39],a[36:39]\n" MF"a[40:43],v[68:71],v[40:43],a[40:43]\n" DR"v[112:115],v9 offset:8192\n" MF"a[44:47],v[68:71],v[44:47],a[44:47]\n"
 MF"a[48:51],v[68:71],v[48:51],a[48:51]\n" DR"v[116:119],v9 offset:10240\n" SA"m0,s43,17408\n" SA"s50,s41,3440640\n" BL"v4,s[16:19],s50 offen lds\n" MF"a[52:55],v[68:71],v[52:55],a[52:55]\n"
 MF"a[56:59],v[68:71],v[56:59],a[56:59]\n" DR"v[120:123],v9 offset:12288\n" MF"a[60:63],v[68:71],v[60:63],a[60:63]\n" MF"a[64:67],v[72:75],v[32:35],a[64:67]\n" DR"v[124:127],v9 offset:14336\n"
 MF"a[68:71],v[72:75],v[36:39],a[68:71]\n" MF"a[72:75],v[72:75],v[40:43],a[72:75]\n" MF"a[76:79],v[72:75],v[44:47],a[76:79]\n" TR"v[128:129],v10 offset:49152\n" TR"v[130:131],v10 offset:50176\n"
 SA"m0,s43,18432\n" SA"s50,s41,3473408\n" BL"v5,s[16:19],s50 offen lds\n" MF"a[80:83],v[72:75],v[48:51],a[80:83]\n" MF"a[84:87],v[72:75],v[52:55],a[84:87]\n" MF"a[88:91],v[72:75],v[56:59],a[88:91]\n"
 MF"a[92:95],v[72:75],v[60:63],a[92:95]\n" MF"a[96:99],v[76:79],v[32:35],a[96:99]\n" TR"v[132:133],v11 offset:49152\n" TR"v[134:135],v11 offset:50176\n" MF"a[100:103],v[76:79],v[36:39],a[100:103]\n"
 MF"a[104:107],v[76:79],v[40:43],a[104:107]\n" SA"m0,s43,19456\n" SA"s50,s41,3506176\n" BL"v5,s[16:19],s50 offen lds\n" MF"a[108:111],v[76:79],v[44:47],a[108:111]\n"
 MF"a[112:115],v[76:79],v[48:51],a[112:115]\n" MF"a[116:119],v[76:79],v[52:55],a[116:119]\n" TR"v[136:137],v12 offset:49152\n" TR"v[138:139],v12 offset:50176\n"
 MF"a[120:123],v[76:79],v[56:59],a[120:123]\n" MF"a[124:127],v[76:79],v[60:63],a[124:127]\n" MF"a[128:131],v[80:83],v[32:35],a[128:131]\n" MF"a[132:135],v[80:83],v[36:39],a[132:135]\n"
 SA"m0,s42,98304\n" SA"s50,s40,896\n" BL"v2,s[20:23],s50 offen lds\n" MF"a[136:139],v[80:83],v[40:43],a[136:139]\n" TR"v[140:141],v13 offset:49152\n" WL"(8)\n" TR"v[142:143],v13 offset:50176\n"
 MF"a[140:143],v[80:83],v[44:47],a[140:143]\n" MF"a[144:147],v[80:83],v[48:51],a[144:147]\n" MF"a[148:151],v[80:83],v[52:55],a[148:151]\n" MF"a[152:155],v[80:83],v[56:59],a[152:155]\n"
 MF"a[156:159],v[80:83],v[60:63],a[156:159]\n" TR"v[144:145],v14 offset:49152\n" TR"v[146:147],v14 offset:50176\n" MF"a[160:163],v[84:87],v[32:35],a[160:163]\n" SA"m0,s42,99328\n" SA"s50,s40,9088\n"
 BL"v3,s[20:23],s50 offen lds\n" MF"a[164:167],v[84:87],v[36:39],a[164:167]\n" MF"a[168:171],v[84:87],v[40:43],a[168:171]\n" MF"a[172:175],v[84:87],v[44:47],a[172:175]\n"
 MF"a[176:179],v[84:87],v[48:51],a[176:179]\n" TR"v[148:149],v15 offset:49152\n" TR"v[150:151],v15 offset:50176\n" MF"a[180:183],v[84:87],v[52:55],a[180:183]\n"
 MF"a[184:187],v[84:87],v[56:59],a[184:187]\n" MF"a[188:191],v[84:87],v[60:63],a[188:191]\n" SA"m0,s42,100352\n" SA"s50,s40,17280\n" BL"v2,s[20:23],s50 offen lds\n"
 MF"a[192:195],v[88:91],v[32:35],a[192:195]\n" MF"a[196:199],v[88:91],v[36:39],a[196:199]\n" TR"v[152:153],v16 offset:49152\n" TR"v[154:155],v16 offset:50176\n"
 MF"a[200:203],v[88:91],v[40:43],a[200:203]\n" MF"a[204:207],v[88:91],v[44:47],a[204:207]\n" MF"a[208:211],v[88:91],v[48:51],a[208:211]\n" MF"a[212:215],v[88:91],v[52:55],a[212:215]\n"
 MF"a[216:219],v[88:91],v[56:59],a[216:219]\n" WL"(8)\n" TR"v[156:157],v17 offset:49152\n" TR"v[158:159],v17 offset:50176\n" SA"m0,s42,101376\n" SA"s50,s40,25472\n" BL"v3,s[20:23],s50 offen lds\n"
 MF"a[220:223],v[88:91],v[60:63],a[220:223]\n" MF"a[224:227],v[92:95],v[32:35],a[224:227]\n" MF"a[228:231],v[92:95],v[36:39],a[228:231]\n" MF"a[232:235],v[92:95],v[40:43],a[232:235]\n"
 MF"a[236:239],v[92:95],v[44:47],a[236:239]\n" MF"a[240:243],v[92:95],v[48:51],a[240:243]\n" MF"a[244:247],v[92:95],v[52:55],a[244:247]\n" MF"a[248:251],v[92:95],v[56:59],a[248:251]\n"
 MF"a[252:255],v[92:95],v[60:63],a[252:255]\n" MF"a[0:3],v[128:131],v[96:99],a[0:3]\n" WV"(8) lgkmcnt(0)\n""s_barrier\n" WV"(24)\n""v_cmp_ne_u64 vcc,s[98:99],v[24:25]\n""s_nop 1\n"
"s_cmp_eq_u64 vcc,0\n""s_cbranch_scc1 Lokp1k_%=\n""s_mov_b32 s57,32768\n""Lspp1k_%=:\n""s_sleep 2\n""buffer_load_dwordx2 v[24:25],v20,s[92:95],0 offen sc1\n" WV"(0)\n"
"v_cmp_ne_u64 vcc,s[98:99],v[24:25]\n""s_nop 1\n""s_cmp_eq_u64 vcc,0\n""s_cbranch_scc1 Lokp1k_%=\n""s_sub_u32 s57,s57,1\n""s_cmp_lg_u32 s57,0\n""s_cbranch_scc1 Lspp1k_%=\n""Lokp1k_%=:\n"
 MF"a[4:7],v[128:131],v[100:103],a[4:7]\n" MF"a[8:11],v[128:131],v[104:107],a[8:11]\n" DR"v[32:35],v6 offset:0\n" MF"a[12:15],v[128:131],v[108:111],a[12:15]\n"
 MF"a[16:19],v[128:131],v[112:115],a[16:19]\n" DR"v[36:39],v6 offset:2048\n" MF"a[20:23],v[128:131],v[116:119],a[20:23]\n" SA"m0,s43,32768\n" SA"s50,s41,3670016\n" BL"v4,s[16:19],s50 offen lds\n"
 MF"a[24:27],v[128:131],v[120:123],a[24:27]\n" DR"v[40:43],v6 offset:4096\n" MF"a[28:31],v[128:131],v[124:127],a[28:31]\n" MF"a[32:35],v[132:135],v[96:99],a[32:35]\n" DR"v[44:47],v6 offset:6144\n"
 MF"a[36:39],v[132:135],v[100:103],a[36:39]\n" MF"a[40:43],v[132:135],v[104:107],a[40:43]\n" DR"v[48:51],v6 offset:8192\n" MF"a[44:47],v[132:135],v[108:111],a[44:47]\n"
 MF"a[48:51],v[132:135],v[112:115],a[48:51]\n" DR"v[52:55],v6 offset:10240\n" SA"m0,s43,33792\n" SA"s50,s41,3702784\n" BL"v4,s[16:19],s50 offen lds\n" MF"a[52:55],v[132:135],v[116:119],a[52:55]\n"
 MF"a[56:59],v[132:135],v[120:123],a[56:59]\n" DR"v[56:59],v6 offset:12288\n" MF"a[60:63],v[132:135],v[124:127],a[60:63]\n" MF"a[64:67],v[136:139],v[96:99],a[64:67]\n" DR"v[60:63],v6 offset:14336\n"
 MF"a[68:71],v[136:139],v[100:103],a[68:71]\n" MF"a[72:75],v[136:139],v[104:107],a[72:75]\n" MF"a[76:79],v[136:139],v[108:111],a[76:79]\n" TR"v[64:65],v10 offset:0\n" TR"v[66:67],v10 offset:1024\n"
 SA"m0,s43,34816\n" SA"s50,s41,3735552\n" BL"v5,s[16:19],s50 offen lds\n" MF"a[80:83],v[136:139],v[112:115],a[80:83]\n" MF"a[84:87],v[136:139],v[116:119],a[84:87]\n"
 MF"a[88:91],v[136:139],v[120:123],a[88:91]\n" MF"a[92:95],v[136:139],v[124:127],a[92:95]\n" MF"a[96:99],v[140:143],v[96:99],a[96:99]\n" TR"v[68:69],v11 offset:0\n" TR"v[70:71],v11 offset:1024\n"
 MF"a[100:103],v[140:143],v[100:103],a[100:103]\n" MF"a[104:107],v[140:143],v[104:107],a[104:107]\n" SA"m0,s43,35840\n" SA"s50,s41,3768320\n" BL"v5,s[16:19],s50 offen lds\n"
 MF"a[108:111],v[140:143],v[108:111],a[108:111]\n" MF"a[112:115],v[140:143],v[112:115],a[112:115]\n" MF"a[116:119],v[140:143],v[116:119],a[116:119]\n" TR"v[72:73],v12 offset:0\n"
 TR"v[74:75],v12 offset:1024\n" MF"a[120:123],v[140:143],v[120:123],a[120:123]\n" MF"a[124:127],v[140:143],v[124:127],a[124:127]\n" MF"a[128:131],v[144:147],v[96:99],a[128:131]\n"
 MF"a[132:135],v[144:147],v[100:103],a[132:135]\n" SA"m0,s42,102400\n" SA"s50,s40,33664\n" BL"v2,s[20:23],s50 offen lds\n" MF"a[136:139],v[144:147],v[104:107],a[136:139]\n" TR"v[76:77],v13 offset:0\n"
 WL"(8)\n" TR"v[78:79],v13 offset:1024\n" MF"a[140:143],v[144:147],v[108:111],a[140:143]\n" MF"a[144:147],v[144:147],v[112:115],a[144:147]\n" MF"a[148:151],v[144:147],v[116:119],a[148:151]\n"
 MF"a[152:155],v[144:147],v[120:123],a[152:155]\n" MF"a[156:159],v[144:147],v[124:127],a[156:159]\n" TR"v[80:81],v14 offset:0\n" TR"v[82:83],v14 offset:1024\n"
 MF"a[160:163],v[148:151],v[96:99],a[160:163]\n" SA"m0,s42,103424\n" SA"s50,s40,41856\n" BL"v3,s[20:23],s50 offen lds\n" MF"a[164:167],v[148:151],v[100:103],a[164:167]\n"
 MF"a[168:171],v[148:151],v[104:107],a[168:171]\n" MF"a[172:175],v[148:151],v[108:111],a[172:175]\n" MF"a[176:179],v[148:151],v[112:115],a[176:179]\n" TR"v[84:85],v15 offset:0\n"
 TR"v[86:87],v15 offset:1024\n" MF"a[180:183],v[148:151],v[116:119],a[180:183]\n" MF"a[184:187],v[148:151],v[120:123],a[184:187]\n" MF"a[188:191],v[148:151],v[124:127],a[188:191]\n"
 SA"m0,s42,104448\n" SA"s50,s40,50048\n" BL"v2,s[20:23],s50 offen lds\n" MF"a[192:195],v[152:155],v[96:99],a[192:195]\n" MF"a[196:199],v[152:155],v[100:103],a[196:199]\n" TR"v[88:89],v16 offset:0\n"
 TR"v[90:91],v16 offset:1024\n" MF"a[200:203],v[152:155],v[104:107],a[200:203]\n" MF"a[204:207],v[152:155],v[108:111],a[204:207]\n" MF"a[208:211],v[152:155],v[112:115],a[208:211]\n"
 MF"a[212:215],v[152:155],v[116:119],a[212:215]\n" MF"a[216:219],v[152:155],v[120:123],a[216:219]\n" WL"(8)\n" TR"v[92:93],v17 offset:0\n" TR"v[94:95],v17 offset:1024\n" SA"m0,s42,105472\n"
 SA"s50,s40,58240\n" BL"v3,s[20:23],s50 offen lds\n" MF"a[220:223],v[152:155],v[124:127],a[220:223]\n" MF"a[224:227],v[156:159],v[96:99],a[224:227]\n" MF"a[228:231],v[156:159],v[100:103],a[228:231]\n"
 MF"a[232:235],v[156:159],v[104:107],a[232:235]\n" MF"a[236:239],v[156:159],v[108:111],a[236:239]\n" MF"a[240:243],v[156:159],v[112:115],a[240:243]\n" MF"a[244:247],v[156:159],v[116:119],a[244:247]\n"
 MF"a[248:251],v[156:159],v[120:123],a[248:251]\n" MF"a[252:255],v[156:159],v[124:127],a[252:255]\n" MF"a[0:3],v[64:67],v[32:35],a[0:3]\n" WV"(12) lgkmcnt(0)\n""s_barrier\n" SA"s72,s52,20971520\n"
"s_addc_u32 s73,s53,0\n""s_mov_b32 s74,2097152\n""s_mov_b32 s75,0x20000\n""s_lshr_b32 s58,s96,5\n""s_sub_u32 s59,15,s58\n""s_bitcmp1_b32 s36,1\n""s_cselect_b32 s58,s59,s58\n""s_lshl_b32 s58,s58,17\n"
"s_bfe_u32 s59,s96,0x20003\n""s_lshl_b32 s59,s59,15\n" SA"s58,s58,s59\n""s_and_b32 s59,s36,1\n""s_lshl_b32 s59,s59,14\n" SA"s58,s58,s59\n""s_mov_b32 s57,s58\n""v_mbcnt_lo_u32_b32 v28,-1,0\n"
"v_mbcnt_hi_u32_b32 v28,-1,v28\n""v_mul_u32_u24 v28,128,v28\n""buffer_load_dword v29,v28,s[72:75],s57 offen\n" MF"a[4:7],v[64:67],v[36:39],a[4:7]\n" MF"a[8:11],v[64:67],v[40:43],a[8:11]\n"
 DR"v[96:99],v7 offset:0\n" MF"a[12:15],v[64:67],v[44:47],a[12:15]\n" MF"a[16:19],v[64:67],v[48:51],a[16:19]\n" DR"v[100:103],v7 offset:2048\n" MF"a[20:23],v[64:67],v[52:55],a[20:23]\n"
 SA"m0,s43,49152\n" SA"s50,s41,3932160\n" BL"v4,s[16:19],s50 offen lds\n" MF"a[24:27],v[64:67],v[56:59],a[24:27]\n" DR"v[104:107],v7 offset:4096\n" MF"a[28:31],v[64:67],v[60:63],a[28:31]\n"
 MF"a[32:35],v[68:71],v[32:35],a[32:35]\n" DR"v[108:111],v7 offset:6144\n" MF"a[36:39],v[68:71],v[36:39],a[36:39]\n" MF"a[40:43],v[68:71],v[40:43],a[40:43]\n" DR"v[112:115],v7 offset:8192\n"
 MF"a[44:47],v[68:71],v[44:47],a[44:47]\n" MF"a[48:51],v[68:71],v[48:51],a[48:51]\n" DR"v[116:119],v7 offset:10240\n" SA"m0,s43,50176\n" SA"s50,s41,3964928\n" BL"v4,s[16:19],s50 offen lds\n"
 MF"a[52:55],v[68:71],v[52:55],a[52:55]\n" MF"a[56:59],v[68:71],v[56:59],a[56:59]\n" DR"v[120:123],v7 offset:12288\n" MF"a[60:63],v[68:71],v[60:63],a[60:63]\n"
 MF"a[64:67],v[72:75],v[32:35],a[64:67]\n" DR"v[124:127],v7 offset:14336\n" MF"a[68:71],v[72:75],v[36:39],a[68:71]\n" MF"a[72:75],v[72:75],v[40:43],a[72:75]\n"
 MF"a[76:79],v[72:75],v[44:47],a[76:79]\n" TR"v[128:129],v10 offset:16384\n" TR"v[130:131],v10 offset:17408\n" SA"m0,s43,51200\n" SA"s50,s41,3997696\n" BL"v5,s[16:19],s50 offen lds\n"
 MF"a[80:83],v[72:75],v[48:51],a[80:83]\n" MF"a[84:87],v[72:75],v[52:55],a[84:87]\n" MF"a[88:91],v[72:75],v[56:59],a[88:91]\n" MF"a[92:95],v[72:75],v[60:63],a[92:95]\n"
 MF"a[96:99],v[76:79],v[32:35],a[96:99]\n" TR"v[132:133],v11 offset:16384\n" TR"v[134:135],v11 offset:17408\n" MF"a[100:103],v[76:79],v[36:39],a[100:103]\n"
 MF"a[104:107],v[76:79],v[40:43],a[104:107]\n" SA"m0,s43,52224\n" SA"s50,s41,4030464\n" BL"v5,s[16:19],s50 offen lds\n" MF"a[108:111],v[76:79],v[44:47],a[108:111]\n"
 MF"a[112:115],v[76:79],v[48:51],a[112:115]\n" MF"a[116:119],v[76:79],v[52:55],a[116:119]\n" TR"v[136:137],v12 offset:16384\n" TR"v[138:139],v12 offset:17408\n"
 MF"a[120:123],v[76:79],v[56:59],a[120:123]\n" MF"a[124:127],v[76:79],v[60:63],a[124:127]\n" MF"a[128:131],v[80:83],v[32:35],a[128:131]\n" MF"a[132:135],v[80:83],v[36:39],a[132:135]\n"
 MF"a[136:139],v[80:83],v[40:43],a[136:139]\n" TR"v[140:141],v13 offset:16384\n" WL"(8)\n" TR"v[142:143],v13 offset:17408\n" MF"a[140:143],v[80:83],v[44:47],a[140:143]\n"
 MF"a[144:147],v[80:83],v[48:51],a[144:147]\n" MF"a[148:151],v[80:83],v[52:55],a[148:151]\n" MF"a[152:155],v[80:83],v[56:59],a[152:155]\n" MF"a[156:159],v[80:83],v[60:63],a[156:159]\n"
 TR"v[144:145],v14 offset:16384\n" TR"v[146:147],v14 offset:17408\n" MF"a[160:163],v[84:87],v[32:35],a[160:163]\n" MF"a[164:167],v[84:87],v[36:39],a[164:167]\n"
 MF"a[168:171],v[84:87],v[40:43],a[168:171]\n" MF"a[172:175],v[84:87],v[44:47],a[172:175]\n" MF"a[176:179],v[84:87],v[48:51],a[176:179]\n" TR"v[148:149],v15 offset:16384\n"
 TR"v[150:151],v15 offset:17408\n" MF"a[180:183],v[84:87],v[52:55],a[180:183]\n" MF"a[184:187],v[84:87],v[56:59],a[184:187]\n" MF"a[188:191],v[84:87],v[60:63],a[188:191]\n"
 MF"a[192:195],v[88:91],v[32:35],a[192:195]\n" MF"a[196:199],v[88:91],v[36:39],a[196:199]\n" TR"v[152:153],v16 offset:16384\n" TR"v[154:155],v16 offset:17408\n"
 MF"a[200:203],v[88:91],v[40:43],a[200:203]\n" MF"a[204:207],v[88:91],v[44:47],a[204:207]\n" MF"a[208:211],v[88:91],v[48:51],a[208:211]\n" MF"a[212:215],v[88:91],v[52:55],a[212:215]\n"
 MF"a[216:219],v[88:91],v[56:59],a[216:219]\n" WL"(8)\n" TR"v[156:157],v17 offset:16384\n" TR"v[158:159],v17 offset:17408\n" MF"a[220:223],v[88:91],v[60:63],a[220:223]\n"
 MF"a[224:227],v[92:95],v[32:35],a[224:227]\n" MF"a[228:231],v[92:95],v[36:39],a[228:231]\n" MF"a[232:235],v[92:95],v[40:43],a[232:235]\n" MF"a[236:239],v[92:95],v[44:47],a[236:239]\n"
 MF"a[240:243],v[92:95],v[48:51],a[240:243]\n" MF"a[244:247],v[92:95],v[52:55],a[244:247]\n" MF"a[248:251],v[92:95],v[56:59],a[248:251]\n" MF"a[252:255],v[92:95],v[60:63],a[252:255]\n"
 MF"a[0:3],v[128:131],v[96:99],a[0:3]\n" WV"(5) lgkmcnt(0)\n""s_barrier\n" SA"s57,s58,8192\n""v_mbcnt_lo_u32_b32 v28,-1,0\n""v_mbcnt_hi_u32_b32 v28,-1,v28\n""v_mul_u32_u24 v28,128,v28\n"
"buffer_load_dword v29,v28,s[72:75],s57 offen\n" SA"s76,s52,3145728\n""s_addc_u32 s77,s53,0\n""s_mov_b32 s78,2097152\n""s_mov_b32 s79,0x20000\n""s_lshr_b32 s60,s96,5\n""s_and_b32 s61,s96,7\n"
"s_lshl_b32 s61,s61,2\n""s_bfe_u32 s57,s96,0x20003\n" SA"s61,s61,s57\n""s_lshl_b32 s62,s61,2\n" SA"s62,s62,s36\n""s_lshl_b32 s57,s61,16\n""s_lshl_b32 s58,s60,13\n" SA"s57,s57,s58\n"
"v_mbcnt_lo_u32_b32 v28,-1,0\n""v_mbcnt_hi_u32_b32 v28,-1,v28\n""v_mul_u32_u24 v28,128,v28\n""buffer_load_dword v29,v28,s[76:79],s57 offen\n" MF"a[4:7],v[128:131],v[100:103],a[4:7]\n"
 MF"a[8:11],v[128:131],v[104:107],a[8:11]\n" DR"v[32:35],v6 offset:32768\n" MF"a[12:15],v[128:131],v[108:111],a[12:15]\n" MF"a[16:19],v[128:131],v[112:115],a[16:19]\n" DR"v[36:39],v6 offset:34816\n"
 MF"a[20:23],v[128:131],v[116:119],a[20:23]\n" MF"a[24:27],v[128:131],v[120:123],a[24:27]\n" DR"v[40:43],v6 offset:36864\n" MF"a[28:31],v[128:131],v[124:127],a[28:31]\n"
 MF"a[32:35],v[132:135],v[96:99],a[32:35]\n" DR"v[44:47],v6 offset:38912\n" MF"a[36:39],v[132:135],v[100:103],a[36:39]\n" MF"a[40:43],v[132:135],v[104:107],a[40:43]\n" DR"v[48:51],v6 offset:40960\n"
 MF"a[44:47],v[132:135],v[108:111],a[44:47]\n" MF"a[48:51],v[132:135],v[112:115],a[48:51]\n" DR"v[52:55],v6 offset:43008\n" MF"a[52:55],v[132:135],v[116:119],a[52:55]\n"
 MF"a[56:59],v[132:135],v[120:123],a[56:59]\n" DR"v[56:59],v6 offset:45056\n" MF"a[60:63],v[132:135],v[124:127],a[60:63]\n" MF"a[64:67],v[136:139],v[96:99],a[64:67]\n" DR"v[60:63],v6 offset:47104\n"
 MF"a[68:71],v[136:139],v[100:103],a[68:71]\n" MF"a[72:75],v[136:139],v[104:107],a[72:75]\n" MF"a[76:79],v[136:139],v[108:111],a[76:79]\n" TR"v[64:65],v10 offset:32768\n"
 TR"v[66:67],v10 offset:33792\n" MF"a[80:83],v[136:139],v[112:115],a[80:83]\n" MF"a[84:87],v[136:139],v[116:119],a[84:87]\n" MF"a[88:91],v[136:139],v[120:123],a[88:91]\n"
 MF"a[92:95],v[136:139],v[124:127],a[92:95]\n" MF"a[96:99],v[140:143],v[96:99],a[96:99]\n" TR"v[68:69],v11 offset:32768\n" TR"v[70:71],v11 offset:33792\n"
 MF"a[100:103],v[140:143],v[100:103],a[100:103]\n" MF"a[104:107],v[140:143],v[104:107],a[104:107]\n" MF"a[108:111],v[140:143],v[108:111],a[108:111]\n" MF"a[112:115],v[140:143],v[112:115],a[112:115]\n"
 MF"a[116:119],v[140:143],v[116:119],a[116:119]\n" TR"v[72:73],v12 offset:32768\n" TR"v[74:75],v12 offset:33792\n" MF"a[120:123],v[140:143],v[120:123],a[120:123]\n"
 MF"a[124:127],v[140:143],v[124:127],a[124:127]\n" MF"a[128:131],v[144:147],v[96:99],a[128:131]\n" MF"a[132:135],v[144:147],v[100:103],a[132:135]\n" MF"a[136:139],v[144:147],v[104:107],a[136:139]\n"
 TR"v[76:77],v13 offset:32768\n" WL"(8)\n" TR"v[78:79],v13 offset:33792\n" MF"a[140:143],v[144:147],v[108:111],a[140:143]\n" MF"a[144:147],v[144:147],v[112:115],a[144:147]\n"
 MF"a[148:151],v[144:147],v[116:119],a[148:151]\n" MF"a[152:155],v[144:147],v[120:123],a[152:155]\n" MF"a[156:159],v[144:147],v[124:127],a[156:159]\n" TR"v[80:81],v14 offset:32768\n"
 TR"v[82:83],v14 offset:33792\n" MF"a[160:163],v[148:151],v[96:99],a[160:163]\n" MF"a[164:167],v[148:151],v[100:103],a[164:167]\n" MF"a[168:171],v[148:151],v[104:107],a[168:171]\n"
 MF"a[172:175],v[148:151],v[108:111],a[172:175]\n" MF"a[176:179],v[148:151],v[112:115],a[176:179]\n" TR"v[84:85],v15 offset:32768\n" TR"v[86:87],v15 offset:33792\n"
 MF"a[180:183],v[148:151],v[116:119],a[180:183]\n" MF"a[184:187],v[148:151],v[120:123],a[184:187]\n" MF"a[188:191],v[148:151],v[124:127],a[188:191]\n" MF"a[192:195],v[152:155],v[96:99],a[192:195]\n"
 MF"a[196:199],v[152:155],v[100:103],a[196:199]\n" TR"v[88:89],v16 offset:32768\n" TR"v[90:91],v16 offset:33792\n" MF"a[200:203],v[152:155],v[104:107],a[200:203]\n"
 MF"a[204:207],v[152:155],v[108:111],a[204:207]\n" MF"a[208:211],v[152:155],v[112:115],a[208:211]\n" MF"a[212:215],v[152:155],v[116:119],a[212:215]\n" MF"a[216:219],v[152:155],v[120:123],a[216:219]\n"
 WL"(8)\n" TR"v[92:93],v17 offset:32768\n" TR"v[94:95],v17 offset:33792\n" MF"a[220:223],v[152:155],v[124:127],a[220:223]\n" MF"a[224:227],v[156:159],v[96:99],a[224:227]\n"
 MF"a[228:231],v[156:159],v[100:103],a[228:231]\n" MF"a[232:235],v[156:159],v[104:107],a[232:235]\n" MF"a[236:239],v[156:159],v[108:111],a[236:239]\n" MF"a[240:243],v[156:159],v[112:115],a[240:243]\n"
 MF"a[244:247],v[156:159],v[116:119],a[244:247]\n" MF"a[248:251],v[156:159],v[120:123],a[248:251]\n" MF"a[252:255],v[156:159],v[124:127],a[252:255]\n" MF"a[0:3],v[64:67],v[32:35],a[0:3]\n"
 WV"(2) lgkmcnt(0)\n""s_barrier\n" MF"a[4:7],v[64:67],v[36:39],a[4:7]\n" MF"a[8:11],v[64:67],v[40:43],a[8:11]\n" DR"v[96:99],v7 offset:32768\n" MF"a[12:15],v[64:67],v[44:47],a[12:15]\n"
 MF"a[16:19],v[64:67],v[48:51],a[16:19]\n" DR"v[100:103],v7 offset:34816\n" MF"a[20:23],v[64:67],v[52:55],a[20:23]\n" MF"a[24:27],v[64:67],v[56:59],a[24:27]\n" DR"v[104:107],v7 offset:36864\n"
 MF"a[28:31],v[64:67],v[60:63],a[28:31]\n" MF"a[32:35],v[68:71],v[32:35],a[32:35]\n" DR"v[108:111],v7 offset:38912\n" MF"a[36:39],v[68:71],v[36:39],a[36:39]\n"
 MF"a[40:43],v[68:71],v[40:43],a[40:43]\n" DR"v[112:115],v7 offset:40960\n" MF"a[44:47],v[68:71],v[44:47],a[44:47]\n" MF"a[48:51],v[68:71],v[48:51],a[48:51]\n" DR"v[116:119],v7 offset:43008\n"
 MF"a[52:55],v[68:71],v[52:55],a[52:55]\n" MF"a[56:59],v[68:71],v[56:59],a[56:59]\n" DR"v[120:123],v7 offset:45056\n" MF"a[60:63],v[68:71],v[60:63],a[60:63]\n"
 MF"a[64:67],v[72:75],v[32:35],a[64:67]\n" DR"v[124:127],v7 offset:47104\n" MF"a[68:71],v[72:75],v[36:39],a[68:71]\n" MF"a[72:75],v[72:75],v[40:43],a[72:75]\n"
 MF"a[76:79],v[72:75],v[44:47],a[76:79]\n" TR"v[128:129],v10 offset:49152\n" TR"v[130:131],v10 offset:50176\n" MF"a[80:83],v[72:75],v[48:51],a[80:83]\n" MF"a[84:87],v[72:75],v[52:55],a[84:87]\n"
 MF"a[88:91],v[72:75],v[56:59],a[88:91]\n" MF"a[92:95],v[72:75],v[60:63],a[92:95]\n" MF"a[96:99],v[76:79],v[32:35],a[96:99]\n" TR"v[132:133],v11 offset:49152\n" TR"v[134:135],v11 offset:50176\n"
 MF"a[100:103],v[76:79],v[36:39],a[100:103]\n" MF"a[104:107],v[76:79],v[40:43],a[104:107]\n" MF"a[108:111],v[76:79],v[44:47],a[108:111]\n" MF"a[112:115],v[76:79],v[48:51],a[112:115]\n"
 MF"a[116:119],v[76:79],v[52:55],a[116:119]\n" TR"v[136:137],v12 offset:49152\n" TR"v[138:139],v12 offset:50176\n" MF"a[120:123],v[76:79],v[56:59],a[120:123]\n"
 MF"a[124:127],v[76:79],v[60:63],a[124:127]\n" MF"a[128:131],v[80:83],v[32:35],a[128:131]\n" MF"a[132:135],v[80:83],v[36:39],a[132:135]\n" MF"a[136:139],v[80:83],v[40:43],a[136:139]\n"
 TR"v[140:141],v13 offset:49152\n" WL"(8)\n" TR"v[142:143],v13 offset:50176\n" MF"a[140:143],v[80:83],v[44:47],a[140:143]\n" MF"a[144:147],v[80:83],v[48:51],a[144:147]\n"
 MF"a[148:151],v[80:83],v[52:55],a[148:151]\n" MF"a[152:155],v[80:83],v[56:59],a[152:155]\n" MF"a[156:159],v[80:83],v[60:63],a[156:159]\n" TR"v[144:145],v14 offset:49152\n"
 TR"v[146:147],v14 offset:50176\n" MF"a[160:163],v[84:87],v[32:35],a[160:163]\n" MF"a[164:167],v[84:87],v[36:39],a[164:167]\n" MF"a[168:171],v[84:87],v[40:43],a[168:171]\n"
 MF"a[172:175],v[84:87],v[44:47],a[172:175]\n" MF"a[176:179],v[84:87],v[48:51],a[176:179]\n" TR"v[148:149],v15 offset:49152\n" TR"v[150:151],v15 offset:50176\n"
 MF"a[180:183],v[84:87],v[52:55],a[180:183]\n" MF"a[184:187],v[84:87],v[56:59],a[184:187]\n" MF"a[188:191],v[84:87],v[60:63],a[188:191]\n" MF"a[192:195],v[88:91],v[32:35],a[192:195]\n"
 MF"a[196:199],v[88:91],v[36:39],a[196:199]\n" TR"v[152:153],v16 offset:49152\n" TR"v[154:155],v16 offset:50176\n" MF"a[200:203],v[88:91],v[40:43],a[200:203]\n"
 MF"a[204:207],v[88:91],v[44:47],a[204:207]\n" MF"a[208:211],v[88:91],v[48:51],a[208:211]\n" MF"a[212:215],v[88:91],v[52:55],a[212:215]\n" MF"a[216:219],v[88:91],v[56:59],a[216:219]\n" WL"(8)\n"
 TR"v[156:157],v17 offset:49152\n" TR"v[158:159],v17 offset:50176\n" MF"a[220:223],v[88:91],v[60:63],a[220:223]\n" MF"a[224:227],v[92:95],v[32:35],a[224:227]\n"
 MF"a[228:231],v[92:95],v[36:39],a[228:231]\n" MF"a[232:235],v[92:95],v[40:43],a[232:235]\n" MF"a[236:239],v[92:95],v[44:47],a[236:239]\n" MF"a[240:243],v[92:95],v[48:51],a[240:243]\n"
 MF"a[244:247],v[92:95],v[52:55],a[244:247]\n" MF"a[248:251],v[92:95],v[56:59],a[248:251]\n" MF"a[252:255],v[92:95],v[60:63],a[252:255]\n" WL"(0)\n" MF"a[224:227],v[156:159],v[96:99],a[224:227]\n"
 WL"(0)\n""s_barrier\n" MF"a[228:231],v[156:159],v[100:103],a[228:231]\n""v_mbcnt_lo_u32_b32 v20,-1,0\n""v_mbcnt_hi_u32_b32 v20,-1,v20\n""v_lshlrev_b32 v0,4,v20\n""v_lshrrev_b32 v21,3,v20\n"
"v_mul_u32_u24 v21,1024,v21\n""v_lshrrev_b32 v22,4,v20\n""v_lshlrev_b32 v23,1,v22\n""v_and_b32 v2,7,v20\n""v_xor_b32 v23,v23,v2\n""v_lshl_add_u32 v2,v23,4,v21\n" VV"v3,v0\n""s_lshl_b32 s63,s36,12\n"
"s_lshr_b32 s38,s96,5\n""s_and_b32 s39,s96,7\n""s_lshl_b32 s39,s39,2\n""s_bfe_u32 s57,s96,0x20003\n" SA"s39,s39,s57\n""s_lshl_b32 s42,s39,2\n" SA"s42,s42,s36\n""v_readlane_b32 s72,v254,12\n"
"v_readlane_b32 s73,v254,13\n""s_mov_b32 s74,524288\n""s_mov_b32 s75,0x20000\n" SA"s76,s52,3145728\n""s_addc_u32 s77,s53,0\n""s_mov_b32 s78,2097152\n""s_mov_b32 s79,0x20000\n""s_lshl_b32 s40,s36,15\n"
"s_lshl_b32 s57,s38,7\n" SA"s40,s40,s57\n""s_lshl_b32 s41,s42,14\n" MF"a[232:235],v[156:159],v[104:107],a[232:235]\n" SA"m0,s63,98304\n" SA"s58,s40,0\n" BL"v2,s[72:75],s58 offen lds\n"
 MF"a[236:239],v[156:159],v[108:111],a[236:239]\n" SA"m0,s63,99328\n" SA"s58,s40,8192\n" BL"v2,s[72:75],s58 offen lds\n" MF"a[240:243],v[156:159],v[112:115],a[240:243]\n" SA"m0,s63,100352\n"
 SA"s58,s40,16384\n" BL"v2,s[72:75],s58 offen lds\n" MF"a[244:247],v[156:159],v[116:119],a[244:247]\n" SA"m0,s63,101376\n" SA"s58,s40,24576\n" BL"v2,s[72:75],s58 offen lds\n"
 MF"a[248:251],v[156:159],v[120:123],a[248:251]\n" SA"s59,s41,0\n" BL"v[32:35],v3,s[76:79],s59 offen offset:0 sc1\n" MF"a[252:255],v[156:159],v[124:127],a[252:255]\n"
 BL"v[36:39],v3,s[76:79],s59 offen offset:1024 sc1\n" MF"a[192:195],v[152:155],v[96:99],a[192:195]\n" BL"v[40:43],v3,s[76:79],s59 offen offset:2048 sc1\n"
 MF"a[196:199],v[152:155],v[100:103],a[196:199]\n" BL"v[44:47],v3,s[76:79],s59 offen offset:3072 sc1\n" MF"a[200:203],v[152:155],v[104:107],a[200:203]\n" SA"m0,s63,114688\n" SA"s58,s40,131072\n"
 BL"v2,s[72:75],s58 offen lds\n" MF"a[204:207],v[152:155],v[108:111],a[204:207]\n" SA"m0,s63,115712\n" SA"s58,s40,139264\n" BL"v2,s[72:75],s58 offen lds\n"
 MF"a[208:211],v[152:155],v[112:115],a[208:211]\n" SA"m0,s63,116736\n" SA"s58,s40,147456\n" BL"v2,s[72:75],s58 offen lds\n" MF"a[212:215],v[152:155],v[116:119],a[212:215]\n" SA"m0,s63,117760\n"
 SA"s58,s40,155648\n" BL"v2,s[72:75],s58 offen lds\n" MF"a[216:219],v[152:155],v[120:123],a[216:219]\n" SA"s59,s41,4096\n" BL"v[48:51],v3,s[76:79],s59 offen offset:0 sc1\n"
 MF"a[220:223],v[152:155],v[124:127],a[220:223]\n" BL"v[52:55],v3,s[76:79],s59 offen offset:1024 sc1\n" MF"a[160:163],v[148:151],v[96:99],a[160:163]\n"
 BL"v[56:59],v3,s[76:79],s59 offen offset:2048 sc1\n" MF"a[164:167],v[148:151],v[100:103],a[164:167]\n" BL"v[60:63],v3,s[76:79],s59 offen offset:3072 sc1\n"
 MF"a[168:171],v[148:151],v[104:107],a[168:171]\n" SA"m0,s63,131072\n" SA"s58,s40,262144\n" BL"v2,s[72:75],s58 offen lds\n" MF"a[172:175],v[148:151],v[108:111],a[172:175]\n" SA"m0,s63,132096\n"
 SA"s58,s40,270336\n" BL"v2,s[72:75],s58 offen lds\n" MF"a[176:179],v[148:151],v[112:115],a[176:179]\n" SA"m0,s63,133120\n" SA"s58,s40,278528\n" BL"v2,s[72:75],s58 offen lds\n"
 MF"a[180:183],v[148:151],v[116:119],a[180:183]\n" SA"m0,s63,134144\n" SA"s58,s40,286720\n" BL"v2,s[72:75],s58 offen lds\n" MF"a[184:187],v[148:151],v[120:123],a[184:187]\n" SA"s59,s41,8192\n"
 BL"v[64:67],v3,s[76:79],s59 offen offset:0 sc1\n" MF"a[188:191],v[148:151],v[124:127],a[188:191]\n" BL"v[68:71],v3,s[76:79],s59 offen offset:1024 sc1\n"
 MF"a[128:131],v[144:147],v[96:99],a[128:131]\n" BL"v[72:75],v3,s[76:79],s59 offen offset:2048 sc1\n" MF"a[132:135],v[144:147],v[100:103],a[132:135]\n"
 BL"v[76:79],v3,s[76:79],s59 offen offset:3072 sc1\n" MF"a[136:139],v[144:147],v[104:107],a[136:139]\n" SA"m0,s63,147456\n" SA"s58,s40,393216\n" BL"v2,s[72:75],s58 offen lds\n"
 MF"a[140:143],v[144:147],v[108:111],a[140:143]\n" SA"m0,s63,148480\n" SA"s58,s40,401408\n" BL"v2,s[72:75],s58 offen lds\n" MF"a[144:147],v[144:147],v[112:115],a[144:147]\n" SA"m0,s63,149504\n"
 SA"s58,s40,409600\n" BL"v2,s[72:75],s58 offen lds\n" MF"a[148:151],v[144:147],v[116:119],a[148:151]\n" SA"m0,s63,150528\n" SA"s58,s40,417792\n" BL"v2,s[72:75],s58 offen lds\n"
 MF"a[152:155],v[144:147],v[120:123],a[152:155]\n" SA"s59,s41,12288\n" BL"v[80:83],v3,s[76:79],s59 offen offset:0 sc1\n" MF"a[156:159],v[144:147],v[124:127],a[156:159]\n"
 BL"v[84:87],v3,s[76:79],s59 offen offset:1024 sc1\n" MF"a[96:99],v[140:143],v[96:99],a[96:99]\n" BL"v[88:91],v3,s[76:79],s59 offen offset:2048 sc1\n" MF"a[100:103],v[140:143],v[100:103],a[100:103]\n"
 BL"v[92:95],v3,s[76:79],s59 offen offset:3072 sc1\n" MF"a[104:107],v[140:143],v[104:107],a[104:107]\n""v_mbcnt_lo_u32_b32 v20,-1,0\n""v_mbcnt_hi_u32_b32 v20,-1,v20\n""v_lshrrev_b32 v21,4,v20\n"
"v_and_b32 v22,15,v20\n""v_add_u32 v23,0,v21\n""v_lshlrev_b32 v23,1,v23\n""v_xor_b32 v23,v23,v22\n""v_lshlrev_b32 v23,4,v23\n""v_lshl_add_u32 v28,v21,13,v23\n""v_add_u32 v23,4,v21\n"
"v_lshlrev_b32 v23,1,v23\n""v_xor_b32 v23,v23,v22\n""v_lshlrev_b32 v23,4,v23\n""v_lshl_add_u32 v29,v21,13,v23\n""s_and_b32 s37,s96,31\n""s_lshr_b32 s47,s96,5\n""s_sub_u32 s55,15,s47\n"
"v_readlane_b32 s16,v254,4\n""v_readlane_b32 s17,v254,5\n""s_mov_b32 s18,4194304\n""s_mov_b32 s19,0x20000\n" SA"s28,s52,20971520\n""s_addc_u32 s29,s53,0\n""s_mov_b32 s30,2097152\n"
"s_mov_b32 s31,0x20000\n""s_lshl_b32 s57,s47,3\n" SA"s68,s57,s36\n" SA"s69,s57,7\n""s_sub_u32 s69,s69,s36\n""s_lshl_b32 s57,s55,3\n" SA"s70,s57,s36\n" SA"s71,s57,7\n""s_sub_u32 s71,s71,s36\n"
"s_lshl_b32 s68,s68,14\n""s_lshl_b32 s69,s69,14\n""s_lshl_b32 s70,s70,14\n""s_lshl_b32 s71,s71,14\n""s_lshl_b32 s57,s37,8\n""s_lshl_b32 s62,s36,18\n" SA"s62,s62,s57\n""s_lshl_b32 s61,s36,13\n"
 SA"s61,s61,32768\n" MF"a[108:111],v[140:143],v[108:111],a[108:111]\n" SA"m0,s61,0\n" SA"s50,s62,0\n" BL"v28,s[16:19],s50 offen lds\n" MF"a[112:115],v[140:143],v[112:115],a[112:115]\n"
 SA"m0,s61,1024\n" SA"s50,s62,32768\n" BL"v29,s[16:19],s50 offen lds\n" MF"a[116:119],v[140:143],v[116:119],a[116:119]\n" SA"m0,s61,2048\n" SA"s50,s62,65536\n" BL"v28,s[16:19],s50 offen lds\n"
 MF"a[120:123],v[140:143],v[120:123],a[120:123]\n" SA"m0,s61,3072\n" SA"s50,s62,98304\n" BL"v29,s[16:19],s50 offen lds\n" MF"a[124:127],v[140:143],v[124:127],a[124:127]\n" SA"m0,s61,4096\n"
 SA"s50,s62,131072\n" BL"v28,s[16:19],s50 offen lds\n" MF"a[64:67],v[136:139],v[96:99],a[64:67]\n" SA"m0,s61,5120\n" SA"s50,s62,163840\n" BL"v29,s[16:19],s50 offen lds\n"
 MF"a[68:71],v[136:139],v[100:103],a[68:71]\n" SA"m0,s61,6144\n" SA"s50,s62,196608\n" BL"v28,s[16:19],s50 offen lds\n" MF"a[72:75],v[136:139],v[104:107],a[72:75]\n" SA"m0,s61,7168\n"
 SA"s50,s62,229376\n" BL"v29,s[16:19],s50 offen lds\n" MF"a[76:79],v[136:139],v[108:111],a[76:79]\n" MF"a[80:83],v[136:139],v[112:115],a[80:83]\n" MF"a[84:87],v[136:139],v[116:119],a[84:87]\n"
 MF"a[88:91],v[136:139],v[120:123],a[88:91]\n" MF"a[92:95],v[136:139],v[124:127],a[92:95]\n" MF"a[32:35],v[132:135],v[96:99],a[32:35]\n" MF"a[36:39],v[132:135],v[100:103],a[36:39]\n"
 MF"a[40:43],v[132:135],v[104:107],a[40:43]\n" MF"a[44:47],v[132:135],v[108:111],a[44:47]\n" MF"a[48:51],v[132:135],v[112:115],a[48:51]\n" MF"a[52:55],v[132:135],v[116:119],a[52:55]\n"
 MF"a[56:59],v[132:135],v[120:123],a[56:59]\n" MF"a[60:63],v[132:135],v[124:127],a[60:63]\n" MF"a[0:3],v[128:131],v[96:99],a[0:3]\n" MF"a[4:7],v[128:131],v[100:103],a[4:7]\n"
 MF"a[8:11],v[128:131],v[104:107],a[8:11]\n" MF"a[12:15],v[128:131],v[108:111],a[12:15]\n" MF"a[16:19],v[128:131],v[112:115],a[16:19]\n" MF"a[20:23],v[128:131],v[116:119],a[20:23]\n"
 MF"a[24:27],v[128:131],v[120:123],a[24:27]\n" MF"a[28:31],v[128:131],v[124:127],a[28:31]\n""s_branch Lkve_%=\n""Lkvv_%=:\n" WV"(26)\n""s_barrier\n" DR"v[32:35],v6 offset:0\n"
 DR"v[36:39],v6 offset:2048\n" DR"v[40:43],v6 offset:4096\n" DR"v[44:47],v6 offset:6144\n" DR"v[48:51],v6 offset:8192\n" DR"v[52:55],v6 offset:10240\n" DR"v[56:59],v6 offset:12288\n"
 DR"v[60:63],v6 offset:14336\n" TR"v[64:65],v10 offset:0\n" TR"v[66:67],v10 offset:1024\n" TR"v[68:69],v11 offset:0\n" TR"v[70:71],v11 offset:1024\n" TR"v[72:73],v12 offset:0\n"
 TR"v[74:75],v12 offset:1024\n" TR"v[76:77],v13 offset:0\n" WL"(8)\n" TR"v[78:79],v13 offset:1024\n" TR"v[80:81],v14 offset:0\n" TR"v[82:83],v14 offset:1024\n" TR"v[84:85],v15 offset:0\n"
 TR"v[86:87],v15 offset:1024\n" TR"v[88:89],v16 offset:0\n" TR"v[90:91],v16 offset:1024\n" WL"(8)\n" TR"v[92:93],v17 offset:0\n" TR"v[94:95],v17 offset:1024\n" MF"a[0:3],v[32:35],v[64:67],0\n"
 WV"(22) lgkmcnt(0)\n""s_barrier\n" MF"a[32:35],v[36:39],v[64:67],0\n" MF"a[8:11],v[40:43],v[64:67],0\n" DR"v[96:99],v7 offset:0\n" MF"a[40:43],v[44:47],v[64:67],0\n"
 MF"a[16:19],v[48:51],v[64:67],0\n" DR"v[100:103],v7 offset:2048\n" MF"a[48:51],v[52:55],v[64:67],0\n" SA"m0,s43,49152\n" SA"s50,s41,786432\n" BL"v4,s[16:19],s50 offen lds\n"
 MF"a[24:27],v[56:59],v[64:67],0\n" DR"v[104:107],v7 offset:4096\n" MF"a[56:59],v[60:63],v[64:67],0\n" MF"a[64:67],v[32:35],v[68:71],0\n" DR"v[108:111],v7 offset:6144\n"
 MF"a[96:99],v[36:39],v[68:71],0\n" MF"a[72:75],v[40:43],v[68:71],0\n" DR"v[112:115],v7 offset:8192\n" MF"a[104:107],v[44:47],v[68:71],0\n" MF"a[80:83],v[48:51],v[68:71],0\n"
 DR"v[116:119],v7 offset:10240\n" SA"m0,s43,50176\n" SA"s50,s41,819200\n" BL"v4,s[16:19],s50 offen lds\n" MF"a[112:115],v[52:55],v[68:71],0\n" MF"a[88:91],v[56:59],v[68:71],0\n"
 DR"v[120:123],v7 offset:12288\n" MF"a[120:123],v[60:63],v[68:71],0\n" MF"a[128:131],v[32:35],v[72:75],0\n" DR"v[124:127],v7 offset:14336\n" MF"a[160:163],v[36:39],v[72:75],0\n"
 MF"a[136:139],v[40:43],v[72:75],0\n" MF"a[168:171],v[44:47],v[72:75],0\n" TR"v[128:129],v10 offset:16384\n" TR"v[130:131],v10 offset:17408\n" SA"m0,s43,51200\n" SA"s50,s41,851968\n"
 BL"v5,s[16:19],s50 offen lds\n" MF"a[144:147],v[48:51],v[72:75],0\n" MF"a[176:179],v[52:55],v[72:75],0\n" MF"a[152:155],v[56:59],v[72:75],0\n" MF"a[184:187],v[60:63],v[72:75],0\n"
 MF"a[192:195],v[32:35],v[76:79],0\n" TR"v[132:133],v11 offset:16384\n" TR"v[134:135],v11 offset:17408\n" MF"a[224:227],v[36:39],v[76:79],0\n" MF"a[200:203],v[40:43],v[76:79],0\n" SA"m0,s43,52224\n"
 SA"s50,s41,884736\n" BL"v5,s[16:19],s50 offen lds\n" MF"a[232:235],v[44:47],v[76:79],0\n" MF"a[208:211],v[48:51],v[76:79],0\n" MF"a[240:243],v[52:55],v[76:79],0\n" TR"v[136:137],v12 offset:16384\n"
 TR"v[138:139],v12 offset:17408\n" MF"a[216:219],v[56:59],v[76:79],0\n" MF"a[248:251],v[60:63],v[76:79],0\n" MF"a[4:7],v[32:35],v[80:83],0\n" MF"a[36:39],v[36:39],v[80:83],0\n" SA"m0,s42,131072\n"
 SA"s50,s40,256\n" BL"v2,s[20:23],s50 offen lds\n" MF"a[12:15],v[40:43],v[80:83],0\n" TR"v[140:141],v13 offset:16384\n" WL"(8)\n" TR"v[142:143],v13 offset:17408\n" MF"a[44:47],v[44:47],v[80:83],0\n"
 MF"a[20:23],v[48:51],v[80:83],0\n" MF"a[52:55],v[52:55],v[80:83],0\n" MF"a[28:31],v[56:59],v[80:83],0\n" MF"a[60:63],v[60:63],v[80:83],0\n" TR"v[144:145],v14 offset:16384\n"
 TR"v[146:147],v14 offset:17408\n" MF"a[68:71],v[32:35],v[84:87],0\n" SA"m0,s42,132096\n" SA"s50,s40,8448\n" BL"v3,s[20:23],s50 offen lds\n" MF"a[100:103],v[36:39],v[84:87],0\n"
 MF"a[76:79],v[40:43],v[84:87],0\n" MF"a[108:111],v[44:47],v[84:87],0\n" MF"a[84:87],v[48:51],v[84:87],0\n" TR"v[148:149],v15 offset:16384\n" TR"v[150:151],v15 offset:17408\n"
 MF"a[116:119],v[52:55],v[84:87],0\n" MF"a[92:95],v[56:59],v[84:87],0\n" MF"a[124:127],v[60:63],v[84:87],0\n" SA"m0,s42,133120\n" SA"s50,s40,16640\n" BL"v2,s[20:23],s50 offen lds\n"
 MF"a[132:135],v[32:35],v[88:91],0\n" MF"a[164:167],v[36:39],v[88:91],0\n" TR"v[152:153],v16 offset:16384\n" TR"v[154:155],v16 offset:17408\n" MF"a[140:143],v[40:43],v[88:91],0\n"
 MF"a[172:175],v[44:47],v[88:91],0\n" MF"a[148:151],v[48:51],v[88:91],0\n" MF"a[180:183],v[52:55],v[88:91],0\n" MF"a[156:159],v[56:59],v[88:91],0\n" WL"(8)\n" TR"v[156:157],v17 offset:16384\n"
 TR"v[158:159],v17 offset:17408\n" SA"m0,s42,134144\n" SA"s50,s40,24832\n" BL"v3,s[20:23],s50 offen lds\n" MF"a[188:191],v[60:63],v[88:91],0\n" MF"a[196:199],v[32:35],v[92:95],0\n"
 MF"a[228:231],v[36:39],v[92:95],0\n" MF"a[204:207],v[40:43],v[92:95],0\n" MF"a[236:239],v[44:47],v[92:95],0\n" MF"a[212:215],v[48:51],v[92:95],0\n" MF"a[244:247],v[52:55],v[92:95],0\n"
 MF"a[220:223],v[56:59],v[92:95],0\n" MF"a[252:255],v[60:63],v[92:95],0\n" MF"a[0:3],v[96:99],v[128:131],a[0:3]\n" WV"(18) lgkmcnt(0)\n""s_barrier\n" MF"a[32:35],v[100:103],v[128:131],a[32:35]\n"
 MF"a[8:11],v[104:107],v[128:131],a[8:11]\n" DR"v[32:35],v6 offset:32768\n" MF"a[40:43],v[108:111],v[128:131],a[40:43]\n" MF"a[16:19],v[112:115],v[128:131],a[16:19]\n" DR"v[36:39],v6 offset:34816\n"
 MF"a[48:51],v[116:119],v[128:131],a[48:51]\n" SA"m0,s43,0\n" SA"s50,s41,1048576\n" BL"v4,s[16:19],s50 offen lds\n" MF"a[24:27],v[120:123],v[128:131],a[24:27]\n" DR"v[40:43],v6 offset:36864\n"
 MF"a[56:59],v[124:127],v[128:131],a[56:59]\n" MF"a[64:67],v[96:99],v[132:135],a[64:67]\n" DR"v[44:47],v6 offset:38912\n" MF"a[96:99],v[100:103],v[132:135],a[96:99]\n"
 MF"a[72:75],v[104:107],v[132:135],a[72:75]\n" DR"v[48:51],v6 offset:40960\n" MF"a[104:107],v[108:111],v[132:135],a[104:107]\n" MF"a[80:83],v[112:115],v[132:135],a[80:83]\n"
 DR"v[52:55],v6 offset:43008\n" SA"m0,s43,1024\n" SA"s50,s41,1081344\n" BL"v4,s[16:19],s50 offen lds\n" MF"a[112:115],v[116:119],v[132:135],a[112:115]\n" MF"a[88:91],v[120:123],v[132:135],a[88:91]\n"
 DR"v[56:59],v6 offset:45056\n" MF"a[120:123],v[124:127],v[132:135],a[120:123]\n" MF"a[128:131],v[96:99],v[136:139],a[128:131]\n" DR"v[60:63],v6 offset:47104\n"
 MF"a[160:163],v[100:103],v[136:139],a[160:163]\n" MF"a[136:139],v[104:107],v[136:139],a[136:139]\n" MF"a[168:171],v[108:111],v[136:139],a[168:171]\n" TR"v[64:65],v10 offset:32768\n"
 TR"v[66:67],v10 offset:33792\n" SA"m0,s43,2048\n" SA"s50,s41,1114112\n" BL"v5,s[16:19],s50 offen lds\n" MF"a[144:147],v[112:115],v[136:139],a[144:147]\n"
 MF"a[176:179],v[116:119],v[136:139],a[176:179]\n" MF"a[152:155],v[120:123],v[136:139],a[152:155]\n" MF"a[184:187],v[124:127],v[136:139],a[184:187]\n" MF"a[192:195],v[96:99],v[140:143],a[192:195]\n"
 TR"v[68:69],v11 offset:32768\n" TR"v[70:71],v11 offset:33792\n" MF"a[224:227],v[100:103],v[140:143],a[224:227]\n" MF"a[200:203],v[104:107],v[140:143],a[200:203]\n" SA"m0,s43,3072\n"
 SA"s50,s41,1146880\n" BL"v5,s[16:19],s50 offen lds\n" MF"a[232:235],v[108:111],v[140:143],a[232:235]\n" MF"a[208:211],v[112:115],v[140:143],a[208:211]\n"
 MF"a[240:243],v[116:119],v[140:143],a[240:243]\n" TR"v[72:73],v12 offset:32768\n" TR"v[74:75],v12 offset:33792\n" MF"a[216:219],v[120:123],v[140:143],a[216:219]\n"
 MF"a[248:251],v[124:127],v[140:143],a[248:251]\n" MF"a[4:7],v[96:99],v[144:147],a[4:7]\n" MF"a[36:39],v[100:103],v[144:147],a[36:39]\n" SA"m0,s42,135168\n" SA"s50,s40,33024\n"
 BL"v2,s[20:23],s50 offen lds\n" MF"a[12:15],v[104:107],v[144:147],a[12:15]\n" TR"v[76:77],v13 offset:32768\n" WL"(8)\n" TR"v[78:79],v13 offset:33792\n" MF"a[44:47],v[108:111],v[144:147],a[44:47]\n"
 MF"a[20:23],v[112:115],v[144:147],a[20:23]\n" MF"a[52:55],v[116:119],v[144:147],a[52:55]\n" MF"a[28:31],v[120:123],v[144:147],a[28:31]\n" MF"a[60:63],v[124:127],v[144:147],a[60:63]\n"
 TR"v[80:81],v14 offset:32768\n" TR"v[82:83],v14 offset:33792\n" MF"a[68:71],v[96:99],v[148:151],a[68:71]\n" SA"m0,s42,136192\n" SA"s50,s40,41216\n" BL"v3,s[20:23],s50 offen lds\n"
 MF"a[100:103],v[100:103],v[148:151],a[100:103]\n" MF"a[76:79],v[104:107],v[148:151],a[76:79]\n" MF"a[108:111],v[108:111],v[148:151],a[108:111]\n" MF"a[84:87],v[112:115],v[148:151],a[84:87]\n"
 TR"v[84:85],v15 offset:32768\n" TR"v[86:87],v15 offset:33792\n" MF"a[116:119],v[116:119],v[148:151],a[116:119]\n" MF"a[92:95],v[120:123],v[148:151],a[92:95]\n"
 MF"a[124:127],v[124:127],v[148:151],a[124:127]\n" SA"m0,s42,137216\n" SA"s50,s40,49408\n" BL"v2,s[20:23],s50 offen lds\n" MF"a[132:135],v[96:99],v[152:155],a[132:135]\n"
 MF"a[164:167],v[100:103],v[152:155],a[164:167]\n" TR"v[88:89],v16 offset:32768\n" TR"v[90:91],v16 offset:33792\n" MF"a[140:143],v[104:107],v[152:155],a[140:143]\n"
 MF"a[172:175],v[108:111],v[152:155],a[172:175]\n" MF"a[148:151],v[112:115],v[152:155],a[148:151]\n" MF"a[180:183],v[116:119],v[152:155],a[180:183]\n" MF"a[156:159],v[120:123],v[152:155],a[156:159]\n"
 WL"(8)\n" TR"v[92:93],v17 offset:32768\n" TR"v[94:95],v17 offset:33792\n" SA"m0,s42,138240\n" SA"s50,s40,57600\n" BL"v3,s[20:23],s50 offen lds\n" MF"a[188:191],v[124:127],v[152:155],a[188:191]\n"
 MF"a[196:199],v[96:99],v[156:159],a[196:199]\n" MF"a[228:231],v[100:103],v[156:159],a[228:231]\n" MF"a[204:207],v[104:107],v[156:159],a[204:207]\n" MF"a[236:239],v[108:111],v[156:159],a[236:239]\n"
 MF"a[212:215],v[112:115],v[156:159],a[212:215]\n" MF"a[244:247],v[116:119],v[156:159],a[244:247]\n" MF"a[220:223],v[120:123],v[156:159],a[220:223]\n" MF"a[252:255],v[124:127],v[156:159],a[252:255]\n"
 MF"a[0:3],v[32:35],v[64:67],a[0:3]\n" WV"(12) lgkmcnt(0)\n""s_barrier\n" MF"a[32:35],v[36:39],v[64:67],a[32:35]\n" MF"a[8:11],v[40:43],v[64:67],a[8:11]\n" DR"v[96:99],v7 offset:32768\n"
 MF"a[40:43],v[44:47],v[64:67],a[40:43]\n" MF"a[16:19],v[48:51],v[64:67],a[16:19]\n" DR"v[100:103],v7 offset:34816\n" MF"a[48:51],v[52:55],v[64:67],a[48:51]\n" SA"m0,s43,16384\n" SA"s50,s41,1310720\n"
 BL"v4,s[16:19],s50 offen lds\n" MF"a[24:27],v[56:59],v[64:67],a[24:27]\n" DR"v[104:107],v7 offset:36864\n" MF"a[56:59],v[60:63],v[64:67],a[56:59]\n" MF"a[64:67],v[32:35],v[68:71],a[64:67]\n"
 DR"v[108:111],v7 offset:38912\n" MF"a[96:99],v[36:39],v[68:71],a[96:99]\n" MF"a[72:75],v[40:43],v[68:71],a[72:75]\n" DR"v[112:115],v7 offset:40960\n" MF"a[104:107],v[44:47],v[68:71],a[104:107]\n"
 MF"a[80:83],v[48:51],v[68:71],a[80:83]\n" DR"v[116:119],v7 offset:43008\n" SA"m0,s43,17408\n" SA"s50,s41,1343488\n" BL"v4,s[16:19],s50 offen lds\n" MF"a[112:115],v[52:55],v[68:71],a[112:115]\n"
 MF"a[88:91],v[56:59],v[68:71],a[88:91]\n" DR"v[120:123],v7 offset:45056\n" MF"a[120:123],v[60:63],v[68:71],a[120:123]\n" MF"a[128:131],v[32:35],v[72:75],a[128:131]\n" DR"v[124:127],v7 offset:47104\n"
 MF"a[160:163],v[36:39],v[72:75],a[160:163]\n" MF"a[136:139],v[40:43],v[72:75],a[136:139]\n" MF"a[168:171],v[44:47],v[72:75],a[168:171]\n" TR"v[128:129],v10 offset:49152\n"
 TR"v[130:131],v10 offset:50176\n" SA"m0,s43,18432\n" SA"s50,s41,1376256\n" BL"v5,s[16:19],s50 offen lds\n" MF"a[144:147],v[48:51],v[72:75],a[144:147]\n" MF"a[176:179],v[52:55],v[72:75],a[176:179]\n"
 MF"a[152:155],v[56:59],v[72:75],a[152:155]\n" MF"a[184:187],v[60:63],v[72:75],a[184:187]\n" MF"a[192:195],v[32:35],v[76:79],a[192:195]\n" TR"v[132:133],v11 offset:49152\n"
 TR"v[134:135],v11 offset:50176\n" MF"a[224:227],v[36:39],v[76:79],a[224:227]\n" MF"a[200:203],v[40:43],v[76:79],a[200:203]\n" SA"m0,s43,19456\n" SA"s50,s41,1409024\n" BL"v5,s[16:19],s50 offen lds\n"
 MF"a[232:235],v[44:47],v[76:79],a[232:235]\n" MF"a[208:211],v[48:51],v[76:79],a[208:211]\n" MF"a[240:243],v[52:55],v[76:79],a[240:243]\n" TR"v[136:137],v12 offset:49152\n"
 TR"v[138:139],v12 offset:50176\n" MF"a[216:219],v[56:59],v[76:79],a[216:219]\n" MF"a[248:251],v[60:63],v[76:79],a[248:251]\n" MF"a[4:7],v[32:35],v[80:83],a[4:7]\n"
 MF"a[36:39],v[36:39],v[80:83],a[36:39]\n" SA"m0,s42,65536\n" SA"s50,s40,384\n" BL"v2,s[20:23],s50 offen lds\n" MF"a[12:15],v[40:43],v[80:83],a[12:15]\n" TR"v[140:141],v13 offset:49152\n" WL"(8)\n"
 TR"v[142:143],v13 offset:50176\n" MF"a[44:47],v[44:47],v[80:83],a[44:47]\n" MF"a[20:23],v[48:51],v[80:83],a[20:23]\n" MF"a[52:55],v[52:55],v[80:83],a[52:55]\n"
 MF"a[28:31],v[56:59],v[80:83],a[28:31]\n" MF"a[60:63],v[60:63],v[80:83],a[60:63]\n" TR"v[144:145],v14 offset:49152\n" TR"v[146:147],v14 offset:50176\n" MF"a[68:71],v[32:35],v[84:87],a[68:71]\n"
 SA"m0,s42,66560\n" SA"s50,s40,8576\n" BL"v3,s[20:23],s50 offen lds\n" MF"a[100:103],v[36:39],v[84:87],a[100:103]\n" MF"a[76:79],v[40:43],v[84:87],a[76:79]\n"
 MF"a[108:111],v[44:47],v[84:87],a[108:111]\n" MF"a[84:87],v[48:51],v[84:87],a[84:87]\n" TR"v[148:149],v15 offset:49152\n" TR"v[150:151],v15 offset:50176\n"
 MF"a[116:119],v[52:55],v[84:87],a[116:119]\n" MF"a[92:95],v[56:59],v[84:87],a[92:95]\n" MF"a[124:127],v[60:63],v[84:87],a[124:127]\n" SA"m0,s42,67584\n" SA"s50,s40,16768\n"
 BL"v2,s[20:23],s50 offen lds\n" MF"a[132:135],v[32:35],v[88:91],a[132:135]\n" MF"a[164:167],v[36:39],v[88:91],a[164:167]\n" TR"v[152:153],v16 offset:49152\n" TR"v[154:155],v16 offset:50176\n"
 MF"a[140:143],v[40:43],v[88:91],a[140:143]\n" MF"a[172:175],v[44:47],v[88:91],a[172:175]\n" MF"a[148:151],v[48:51],v[88:91],a[148:151]\n" MF"a[180:183],v[52:55],v[88:91],a[180:183]\n"
 MF"a[156:159],v[56:59],v[88:91],a[156:159]\n" WL"(8)\n" TR"v[156:157],v17 offset:49152\n" TR"v[158:159],v17 offset:50176\n" SA"m0,s42,68608\n" SA"s50,s40,24960\n" BL"v3,s[20:23],s50 offen lds\n"
 MF"a[188:191],v[60:63],v[88:91],a[188:191]\n" MF"a[196:199],v[32:35],v[92:95],a[196:199]\n" MF"a[228:231],v[36:39],v[92:95],a[228:231]\n" MF"a[204:207],v[40:43],v[92:95],a[204:207]\n"
 MF"a[236:239],v[44:47],v[92:95],a[236:239]\n" MF"a[212:215],v[48:51],v[92:95],a[212:215]\n" MF"a[244:247],v[52:55],v[92:95],a[244:247]\n" MF"a[220:223],v[56:59],v[92:95],a[220:223]\n"
 MF"a[252:255],v[60:63],v[92:95],a[252:255]\n" MF"a[0:3],v[96:99],v[128:131],a[0:3]\n" WV"(8) lgkmcnt(0)\n""s_barrier\n""s_lshl_b32 s57,s96,3\n" SA"s57,s57,4096\n" VV"v20,s98\n" VV"v21,s99\n"
 VV"v22,0\n""buffer_store_dwordx2 v[20:21],v22,s[92:95],s57 offen sc1\n" MF"a[32:35],v[100:103],v[128:131],a[32:35]\n" MF"a[8:11],v[104:107],v[128:131],a[8:11]\n" DR"v[32:35],v8 offset:0\n"
 MF"a[40:43],v[108:111],v[128:131],a[40:43]\n" MF"a[16:19],v[112:115],v[128:131],a[16:19]\n" DR"v[36:39],v8 offset:2048\n" MF"a[48:51],v[116:119],v[128:131],a[48:51]\n" SA"m0,s43,32768\n"
 SA"s50,s41,1572864\n" BL"v4,s[16:19],s50 offen lds\n" MF"a[24:27],v[120:123],v[128:131],a[24:27]\n" DR"v[40:43],v8 offset:4096\n" MF"a[56:59],v[124:127],v[128:131],a[56:59]\n"
 MF"a[64:67],v[96:99],v[132:135],a[64:67]\n" DR"v[44:47],v8 offset:6144\n" MF"a[96:99],v[100:103],v[132:135],a[96:99]\n" MF"a[72:75],v[104:107],v[132:135],a[72:75]\n" DR"v[48:51],v8 offset:8192\n"
 MF"a[104:107],v[108:111],v[132:135],a[104:107]\n" MF"a[80:83],v[112:115],v[132:135],a[80:83]\n" DR"v[52:55],v8 offset:10240\n" SA"m0,s43,33792\n" SA"s50,s41,1605632\n" BL"v4,s[16:19],s50 offen lds\n"
 MF"a[112:115],v[116:119],v[132:135],a[112:115]\n" MF"a[88:91],v[120:123],v[132:135],a[88:91]\n" DR"v[56:59],v8 offset:12288\n" MF"a[120:123],v[124:127],v[132:135],a[120:123]\n"
 MF"a[128:131],v[96:99],v[136:139],a[128:131]\n" DR"v[60:63],v8 offset:14336\n" MF"a[160:163],v[100:103],v[136:139],a[160:163]\n" MF"a[136:139],v[104:107],v[136:139],a[136:139]\n"
 MF"a[168:171],v[108:111],v[136:139],a[168:171]\n" TR"v[64:65],v10 offset:0\n" TR"v[66:67],v10 offset:1024\n" SA"m0,s43,34816\n" SA"s50,s41,1638400\n" BL"v5,s[16:19],s50 offen lds\n"
 MF"a[144:147],v[112:115],v[136:139],a[144:147]\n" MF"a[176:179],v[116:119],v[136:139],a[176:179]\n" MF"a[152:155],v[120:123],v[136:139],a[152:155]\n" MF"a[184:187],v[124:127],v[136:139],a[184:187]\n"
 MF"a[192:195],v[96:99],v[140:143],a[192:195]\n" TR"v[68:69],v11 offset:0\n" TR"v[70:71],v11 offset:1024\n" MF"a[224:227],v[100:103],v[140:143],a[224:227]\n"
 MF"a[200:203],v[104:107],v[140:143],a[200:203]\n" SA"m0,s43,35840\n" SA"s50,s41,1671168\n" BL"v5,s[16:19],s50 offen lds\n" MF"a[232:235],v[108:111],v[140:143],a[232:235]\n"
 MF"a[208:211],v[112:115],v[140:143],a[208:211]\n" MF"a[240:243],v[116:119],v[140:143],a[240:243]\n" TR"v[72:73],v12 offset:0\n" TR"v[74:75],v12 offset:1024\n"
 MF"a[216:219],v[120:123],v[140:143],a[216:219]\n" MF"a[248:251],v[124:127],v[140:143],a[248:251]\n" MF"a[4:7],v[96:99],v[144:147],a[4:7]\n" MF"a[36:39],v[100:103],v[144:147],a[36:39]\n"
 SA"m0,s42,69632\n" SA"s50,s40,33152\n" BL"v2,s[20:23],s50 offen lds\n" MF"a[12:15],v[104:107],v[144:147],a[12:15]\n" TR"v[76:77],v13 offset:0\n" WL"(8)\n" TR"v[78:79],v13 offset:1024\n"
 MF"a[44:47],v[108:111],v[144:147],a[44:47]\n" MF"a[20:23],v[112:115],v[144:147],a[20:23]\n" MF"a[52:55],v[116:119],v[144:147],a[52:55]\n" MF"a[28:31],v[120:123],v[144:147],a[28:31]\n"
 MF"a[60:63],v[124:127],v[144:147],a[60:63]\n" TR"v[80:81],v14 offset:0\n" TR"v[82:83],v14 offset:1024\n" MF"a[68:71],v[96:99],v[148:151],a[68:71]\n" SA"m0,s42,70656\n" SA"s50,s40,41344\n"
 BL"v3,s[20:23],s50 offen lds\n" MF"a[100:103],v[100:103],v[148:151],a[100:103]\n" MF"a[76:79],v[104:107],v[148:151],a[76:79]\n" MF"a[108:111],v[108:111],v[148:151],a[108:111]\n"
 MF"a[84:87],v[112:115],v[148:151],a[84:87]\n" TR"v[84:85],v15 offset:0\n" TR"v[86:87],v15 offset:1024\n" MF"a[116:119],v[116:119],v[148:151],a[116:119]\n"
 MF"a[92:95],v[120:123],v[148:151],a[92:95]\n" MF"a[124:127],v[124:127],v[148:151],a[124:127]\n" SA"m0,s42,71680\n" SA"s50,s40,49536\n" BL"v2,s[20:23],s50 offen lds\n"
 MF"a[132:135],v[96:99],v[152:155],a[132:135]\n" MF"a[164:167],v[100:103],v[152:155],a[164:167]\n" TR"v[88:89],v16 offset:0\n" TR"v[90:91],v16 offset:1024\n"
 MF"a[140:143],v[104:107],v[152:155],a[140:143]\n" MF"a[172:175],v[108:111],v[152:155],a[172:175]\n" MF"a[148:151],v[112:115],v[152:155],a[148:151]\n" MF"a[180:183],v[116:119],v[152:155],a[180:183]\n"
 MF"a[156:159],v[120:123],v[152:155],a[156:159]\n" WL"(8)\n" TR"v[92:93],v17 offset:0\n" TR"v[94:95],v17 offset:1024\n" SA"m0,s42,72704\n" SA"s50,s40,57728\n" BL"v3,s[20:23],s50 offen lds\n"
 MF"a[188:191],v[124:127],v[152:155],a[188:191]\n" MF"a[196:199],v[96:99],v[156:159],a[196:199]\n" MF"a[228:231],v[100:103],v[156:159],a[228:231]\n" MF"a[204:207],v[104:107],v[156:159],a[204:207]\n"
 MF"a[236:239],v[108:111],v[156:159],a[236:239]\n" MF"a[212:215],v[112:115],v[156:159],a[212:215]\n" MF"a[244:247],v[116:119],v[156:159],a[244:247]\n" MF"a[220:223],v[120:123],v[156:159],a[220:223]\n"
 MF"a[252:255],v[124:127],v[156:159],a[252:255]\n" MF"a[0:3],v[32:35],v[64:67],a[0:3]\n" WV"(13) lgkmcnt(0)\n""s_barrier\n" MF"a[32:35],v[36:39],v[64:67],a[32:35]\n"
 MF"a[8:11],v[40:43],v[64:67],a[8:11]\n" DR"v[96:99],v9 offset:0\n" MF"a[40:43],v[44:47],v[64:67],a[40:43]\n" MF"a[16:19],v[48:51],v[64:67],a[16:19]\n" DR"v[100:103],v9 offset:2048\n"
 MF"a[48:51],v[52:55],v[64:67],a[48:51]\n" SA"m0,s43,49152\n" SA"s50,s41,1835008\n" BL"v4,s[16:19],s50 offen lds\n" MF"a[24:27],v[56:59],v[64:67],a[24:27]\n" DR"v[104:107],v9 offset:4096\n"
 MF"a[56:59],v[60:63],v[64:67],a[56:59]\n" MF"a[64:67],v[32:35],v[68:71],a[64:67]\n" DR"v[108:111],v9 offset:6144\n" MF"a[96:99],v[36:39],v[68:71],a[96:99]\n" MF"a[72:75],v[40:43],v[68:71],a[72:75]\n"
 DR"v[112:115],v9 offset:8192\n" MF"a[104:107],v[44:47],v[68:71],a[104:107]\n" MF"a[80:83],v[48:51],v[68:71],a[80:83]\n" DR"v[116:119],v9 offset:10240\n" SA"m0,s43,50176\n" SA"s50,s41,1867776\n"
 BL"v4,s[16:19],s50 offen lds\n" MF"a[112:115],v[52:55],v[68:71],a[112:115]\n" MF"a[88:91],v[56:59],v[68:71],a[88:91]\n" DR"v[120:123],v9 offset:12288\n" MF"a[120:123],v[60:63],v[68:71],a[120:123]\n"
 MF"a[128:131],v[32:35],v[72:75],a[128:131]\n" DR"v[124:127],v9 offset:14336\n" MF"a[160:163],v[36:39],v[72:75],a[160:163]\n" MF"a[136:139],v[40:43],v[72:75],a[136:139]\n"
 MF"a[168:171],v[44:47],v[72:75],a[168:171]\n" TR"v[128:129],v10 offset:16384\n" TR"v[130:131],v10 offset:17408\n" SA"m0,s43,51200\n" SA"s50,s41,1900544\n" BL"v5,s[16:19],s50 offen lds\n"
 MF"a[144:147],v[48:51],v[72:75],a[144:147]\n" MF"a[176:179],v[52:55],v[72:75],a[176:179]\n" MF"a[152:155],v[56:59],v[72:75],a[152:155]\n" MF"a[184:187],v[60:63],v[72:75],a[184:187]\n"
 MF"a[192:195],v[32:35],v[76:79],a[192:195]\n" TR"v[132:133],v11 offset:16384\n" TR"v[134:135],v11 offset:17408\n" MF"a[224:227],v[36:39],v[76:79],a[224:227]\n"
 MF"a[200:203],v[40:43],v[76:79],a[200:203]\n" SA"m0,s43,52224\n" SA"s50,s41,1933312\n" BL"v5,s[16:19],s50 offen lds\n" MF"a[232:235],v[44:47],v[76:79],a[232:235]\n"
 MF"a[208:211],v[48:51],v[76:79],a[208:211]\n" MF"a[240:243],v[52:55],v[76:79],a[240:243]\n" TR"v[136:137],v12 offset:16384\n" TR"v[138:139],v12 offset:17408\n"
 MF"a[216:219],v[56:59],v[76:79],a[216:219]\n" MF"a[248:251],v[60:63],v[76:79],a[248:251]\n" MF"a[4:7],v[32:35],v[80:83],a[4:7]\n" MF"a[36:39],v[36:39],v[80:83],a[36:39]\n" SA"m0,s42,98304\n"
 SA"s50,s40,512\n" BL"v2,s[20:23],s50 offen lds\n" MF"a[12:15],v[40:43],v[80:83],a[12:15]\n" TR"v[140:141],v13 offset:16384\n" WL"(8)\n" TR"v[142:143],v13 offset:17408\n"
 MF"a[44:47],v[44:47],v[80:83],a[44:47]\n" MF"a[20:23],v[48:51],v[80:83],a[20:23]\n" MF"a[52:55],v[52:55],v[80:83],a[52:55]\n" MF"a[28:31],v[56:59],v[80:83],a[28:31]\n"
 MF"a[60:63],v[60:63],v[80:83],a[60:63]\n" TR"v[144:145],v14 offset:16384\n" TR"v[146:147],v14 offset:17408\n" MF"a[68:71],v[32:35],v[84:87],a[68:71]\n" SA"m0,s42,99328\n" SA"s50,s40,8704\n"
 BL"v3,s[20:23],s50 offen lds\n" MF"a[100:103],v[36:39],v[84:87],a[100:103]\n" MF"a[76:79],v[40:43],v[84:87],a[76:79]\n" MF"a[108:111],v[44:47],v[84:87],a[108:111]\n"
 MF"a[84:87],v[48:51],v[84:87],a[84:87]\n" TR"v[148:149],v15 offset:16384\n" TR"v[150:151],v15 offset:17408\n" MF"a[116:119],v[52:55],v[84:87],a[116:119]\n" MF"a[92:95],v[56:59],v[84:87],a[92:95]\n"
 MF"a[124:127],v[60:63],v[84:87],a[124:127]\n" SA"m0,s42,100352\n" SA"s50,s40,16896\n" BL"v2,s[20:23],s50 offen lds\n" MF"a[132:135],v[32:35],v[88:91],a[132:135]\n"
 MF"a[164:167],v[36:39],v[88:91],a[164:167]\n" TR"v[152:153],v16 offset:16384\n" TR"v[154:155],v16 offset:17408\n" MF"a[140:143],v[40:43],v[88:91],a[140:143]\n"
 MF"a[172:175],v[44:47],v[88:91],a[172:175]\n" MF"a[148:151],v[48:51],v[88:91],a[148:151]\n" MF"a[180:183],v[52:55],v[88:91],a[180:183]\n" MF"a[156:159],v[56:59],v[88:91],a[156:159]\n" WL"(8)\n"
 TR"v[156:157],v17 offset:16384\n" TR"v[158:159],v17 offset:17408\n" SA"m0,s42,101376\n" SA"s50,s40,25088\n" BL"v3,s[20:23],s50 offen lds\n" MF"a[188:191],v[60:63],v[88:91],a[188:191]\n"
 MF"a[196:199],v[32:35],v[92:95],a[196:199]\n" MF"a[228:231],v[36:39],v[92:95],a[228:231]\n" MF"a[204:207],v[40:43],v[92:95],a[204:207]\n" MF"a[236:239],v[44:47],v[92:95],a[236:239]\n"
 MF"a[212:215],v[48:51],v[92:95],a[212:215]\n" MF"a[244:247],v[52:55],v[92:95],a[244:247]\n" MF"a[220:223],v[56:59],v[92:95],a[220:223]\n" MF"a[252:255],v[60:63],v[92:95],a[252:255]\n"
 MF"a[0:3],v[96:99],v[128:131],a[0:3]\n" WV"(8) lgkmcnt(0)\n""s_barrier\n""v_readlane_b32 s72,v254,4\n""v_readlane_b32 s73,v254,5\n""s_mov_b32 s74,4194304\n""s_mov_b32 s75,0x20000\n"
"v_mbcnt_lo_u32_b32 v28,-1,0\n""v_mbcnt_hi_u32_b32 v28,-1,v28\n""v_lshlrev_b32 v28,13,v28\n""s_lshr_b32 s57,s96,5\n""s_lshl_b32 s57,s57,19\n""s_and_b32 s58,s96,31\n""s_lshl_b32 s58,s58,8\n"
 SA"s57,s57,s58\n""s_and_b32 s58,s36,1\n""s_lshl_b32 s58,s58,7\n" SA"s57,s57,s58\n""buffer_load_dword v29,v28,s[72:75],s57 offen\n" MF"a[32:35],v[100:103],v[128:131],a[32:35]\n"
 MF"a[8:11],v[104:107],v[128:131],a[8:11]\n" DR"v[32:35],v6 offset:0\n" MF"a[40:43],v[108:111],v[128:131],a[40:43]\n" MF"a[16:19],v[112:115],v[128:131],a[16:19]\n" DR"v[36:39],v6 offset:2048\n"
 MF"a[48:51],v[116:119],v[128:131],a[48:51]\n" SA"m0,s43,0\n" SA"s50,s41,2097152\n" BL"v4,s[16:19],s50 offen lds\n" MF"a[24:27],v[120:123],v[128:131],a[24:27]\n" DR"v[40:43],v6 offset:4096\n"
 MF"a[56:59],v[124:127],v[128:131],a[56:59]\n" MF"a[64:67],v[96:99],v[132:135],a[64:67]\n" DR"v[44:47],v6 offset:6144\n" MF"a[96:99],v[100:103],v[132:135],a[96:99]\n"
 MF"a[72:75],v[104:107],v[132:135],a[72:75]\n" DR"v[48:51],v6 offset:8192\n" MF"a[104:107],v[108:111],v[132:135],a[104:107]\n" MF"a[80:83],v[112:115],v[132:135],a[80:83]\n"
 DR"v[52:55],v6 offset:10240\n" SA"m0,s43,1024\n" SA"s50,s41,2129920\n" BL"v4,s[16:19],s50 offen lds\n" MF"a[112:115],v[116:119],v[132:135],a[112:115]\n" MF"a[88:91],v[120:123],v[132:135],a[88:91]\n"
 DR"v[56:59],v6 offset:12288\n" MF"a[120:123],v[124:127],v[132:135],a[120:123]\n" MF"a[128:131],v[96:99],v[136:139],a[128:131]\n" DR"v[60:63],v6 offset:14336\n"
 MF"a[160:163],v[100:103],v[136:139],a[160:163]\n" MF"a[136:139],v[104:107],v[136:139],a[136:139]\n" MF"a[168:171],v[108:111],v[136:139],a[168:171]\n" TR"v[64:65],v10 offset:32768\n"
 TR"v[66:67],v10 offset:33792\n" SA"m0,s43,2048\n" SA"s50,s41,2162688\n" BL"v5,s[16:19],s50 offen lds\n" MF"a[144:147],v[112:115],v[136:139],a[144:147]\n"
 MF"a[176:179],v[116:119],v[136:139],a[176:179]\n" MF"a[152:155],v[120:123],v[136:139],a[152:155]\n" MF"a[184:187],v[124:127],v[136:139],a[184:187]\n" MF"a[192:195],v[96:99],v[140:143],a[192:195]\n"
 TR"v[68:69],v11 offset:32768\n" TR"v[70:71],v11 offset:33792\n" MF"a[224:227],v[100:103],v[140:143],a[224:227]\n" MF"a[200:203],v[104:107],v[140:143],a[200:203]\n" SA"m0,s43,3072\n"
 SA"s50,s41,2195456\n" BL"v5,s[16:19],s50 offen lds\n" MF"a[232:235],v[108:111],v[140:143],a[232:235]\n" MF"a[208:211],v[112:115],v[140:143],a[208:211]\n"
 MF"a[240:243],v[116:119],v[140:143],a[240:243]\n" TR"v[72:73],v12 offset:32768\n" TR"v[74:75],v12 offset:33792\n" MF"a[216:219],v[120:123],v[140:143],a[216:219]\n"
 MF"a[248:251],v[124:127],v[140:143],a[248:251]\n" MF"a[4:7],v[96:99],v[144:147],a[4:7]\n" MF"a[36:39],v[100:103],v[144:147],a[36:39]\n" SA"m0,s42,102400\n" SA"s50,s40,33280\n"
 BL"v2,s[20:23],s50 offen lds\n" MF"a[12:15],v[104:107],v[144:147],a[12:15]\n" TR"v[76:77],v13 offset:32768\n" WL"(8)\n" TR"v[78:79],v13 offset:33792\n" MF"a[44:47],v[108:111],v[144:147],a[44:47]\n"
 MF"a[20:23],v[112:115],v[144:147],a[20:23]\n" MF"a[52:55],v[116:119],v[144:147],a[52:55]\n" MF"a[28:31],v[120:123],v[144:147],a[28:31]\n" MF"a[60:63],v[124:127],v[144:147],a[60:63]\n"
 TR"v[80:81],v14 offset:32768\n" TR"v[82:83],v14 offset:33792\n" MF"a[68:71],v[96:99],v[148:151],a[68:71]\n" SA"m0,s42,103424\n" SA"s50,s40,41472\n" BL"v3,s[20:23],s50 offen lds\n"
 MF"a[100:103],v[100:103],v[148:151],a[100:103]\n" MF"a[76:79],v[104:107],v[148:151],a[76:79]\n" MF"a[108:111],v[108:111],v[148:151],a[108:111]\n" MF"a[84:87],v[112:115],v[148:151],a[84:87]\n"
 TR"v[84:85],v15 offset:32768\n" TR"v[86:87],v15 offset:33792\n" MF"a[116:119],v[116:119],v[148:151],a[116:119]\n" MF"a[92:95],v[120:123],v[148:151],a[92:95]\n"
 MF"a[124:127],v[124:127],v[148:151],a[124:127]\n" SA"m0,s42,104448\n" SA"s50,s40,49664\n" BL"v2,s[20:23],s50 offen lds\n" MF"a[132:135],v[96:99],v[152:155],a[132:135]\n"
 MF"a[164:167],v[100:103],v[152:155],a[164:167]\n" TR"v[88:89],v16 offset:32768\n" TR"v[90:91],v16 offset:33792\n" MF"a[140:143],v[104:107],v[152:155],a[140:143]\n"
 MF"a[172:175],v[108:111],v[152:155],a[172:175]\n" MF"a[148:151],v[112:115],v[152:155],a[148:151]\n" MF"a[180:183],v[116:119],v[152:155],a[180:183]\n" MF"a[156:159],v[120:123],v[152:155],a[156:159]\n"
 WL"(8)\n" TR"v[92:93],v17 offset:32768\n" TR"v[94:95],v17 offset:33792\n" SA"m0,s42,105472\n" SA"s50,s40,57856\n" BL"v3,s[20:23],s50 offen lds\n" MF"a[188:191],v[124:127],v[152:155],a[188:191]\n"
 MF"a[196:199],v[96:99],v[156:159],a[196:199]\n" MF"a[228:231],v[100:103],v[156:159],a[228:231]\n" MF"a[204:207],v[104:107],v[156:159],a[204:207]\n" MF"a[236:239],v[108:111],v[156:159],a[236:239]\n"
 MF"a[212:215],v[112:115],v[156:159],a[212:215]\n" MF"a[244:247],v[116:119],v[156:159],a[244:247]\n" MF"a[220:223],v[120:123],v[156:159],a[220:223]\n" MF"a[252:255],v[124:127],v[156:159],a[252:255]\n"
 MF"a[0:3],v[32:35],v[64:67],a[0:3]\n" WV"(13) lgkmcnt(0)\n""s_barrier\n""s_bitcmp1_b32 s36,0\n""s_cselect_b32 s57,14,12\n""v_readlane_b32 s76,v254,s57\n" SA"s57,s57,1\n"
"v_readlane_b32 s77,v254,s57\n""s_mov_b32 s78,0x80000\n""s_mov_b32 s79,0x20000\n""s_lshl_b32 s57,s96,11\n""v_mbcnt_lo_u32_b32 v28,-1,0\n""v_mbcnt_hi_u32_b32 v28,-1,v28\n""v_min_u32 v28,15,v28\n"
"v_mul_u32_u24 v28,128,v28\n""buffer_load_dword v29,v28,s[76:79],s57 offen\n" MF"a[32:35],v[36:39],v[64:67],a[32:35]\n" MF"a[8:11],v[40:43],v[64:67],a[8:11]\n" DR"v[96:99],v7 offset:0\n"
 MF"a[40:43],v[44:47],v[64:67],a[40:43]\n" MF"a[16:19],v[48:51],v[64:67],a[16:19]\n" DR"v[100:103],v7 offset:2048\n" MF"a[48:51],v[52:55],v[64:67],a[48:51]\n" SA"m0,s43,16384\n" SA"s50,s41,2359296\n"
 BL"v4,s[16:19],s50 offen lds\n" MF"a[24:27],v[56:59],v[64:67],a[24:27]\n" DR"v[104:107],v7 offset:4096\n" MF"a[56:59],v[60:63],v[64:67],a[56:59]\n" MF"a[64:67],v[32:35],v[68:71],a[64:67]\n"
 DR"v[108:111],v7 offset:6144\n" MF"a[96:99],v[36:39],v[68:71],a[96:99]\n" MF"a[72:75],v[40:43],v[68:71],a[72:75]\n" DR"v[112:115],v7 offset:8192\n" MF"a[104:107],v[44:47],v[68:71],a[104:107]\n"
 MF"a[80:83],v[48:51],v[68:71],a[80:83]\n" DR"v[116:119],v7 offset:10240\n" SA"m0,s43,17408\n" SA"s50,s41,2392064\n" BL"v4,s[16:19],s50 offen lds\n" MF"a[112:115],v[52:55],v[68:71],a[112:115]\n"
 MF"a[88:91],v[56:59],v[68:71],a[88:91]\n" DR"v[120:123],v7 offset:12288\n" MF"a[120:123],v[60:63],v[68:71],a[120:123]\n" MF"a[128:131],v[32:35],v[72:75],a[128:131]\n" DR"v[124:127],v7 offset:14336\n"
 MF"a[160:163],v[36:39],v[72:75],a[160:163]\n" MF"a[136:139],v[40:43],v[72:75],a[136:139]\n" MF"a[168:171],v[44:47],v[72:75],a[168:171]\n" TR"v[128:129],v10 offset:49152\n"
 TR"v[130:131],v10 offset:50176\n" SA"m0,s43,18432\n" SA"s50,s41,2424832\n" BL"v5,s[16:19],s50 offen lds\n" MF"a[144:147],v[48:51],v[72:75],a[144:147]\n" MF"a[176:179],v[52:55],v[72:75],a[176:179]\n"
 MF"a[152:155],v[56:59],v[72:75],a[152:155]\n" MF"a[184:187],v[60:63],v[72:75],a[184:187]\n" MF"a[192:195],v[32:35],v[76:79],a[192:195]\n" TR"v[132:133],v11 offset:49152\n"
 TR"v[134:135],v11 offset:50176\n" MF"a[224:227],v[36:39],v[76:79],a[224:227]\n" MF"a[200:203],v[40:43],v[76:79],a[200:203]\n" SA"m0,s43,19456\n" SA"s50,s41,2457600\n" BL"v5,s[16:19],s50 offen lds\n"
 MF"a[232:235],v[44:47],v[76:79],a[232:235]\n" MF"a[208:211],v[48:51],v[76:79],a[208:211]\n" MF"a[240:243],v[52:55],v[76:79],a[240:243]\n" TR"v[136:137],v12 offset:49152\n"
 TR"v[138:139],v12 offset:50176\n" MF"a[216:219],v[56:59],v[76:79],a[216:219]\n" MF"a[248:251],v[60:63],v[76:79],a[248:251]\n" MF"a[4:7],v[32:35],v[80:83],a[4:7]\n"
 MF"a[36:39],v[36:39],v[80:83],a[36:39]\n" SA"m0,s42,131072\n" SA"s50,s40,640\n" BL"v2,s[20:23],s50 offen lds\n" MF"a[12:15],v[40:43],v[80:83],a[12:15]\n" TR"v[140:141],v13 offset:49152\n" WL"(8)\n"
 TR"v[142:143],v13 offset:50176\n" MF"a[44:47],v[44:47],v[80:83],a[44:47]\n" MF"a[20:23],v[48:51],v[80:83],a[20:23]\n" MF"a[52:55],v[52:55],v[80:83],a[52:55]\n"
 MF"a[28:31],v[56:59],v[80:83],a[28:31]\n" MF"a[60:63],v[60:63],v[80:83],a[60:63]\n" TR"v[144:145],v14 offset:49152\n" TR"v[146:147],v14 offset:50176\n" MF"a[68:71],v[32:35],v[84:87],a[68:71]\n"
 SA"m0,s42,132096\n" SA"s50,s40,8832\n" BL"v3,s[20:23],s50 offen lds\n" MF"a[100:103],v[36:39],v[84:87],a[100:103]\n" MF"a[76:79],v[40:43],v[84:87],a[76:79]\n"
 MF"a[108:111],v[44:47],v[84:87],a[108:111]\n" MF"a[84:87],v[48:51],v[84:87],a[84:87]\n" TR"v[148:149],v15 offset:49152\n" TR"v[150:151],v15 offset:50176\n"
 MF"a[116:119],v[52:55],v[84:87],a[116:119]\n" MF"a[92:95],v[56:59],v[84:87],a[92:95]\n" MF"a[124:127],v[60:63],v[84:87],a[124:127]\n" SA"m0,s42,133120\n" SA"s50,s40,17024\n"
 BL"v2,s[20:23],s50 offen lds\n" MF"a[132:135],v[32:35],v[88:91],a[132:135]\n" MF"a[164:167],v[36:39],v[88:91],a[164:167]\n" TR"v[152:153],v16 offset:49152\n" TR"v[154:155],v16 offset:50176\n"
 MF"a[140:143],v[40:43],v[88:91],a[140:143]\n" MF"a[172:175],v[44:47],v[88:91],a[172:175]\n" MF"a[148:151],v[48:51],v[88:91],a[148:151]\n" MF"a[180:183],v[52:55],v[88:91],a[180:183]\n"
 MF"a[156:159],v[56:59],v[88:91],a[156:159]\n" WL"(8)\n" TR"v[156:157],v17 offset:49152\n" TR"v[158:159],v17 offset:50176\n" SA"m0,s42,134144\n" SA"s50,s40,25216\n" BL"v3,s[20:23],s50 offen lds\n"
 MF"a[188:191],v[60:63],v[88:91],a[188:191]\n" MF"a[196:199],v[32:35],v[92:95],a[196:199]\n" MF"a[228:231],v[36:39],v[92:95],a[228:231]\n" MF"a[204:207],v[40:43],v[92:95],a[204:207]\n"
 MF"a[236:239],v[44:47],v[92:95],a[236:239]\n" MF"a[212:215],v[48:51],v[92:95],a[212:215]\n" MF"a[244:247],v[52:55],v[92:95],a[244:247]\n" MF"a[220:223],v[56:59],v[92:95],a[220:223]\n"
 MF"a[252:255],v[60:63],v[92:95],a[252:255]\n" MF"a[0:3],v[96:99],v[128:131],a[0:3]\n" WV"(9) lgkmcnt(0)\n""s_barrier\n" MF"a[32:35],v[100:103],v[128:131],a[32:35]\n"
 MF"a[8:11],v[104:107],v[128:131],a[8:11]\n" DR"v[32:35],v6 offset:32768\n" MF"a[40:43],v[108:111],v[128:131],a[40:43]\n" MF"a[16:19],v[112:115],v[128:131],a[16:19]\n" DR"v[36:39],v6 offset:34816\n"
 MF"a[48:51],v[116:119],v[128:131],a[48:51]\n" SA"m0,s43,32768\n" SA"s50,s41,2621440\n" BL"v4,s[16:19],s50 offen lds\n" MF"a[24:27],v[120:123],v[128:131],a[24:27]\n" DR"v[40:43],v6 offset:36864\n"
 MF"a[56:59],v[124:127],v[128:131],a[56:59]\n" MF"a[64:67],v[96:99],v[132:135],a[64:67]\n" DR"v[44:47],v6 offset:38912\n" MF"a[96:99],v[100:103],v[132:135],a[96:99]\n"
 MF"a[72:75],v[104:107],v[132:135],a[72:75]\n" DR"v[48:51],v6 offset:40960\n" MF"a[104:107],v[108:111],v[132:135],a[104:107]\n" MF"a[80:83],v[112:115],v[132:135],a[80:83]\n"
 DR"v[52:55],v6 offset:43008\n" SA"m0,s43,33792\n" SA"s50,s41,2654208\n" BL"v4,s[16:19],s50 offen lds\n" MF"a[112:115],v[116:119],v[132:135],a[112:115]\n" MF"a[88:91],v[120:123],v[132:135],a[88:91]\n"
 DR"v[56:59],v6 offset:45056\n" MF"a[120:123],v[124:127],v[132:135],a[120:123]\n" MF"a[128:131],v[96:99],v[136:139],a[128:131]\n" DR"v[60:63],v6 offset:47104\n"
 MF"a[160:163],v[100:103],v[136:139],a[160:163]\n" MF"a[136:139],v[104:107],v[136:139],a[136:139]\n" MF"a[168:171],v[108:111],v[136:139],a[168:171]\n" TR"v[64:65],v10 offset:0\n"
 TR"v[66:67],v10 offset:1024\n" SA"m0,s43,34816\n" SA"s50,s41,2686976\n" BL"v5,s[16:19],s50 offen lds\n" MF"a[144:147],v[112:115],v[136:139],a[144:147]\n"
 MF"a[176:179],v[116:119],v[136:139],a[176:179]\n" MF"a[152:155],v[120:123],v[136:139],a[152:155]\n" MF"a[184:187],v[124:127],v[136:139],a[184:187]\n" MF"a[192:195],v[96:99],v[140:143],a[192:195]\n"
 TR"v[68:69],v11 offset:0\n" TR"v[70:71],v11 offset:1024\n" MF"a[224:227],v[100:103],v[140:143],a[224:227]\n" MF"a[200:203],v[104:107],v[140:143],a[200:203]\n" SA"m0,s43,35840\n" SA"s50,s41,2719744\n"
 BL"v5,s[16:19],s50 offen lds\n" MF"a[232:235],v[108:111],v[140:143],a[232:235]\n" MF"a[208:211],v[112:115],v[140:143],a[208:211]\n" MF"a[240:243],v[116:119],v[140:143],a[240:243]\n"
 TR"v[72:73],v12 offset:0\n" TR"v[74:75],v12 offset:1024\n" MF"a[216:219],v[120:123],v[140:143],a[216:219]\n" MF"a[248:251],v[124:127],v[140:143],a[248:251]\n" MF"a[4:7],v[96:99],v[144:147],a[4:7]\n"
 MF"a[36:39],v[100:103],v[144:147],a[36:39]\n" SA"m0,s42,135168\n" SA"s50,s40,33408\n" BL"v2,s[20:23],s50 offen lds\n" MF"a[12:15],v[104:107],v[144:147],a[12:15]\n" TR"v[76:77],v13 offset:0\n"
 WL"(8)\n" TR"v[78:79],v13 offset:1024\n" MF"a[44:47],v[108:111],v[144:147],a[44:47]\n" MF"a[20:23],v[112:115],v[144:147],a[20:23]\n" MF"a[52:55],v[116:119],v[144:147],a[52:55]\n"
 MF"a[28:31],v[120:123],v[144:147],a[28:31]\n" MF"a[60:63],v[124:127],v[144:147],a[60:63]\n" TR"v[80:81],v14 offset:0\n" TR"v[82:83],v14 offset:1024\n" MF"a[68:71],v[96:99],v[148:151],a[68:71]\n"
 SA"m0,s42,136192\n" SA"s50,s40,41600\n" BL"v3,s[20:23],s50 offen lds\n" MF"a[100:103],v[100:103],v[148:151],a[100:103]\n" MF"a[76:79],v[104:107],v[148:151],a[76:79]\n"
 MF"a[108:111],v[108:111],v[148:151],a[108:111]\n" MF"a[84:87],v[112:115],v[148:151],a[84:87]\n" TR"v[84:85],v15 offset:0\n" TR"v[86:87],v15 offset:1024\n"
 MF"a[116:119],v[116:119],v[148:151],a[116:119]\n" MF"a[92:95],v[120:123],v[148:151],a[92:95]\n" MF"a[124:127],v[124:127],v[148:151],a[124:127]\n" SA"m0,s42,137216\n" SA"s50,s40,49792\n"
 BL"v2,s[20:23],s50 offen lds\n" MF"a[132:135],v[96:99],v[152:155],a[132:135]\n" MF"a[164:167],v[100:103],v[152:155],a[164:167]\n" TR"v[88:89],v16 offset:0\n" TR"v[90:91],v16 offset:1024\n"
 MF"a[140:143],v[104:107],v[152:155],a[140:143]\n" MF"a[172:175],v[108:111],v[152:155],a[172:175]\n" MF"a[148:151],v[112:115],v[152:155],a[148:151]\n" MF"a[180:183],v[116:119],v[152:155],a[180:183]\n"
 MF"a[156:159],v[120:123],v[152:155],a[156:159]\n" WL"(8)\n" TR"v[92:93],v17 offset:0\n" TR"v[94:95],v17 offset:1024\n" SA"m0,s42,138240\n" SA"s50,s40,57984\n" BL"v3,s[20:23],s50 offen lds\n"
 MF"a[188:191],v[124:127],v[152:155],a[188:191]\n" MF"a[196:199],v[96:99],v[156:159],a[196:199]\n" MF"a[228:231],v[100:103],v[156:159],a[228:231]\n" MF"a[204:207],v[104:107],v[156:159],a[204:207]\n"
 MF"a[236:239],v[108:111],v[156:159],a[236:239]\n" MF"a[212:215],v[112:115],v[156:159],a[212:215]\n" MF"a[244:247],v[116:119],v[156:159],a[244:247]\n" MF"a[220:223],v[120:123],v[156:159],a[220:223]\n"
 MF"a[252:255],v[124:127],v[156:159],a[252:255]\n" MF"a[0:3],v[32:35],v[64:67],a[0:3]\n" WV"(12) lgkmcnt(0)\n""s_barrier\n""v_mbcnt_lo_u32_b32 v23,-1,0\n""v_mbcnt_hi_u32_b32 v23,-1,v23\n"
"v_min_u32 v23,39,v23\n""s_lshr_b32 s60,s96,5\n""s_and_b32 s61,s96,7\n""s_lshl_b32 s61,s61,2\n""s_bfe_u32 s57,s96,0x20003\n" SA"s61,s61,s57\n""s_lshl_b32 s62,s61,2\n" SA"s62,s62,s36\n"
"s_lshl_b32 s57,s61,6\n" SA"s57,s57,4096\n" VV"v20,s57\n""s_lshr_b32 s57,s96,5\n""s_lshl_b32 s57,s57,7\n" SA"s57,s57,4096\n""s_sub_u32 s57,s57,64\n" VV"v21,s57\n""v_cmp_le_u32 vcc,8,v23\n""s_nop 1\n"
"v_cndmask_b32 v20,v20,v21,vcc\n""s_lshr_b32 s57,s96,5\n""s_sub_u32 s57,15,s57\n""s_lshl_b32 s57,s57,7\n" SA"s57,s57,4096\n""s_sub_u32 s57,s57,192\n" VV"v21,s57\n""v_cmp_le_u32 vcc,24,v23\n"
"s_nop 1\n""v_cndmask_b32 v20,v20,v21,vcc\n""v_lshl_add_u32 v20,v23,3,v20\n""buffer_load_dwordx2 v[24:25],v20,s[92:95],0 offen sc1\n" MF"a[32:35],v[36:39],v[64:67],a[32:35]\n"
 MF"a[8:11],v[40:43],v[64:67],a[8:11]\n" DR"v[96:99],v7 offset:32768\n" MF"a[40:43],v[44:47],v[64:67],a[40:43]\n" MF"a[16:19],v[48:51],v[64:67],a[16:19]\n" DR"v[100:103],v7 offset:34816\n"
 MF"a[48:51],v[52:55],v[64:67],a[48:51]\n" SA"m0,s43,49152\n" SA"s50,s41,2883584\n" BL"v4,s[16:19],s50 offen lds\n" MF"a[24:27],v[56:59],v[64:67],a[24:27]\n" DR"v[104:107],v7 offset:36864\n"
 MF"a[56:59],v[60:63],v[64:67],a[56:59]\n" MF"a[64:67],v[32:35],v[68:71],a[64:67]\n" DR"v[108:111],v7 offset:38912\n" MF"a[96:99],v[36:39],v[68:71],a[96:99]\n"
 MF"a[72:75],v[40:43],v[68:71],a[72:75]\n" DR"v[112:115],v7 offset:40960\n" MF"a[104:107],v[44:47],v[68:71],a[104:107]\n" MF"a[80:83],v[48:51],v[68:71],a[80:83]\n" DR"v[116:119],v7 offset:43008\n"
 SA"m0,s43,50176\n" SA"s50,s41,2916352\n" BL"v4,s[16:19],s50 offen lds\n" MF"a[112:115],v[52:55],v[68:71],a[112:115]\n" MF"a[88:91],v[56:59],v[68:71],a[88:91]\n" DR"v[120:123],v7 offset:45056\n"
 MF"a[120:123],v[60:63],v[68:71],a[120:123]\n" MF"a[128:131],v[32:35],v[72:75],a[128:131]\n" DR"v[124:127],v7 offset:47104\n" MF"a[160:163],v[36:39],v[72:75],a[160:163]\n"
 MF"a[136:139],v[40:43],v[72:75],a[136:139]\n" MF"a[168:171],v[44:47],v[72:75],a[168:171]\n" TR"v[128:129],v10 offset:16384\n" TR"v[130:131],v10 offset:17408\n" SA"m0,s43,51200\n"
 SA"s50,s41,2949120\n" BL"v5,s[16:19],s50 offen lds\n" MF"a[144:147],v[48:51],v[72:75],a[144:147]\n" MF"a[176:179],v[52:55],v[72:75],a[176:179]\n" MF"a[152:155],v[56:59],v[72:75],a[152:155]\n"
 MF"a[184:187],v[60:63],v[72:75],a[184:187]\n" MF"a[192:195],v[32:35],v[76:79],a[192:195]\n" TR"v[132:133],v11 offset:16384\n" TR"v[134:135],v11 offset:17408\n"
 MF"a[224:227],v[36:39],v[76:79],a[224:227]\n" MF"a[200:203],v[40:43],v[76:79],a[200:203]\n" SA"m0,s43,52224\n" SA"s50,s41,2981888\n" BL"v5,s[16:19],s50 offen lds\n"
 MF"a[232:235],v[44:47],v[76:79],a[232:235]\n" MF"a[208:211],v[48:51],v[76:79],a[208:211]\n" MF"a[240:243],v[52:55],v[76:79],a[240:243]\n" TR"v[136:137],v12 offset:16384\n"
 TR"v[138:139],v12 offset:17408\n" MF"a[216:219],v[56:59],v[76:79],a[216:219]\n" MF"a[248:251],v[60:63],v[76:79],a[248:251]\n" MF"a[4:7],v[32:35],v[80:83],a[4:7]\n"
 MF"a[36:39],v[36:39],v[80:83],a[36:39]\n" SA"m0,s42,65536\n" SA"s50,s40,768\n" BL"v2,s[20:23],s50 offen lds\n" MF"a[12:15],v[40:43],v[80:83],a[12:15]\n" TR"v[140:141],v13 offset:16384\n" WL"(8)\n"
 TR"v[142:143],v13 offset:17408\n" MF"a[44:47],v[44:47],v[80:83],a[44:47]\n" MF"a[20:23],v[48:51],v[80:83],a[20:23]\n" MF"a[52:55],v[52:55],v[80:83],a[52:55]\n"
 MF"a[28:31],v[56:59],v[80:83],a[28:31]\n" MF"a[60:63],v[60:63],v[80:83],a[60:63]\n" TR"v[144:145],v14 offset:16384\n" TR"v[146:147],v14 offset:17408\n" MF"a[68:71],v[32:35],v[84:87],a[68:71]\n"
 SA"m0,s42,66560\n" SA"s50,s40,8960\n" BL"v3,s[20:23],s50 offen lds\n" MF"a[100:103],v[36:39],v[84:87],a[100:103]\n" MF"a[76:79],v[40:43],v[84:87],a[76:79]\n"
 MF"a[108:111],v[44:47],v[84:87],a[108:111]\n" MF"a[84:87],v[48:51],v[84:87],a[84:87]\n" TR"v[148:149],v15 offset:16384\n" TR"v[150:151],v15 offset:17408\n"
 MF"a[116:119],v[52:55],v[84:87],a[116:119]\n" MF"a[92:95],v[56:59],v[84:87],a[92:95]\n" MF"a[124:127],v[60:63],v[84:87],a[124:127]\n" SA"m0,s42,67584\n" SA"s50,s40,17152\n"
 BL"v2,s[20:23],s50 offen lds\n" MF"a[132:135],v[32:35],v[88:91],a[132:135]\n" MF"a[164:167],v[36:39],v[88:91],a[164:167]\n" TR"v[152:153],v16 offset:16384\n" TR"v[154:155],v16 offset:17408\n"
 MF"a[140:143],v[40:43],v[88:91],a[140:143]\n" MF"a[172:175],v[44:47],v[88:91],a[172:175]\n" MF"a[148:151],v[48:51],v[88:91],a[148:151]\n" MF"a[180:183],v[52:55],v[88:91],a[180:183]\n"
 MF"a[156:159],v[56:59],v[88:91],a[156:159]\n" WL"(8)\n" TR"v[156:157],v17 offset:16384\n" TR"v[158:159],v17 offset:17408\n" SA"m0,s42,68608\n" SA"s50,s40,25344\n" BL"v3,s[20:23],s50 offen lds\n"
 MF"a[188:191],v[60:63],v[88:91],a[188:191]\n" MF"a[196:199],v[32:35],v[92:95],a[196:199]\n" MF"a[228:231],v[36:39],v[92:95],a[228:231]\n" MF"a[204:207],v[40:43],v[92:95],a[204:207]\n"
 MF"a[236:239],v[44:47],v[92:95],a[236:239]\n" MF"a[212:215],v[48:51],v[92:95],a[212:215]\n" MF"a[244:247],v[52:55],v[92:95],a[244:247]\n" MF"a[220:223],v[56:59],v[92:95],a[220:223]\n"
 MF"a[252:255],v[60:63],v[92:95],a[252:255]\n" MF"a[0:3],v[96:99],v[128:131],a[0:3]\n" WV"(9) lgkmcnt(0)\n""s_barrier\n" MF"a[32:35],v[100:103],v[128:131],a[32:35]\n"
 MF"a[8:11],v[104:107],v[128:131],a[8:11]\n" DR"v[32:35],v8 offset:0\n" MF"a[40:43],v[108:111],v[128:131],a[40:43]\n" MF"a[16:19],v[112:115],v[128:131],a[16:19]\n" DR"v[36:39],v8 offset:2048\n"
 MF"a[48:51],v[116:119],v[128:131],a[48:51]\n" SA"m0,s43,0\n" SA"s50,s41,3145728\n" BL"v4,s[16:19],s50 offen lds\n" MF"a[24:27],v[120:123],v[128:131],a[24:27]\n" DR"v[40:43],v8 offset:4096\n"
 MF"a[56:59],v[124:127],v[128:131],a[56:59]\n" MF"a[64:67],v[96:99],v[132:135],a[64:67]\n" DR"v[44:47],v8 offset:6144\n" MF"a[96:99],v[100:103],v[132:135],a[96:99]\n"
 MF"a[72:75],v[104:107],v[132:135],a[72:75]\n" DR"v[48:51],v8 offset:8192\n" MF"a[104:107],v[108:111],v[132:135],a[104:107]\n" MF"a[80:83],v[112:115],v[132:135],a[80:83]\n"
 DR"v[52:55],v8 offset:10240\n" SA"m0,s43,1024\n" SA"s50,s41,3178496\n" BL"v4,s[16:19],s50 offen lds\n" MF"a[112:115],v[116:119],v[132:135],a[112:115]\n" MF"a[88:91],v[120:123],v[132:135],a[88:91]\n"
 DR"v[56:59],v8 offset:12288\n" MF"a[120:123],v[124:127],v[132:135],a[120:123]\n" MF"a[128:131],v[96:99],v[136:139],a[128:131]\n" DR"v[60:63],v8 offset:14336\n"
 MF"a[160:163],v[100:103],v[136:139],a[160:163]\n" MF"a[136:139],v[104:107],v[136:139],a[136:139]\n" MF"a[168:171],v[108:111],v[136:139],a[168:171]\n" TR"v[64:65],v10 offset:32768\n"
 TR"v[66:67],v10 offset:33792\n" SA"m0,s43,2048\n" SA"s50,s41,3211264\n" BL"v5,s[16:19],s50 offen lds\n" MF"a[144:147],v[112:115],v[136:139],a[144:147]\n"
 MF"a[176:179],v[116:119],v[136:139],a[176:179]\n" MF"a[152:155],v[120:123],v[136:139],a[152:155]\n" MF"a[184:187],v[124:127],v[136:139],a[184:187]\n" MF"a[192:195],v[96:99],v[140:143],a[192:195]\n"
 TR"v[68:69],v11 offset:32768\n" TR"v[70:71],v11 offset:33792\n" MF"a[224:227],v[100:103],v[140:143],a[224:227]\n" MF"a[200:203],v[104:107],v[140:143],a[200:203]\n" SA"m0,s43,3072\n"
 SA"s50,s41,3244032\n" BL"v5,s[16:19],s50 offen lds\n" MF"a[232:235],v[108:111],v[140:143],a[232:235]\n" MF"a[208:211],v[112:115],v[140:143],a[208:211]\n"
 MF"a[240:243],v[116:119],v[140:143],a[240:243]\n" TR"v[72:73],v12 offset:32768\n" TR"v[74:75],v12 offset:33792\n" MF"a[216:219],v[120:123],v[140:143],a[216:219]\n"
 MF"a[248:251],v[124:127],v[140:143],a[248:251]\n" MF"a[4:7],v[96:99],v[144:147],a[4:7]\n" MF"a[36:39],v[100:103],v[144:147],a[36:39]\n" SA"m0,s42,69632\n" SA"s50,s40,33536\n"
 BL"v2,s[20:23],s50 offen lds\n" MF"a[12:15],v[104:107],v[144:147],a[12:15]\n" TR"v[76:77],v13 offset:32768\n" WL"(8)\n" TR"v[78:79],v13 offset:33792\n" MF"a[44:47],v[108:111],v[144:147],a[44:47]\n"
 MF"a[20:23],v[112:115],v[144:147],a[20:23]\n" MF"a[52:55],v[116:119],v[144:147],a[52:55]\n" MF"a[28:31],v[120:123],v[144:147],a[28:31]\n" MF"a[60:63],v[124:127],v[144:147],a[60:63]\n"
 TR"v[80:81],v14 offset:32768\n" TR"v[82:83],v14 offset:33792\n" MF"a[68:71],v[96:99],v[148:151],a[68:71]\n" SA"m0,s42,70656\n" SA"s50,s40,41728\n" BL"v3,s[20:23],s50 offen lds\n"
 MF"a[100:103],v[100:103],v[148:151],a[100:103]\n" MF"a[76:79],v[104:107],v[148:151],a[76:79]\n" MF"a[108:111],v[108:111],v[148:151],a[108:111]\n" MF"a[84:87],v[112:115],v[148:151],a[84:87]\n"
 TR"v[84:85],v15 offset:32768\n" TR"v[86:87],v15 offset:33792\n" MF"a[116:119],v[116:119],v[148:151],a[116:119]\n" MF"a[92:95],v[120:123],v[148:151],a[92:95]\n"
 MF"a[124:127],v[124:127],v[148:151],a[124:127]\n" SA"m0,s42,71680\n" SA"s50,s40,49920\n" BL"v2,s[20:23],s50 offen lds\n" MF"a[132:135],v[96:99],v[152:155],a[132:135]\n"
 MF"a[164:167],v[100:103],v[152:155],a[164:167]\n" TR"v[88:89],v16 offset:32768\n" TR"v[90:91],v16 offset:33792\n" MF"a[140:143],v[104:107],v[152:155],a[140:143]\n"
 MF"a[172:175],v[108:111],v[152:155],a[172:175]\n" MF"a[148:151],v[112:115],v[152:155],a[148:151]\n" MF"a[180:183],v[116:119],v[152:155],a[180:183]\n" MF"a[156:159],v[120:123],v[152:155],a[156:159]\n"
 WL"(8)\n" TR"v[92:93],v17 offset:32768\n" TR"v[94:95],v17 offset:33792\n" SA"m0,s42,72704\n" SA"s50,s40,58112\n" BL"v3,s[20:23],s50 offen lds\n" MF"a[188:191],v[124:127],v[152:155],a[188:191]\n"
 MF"a[196:199],v[96:99],v[156:159],a[196:199]\n" MF"a[228:231],v[100:103],v[156:159],a[228:231]\n" MF"a[204:207],v[104:107],v[156:159],a[204:207]\n" MF"a[236:239],v[108:111],v[156:159],a[236:239]\n"
 MF"a[212:215],v[112:115],v[156:159],a[212:215]\n" MF"a[244:247],v[116:119],v[156:159],a[244:247]\n" MF"a[220:223],v[120:123],v[156:159],a[220:223]\n" MF"a[252:255],v[124:127],v[156:159],a[252:255]\n"
 MF"a[0:3],v[32:35],v[64:67],a[0:3]\n" WV"(12) lgkmcnt(0)\n""s_barrier\n" MF"a[32:35],v[36:39],v[64:67],a[32:35]\n" MF"a[8:11],v[40:43],v[64:67],a[8:11]\n" DR"v[96:99],v9 offset:0\n"
 MF"a[40:43],v[44:47],v[64:67],a[40:43]\n" MF"a[16:19],v[48:51],v[64:67],a[16:19]\n" DR"v[100:103],v9 offset:2048\n" MF"a[48:51],v[52:55],v[64:67],a[48:51]\n" SA"m0,s43,16384\n" SA"s50,s41,3407872\n"
 BL"v4,s[16:19],s50 offen lds\n" MF"a[24:27],v[56:59],v[64:67],a[24:27]\n" DR"v[104:107],v9 offset:4096\n" MF"a[56:59],v[60:63],v[64:67],a[56:59]\n" MF"a[64:67],v[32:35],v[68:71],a[64:67]\n"
 DR"v[108:111],v9 offset:6144\n" MF"a[96:99],v[36:39],v[68:71],a[96:99]\n" MF"a[72:75],v[40:43],v[68:71],a[72:75]\n" DR"v[112:115],v9 offset:8192\n" MF"a[104:107],v[44:47],v[68:71],a[104:107]\n"
 MF"a[80:83],v[48:51],v[68:71],a[80:83]\n" DR"v[116:119],v9 offset:10240\n" SA"m0,s43,17408\n" SA"s50,s41,3440640\n" BL"v4,s[16:19],s50 offen lds\n" MF"a[112:115],v[52:55],v[68:71],a[112:115]\n"
 MF"a[88:91],v[56:59],v[68:71],a[88:91]\n" DR"v[120:123],v9 offset:12288\n" MF"a[120:123],v[60:63],v[68:71],a[120:123]\n" MF"a[128:131],v[32:35],v[72:75],a[128:131]\n" DR"v[124:127],v9 offset:14336\n"
 MF"a[160:163],v[36:39],v[72:75],a[160:163]\n" MF"a[136:139],v[40:43],v[72:75],a[136:139]\n" MF"a[168:171],v[44:47],v[72:75],a[168:171]\n" TR"v[128:129],v10 offset:49152\n"
 TR"v[130:131],v10 offset:50176\n" SA"m0,s43,18432\n" SA"s50,s41,3473408\n" BL"v5,s[16:19],s50 offen lds\n" MF"a[144:147],v[48:51],v[72:75],a[144:147]\n" MF"a[176:179],v[52:55],v[72:75],a[176:179]\n"
 MF"a[152:155],v[56:59],v[72:75],a[152:155]\n" MF"a[184:187],v[60:63],v[72:75],a[184:187]\n" MF"a[192:195],v[32:35],v[76:79],a[192:195]\n" TR"v[132:133],v11 offset:49152\n"
 TR"v[134:135],v11 offset:50176\n" MF"a[224:227],v[36:39],v[76:79],a[224:227]\n" MF"a[200:203],v[40:43],v[76:79],a[200:203]\n" SA"m0,s43,19456\n" SA"s50,s41,3506176\n" BL"v5,s[16:19],s50 offen lds\n"
 MF"a[232:235],v[44:47],v[76:79],a[232:235]\n" MF"a[208:211],v[48:51],v[76:79],a[208:211]\n" MF"a[240:243],v[52:55],v[76:79],a[240:243]\n" TR"v[136:137],v12 offset:49152\n"
 TR"v[138:139],v12 offset:50176\n" MF"a[216:219],v[56:59],v[76:79],a[216:219]\n" MF"a[248:251],v[60:63],v[76:79],a[248:251]\n" MF"a[4:7],v[32:35],v[80:83],a[4:7]\n"
 MF"a[36:39],v[36:39],v[80:83],a[36:39]\n" SA"m0,s42,98304\n" SA"s50,s40,896\n" BL"v2,s[20:23],s50 offen lds\n" MF"a[12:15],v[40:43],v[80:83],a[12:15]\n" TR"v[140:141],v13 offset:49152\n" WL"(8)\n"
 TR"v[142:143],v13 offset:50176\n" MF"a[44:47],v[44:47],v[80:83],a[44:47]\n" MF"a[20:23],v[48:51],v[80:83],a[20:23]\n" MF"a[52:55],v[52:55],v[80:83],a[52:55]\n"
 MF"a[28:31],v[56:59],v[80:83],a[28:31]\n" MF"a[60:63],v[60:63],v[80:83],a[60:63]\n" TR"v[144:145],v14 offset:49152\n" TR"v[146:147],v14 offset:50176\n" MF"a[68:71],v[32:35],v[84:87],a[68:71]\n"
 SA"m0,s42,99328\n" SA"s50,s40,9088\n" BL"v3,s[20:23],s50 offen lds\n" MF"a[100:103],v[36:39],v[84:87],a[100:103]\n" MF"a[76:79],v[40:43],v[84:87],a[76:79]\n"
 MF"a[108:111],v[44:47],v[84:87],a[108:111]\n" MF"a[84:87],v[48:51],v[84:87],a[84:87]\n" TR"v[148:149],v15 offset:49152\n" TR"v[150:151],v15 offset:50176\n"
 MF"a[116:119],v[52:55],v[84:87],a[116:119]\n" MF"a[92:95],v[56:59],v[84:87],a[92:95]\n" MF"a[124:127],v[60:63],v[84:87],a[124:127]\n" SA"m0,s42,100352\n" SA"s50,s40,17280\n"
 BL"v2,s[20:23],s50 offen lds\n" MF"a[132:135],v[32:35],v[88:91],a[132:135]\n" MF"a[164:167],v[36:39],v[88:91],a[164:167]\n" TR"v[152:153],v16 offset:49152\n" TR"v[154:155],v16 offset:50176\n"
 MF"a[140:143],v[40:43],v[88:91],a[140:143]\n" MF"a[172:175],v[44:47],v[88:91],a[172:175]\n" MF"a[148:151],v[48:51],v[88:91],a[148:151]\n" MF"a[180:183],v[52:55],v[88:91],a[180:183]\n"
 MF"a[156:159],v[56:59],v[88:91],a[156:159]\n" WL"(8)\n" TR"v[156:157],v17 offset:49152\n" TR"v[158:159],v17 offset:50176\n" SA"m0,s42,101376\n" SA"s50,s40,25472\n" BL"v3,s[20:23],s50 offen lds\n"
 MF"a[188:191],v[60:63],v[88:91],a[188:191]\n" MF"a[196:199],v[32:35],v[92:95],a[196:199]\n" MF"a[228:231],v[36:39],v[92:95],a[228:231]\n" MF"a[204:207],v[40:43],v[92:95],a[204:207]\n"
 MF"a[236:239],v[44:47],v[92:95],a[236:239]\n" MF"a[212:215],v[48:51],v[92:95],a[212:215]\n" MF"a[244:247],v[52:55],v[92:95],a[244:247]\n" MF"a[220:223],v[56:59],v[92:95],a[220:223]\n"
 MF"a[252:255],v[60:63],v[92:95],a[252:255]\n" MF"a[0:3],v[96:99],v[128:131],a[0:3]\n" WV"(8) lgkmcnt(0)\n""s_barrier\n" WV"(24)\n""v_cmp_ne_u64 vcc,s[98:99],v[24:25]\n""s_nop 1\n"
"s_cmp_eq_u64 vcc,0\n""s_cbranch_scc1 Lokp1v_%=\n""s_mov_b32 s57,32768\n""Lspp1v_%=:\n""s_sleep 2\n""buffer_load_dwordx2 v[24:25],v20,s[92:95],0 offen sc1\n" WV"(0)\n"
"v_cmp_ne_u64 vcc,s[98:99],v[24:25]\n""s_nop 1\n""s_cmp_eq_u64 vcc,0\n""s_cbranch_scc1 Lokp1v_%=\n""s_sub_u32 s57,s57,1\n""s_cmp_lg_u32 s57,0\n""s_cbranch_scc1 Lspp1v_%=\n""Lokp1v_%=:\n"
 MF"a[32:35],v[100:103],v[128:131],a[32:35]\n" MF"a[8:11],v[104:107],v[128:131],a[8:11]\n" DR"v[32:35],v6 offset:0\n" MF"a[40:43],v[108:111],v[128:131],a[40:43]\n"
 MF"a[16:19],v[112:115],v[128:131],a[16:19]\n" DR"v[36:39],v6 offset:2048\n" MF"a[48:51],v[116:119],v[128:131],a[48:51]\n" SA"m0,s43,32768\n" SA"s50,s41,3670016\n" BL"v4,s[16:19],s50 offen lds\n"
 MF"a[24:27],v[120:123],v[128:131],a[24:27]\n" DR"v[40:43],v6 offset:4096\n" MF"a[56:59],v[124:127],v[128:131],a[56:59]\n" MF"a[64:67],v[96:99],v[132:135],a[64:67]\n" DR"v[44:47],v6 offset:6144\n"
 MF"a[96:99],v[100:103],v[132:135],a[96:99]\n" MF"a[72:75],v[104:107],v[132:135],a[72:75]\n" DR"v[48:51],v6 offset:8192\n" MF"a[104:107],v[108:111],v[132:135],a[104:107]\n"
 MF"a[80:83],v[112:115],v[132:135],a[80:83]\n" DR"v[52:55],v6 offset:10240\n" SA"m0,s43,33792\n" SA"s50,s41,3702784\n" BL"v4,s[16:19],s50 offen lds\n" MF"a[112:115],v[116:119],v[132:135],a[112:115]\n"
 MF"a[88:91],v[120:123],v[132:135],a[88:91]\n" DR"v[56:59],v6 offset:12288\n" MF"a[120:123],v[124:127],v[132:135],a[120:123]\n" MF"a[128:131],v[96:99],v[136:139],a[128:131]\n"
 DR"v[60:63],v6 offset:14336\n" MF"a[160:163],v[100:103],v[136:139],a[160:163]\n" MF"a[136:139],v[104:107],v[136:139],a[136:139]\n" MF"a[168:171],v[108:111],v[136:139],a[168:171]\n"
 TR"v[64:65],v10 offset:0\n" TR"v[66:67],v10 offset:1024\n" SA"m0,s43,34816\n" SA"s50,s41,3735552\n" BL"v5,s[16:19],s50 offen lds\n" MF"a[144:147],v[112:115],v[136:139],a[144:147]\n"
 MF"a[176:179],v[116:119],v[136:139],a[176:179]\n" MF"a[152:155],v[120:123],v[136:139],a[152:155]\n" MF"a[184:187],v[124:127],v[136:139],a[184:187]\n" MF"a[192:195],v[96:99],v[140:143],a[192:195]\n"
 TR"v[68:69],v11 offset:0\n" TR"v[70:71],v11 offset:1024\n" MF"a[224:227],v[100:103],v[140:143],a[224:227]\n" MF"a[200:203],v[104:107],v[140:143],a[200:203]\n" SA"m0,s43,35840\n" SA"s50,s41,3768320\n"
 BL"v5,s[16:19],s50 offen lds\n" MF"a[232:235],v[108:111],v[140:143],a[232:235]\n" MF"a[208:211],v[112:115],v[140:143],a[208:211]\n" MF"a[240:243],v[116:119],v[140:143],a[240:243]\n"
 TR"v[72:73],v12 offset:0\n" TR"v[74:75],v12 offset:1024\n" MF"a[216:219],v[120:123],v[140:143],a[216:219]\n" MF"a[248:251],v[124:127],v[140:143],a[248:251]\n" MF"a[4:7],v[96:99],v[144:147],a[4:7]\n"
 MF"a[36:39],v[100:103],v[144:147],a[36:39]\n" SA"m0,s42,102400\n" SA"s50,s40,33664\n" BL"v2,s[20:23],s50 offen lds\n" MF"a[12:15],v[104:107],v[144:147],a[12:15]\n" TR"v[76:77],v13 offset:0\n"
 WL"(8)\n" TR"v[78:79],v13 offset:1024\n" MF"a[44:47],v[108:111],v[144:147],a[44:47]\n" MF"a[20:23],v[112:115],v[144:147],a[20:23]\n" MF"a[52:55],v[116:119],v[144:147],a[52:55]\n"
 MF"a[28:31],v[120:123],v[144:147],a[28:31]\n" MF"a[60:63],v[124:127],v[144:147],a[60:63]\n" TR"v[80:81],v14 offset:0\n" TR"v[82:83],v14 offset:1024\n" MF"a[68:71],v[96:99],v[148:151],a[68:71]\n"
 SA"m0,s42,103424\n" SA"s50,s40,41856\n" BL"v3,s[20:23],s50 offen lds\n" MF"a[100:103],v[100:103],v[148:151],a[100:103]\n" MF"a[76:79],v[104:107],v[148:151],a[76:79]\n"
 MF"a[108:111],v[108:111],v[148:151],a[108:111]\n" MF"a[84:87],v[112:115],v[148:151],a[84:87]\n" TR"v[84:85],v15 offset:0\n" TR"v[86:87],v15 offset:1024\n"
 MF"a[116:119],v[116:119],v[148:151],a[116:119]\n" MF"a[92:95],v[120:123],v[148:151],a[92:95]\n" MF"a[124:127],v[124:127],v[148:151],a[124:127]\n" SA"m0,s42,104448\n" SA"s50,s40,50048\n"
 BL"v2,s[20:23],s50 offen lds\n" MF"a[132:135],v[96:99],v[152:155],a[132:135]\n" MF"a[164:167],v[100:103],v[152:155],a[164:167]\n" TR"v[88:89],v16 offset:0\n" TR"v[90:91],v16 offset:1024\n"
 MF"a[140:143],v[104:107],v[152:155],a[140:143]\n" MF"a[172:175],v[108:111],v[152:155],a[172:175]\n" MF"a[148:151],v[112:115],v[152:155],a[148:151]\n" MF"a[180:183],v[116:119],v[152:155],a[180:183]\n"
 MF"a[156:159],v[120:123],v[152:155],a[156:159]\n" WL"(8)\n" TR"v[92:93],v17 offset:0\n" TR"v[94:95],v17 offset:1024\n" SA"m0,s42,105472\n" SA"s50,s40,58240\n" BL"v3,s[20:23],s50 offen lds\n"
 MF"a[188:191],v[124:127],v[152:155],a[188:191]\n" MF"a[196:199],v[96:99],v[156:159],a[196:199]\n" MF"a[228:231],v[100:103],v[156:159],a[228:231]\n" MF"a[204:207],v[104:107],v[156:159],a[204:207]\n"
 MF"a[236:239],v[108:111],v[156:159],a[236:239]\n" MF"a[212:215],v[112:115],v[156:159],a[212:215]\n" MF"a[244:247],v[116:119],v[156:159],a[244:247]\n" MF"a[220:223],v[120:123],v[156:159],a[220:223]\n"
 MF"a[252:255],v[124:127],v[156:159],a[252:255]\n" MF"a[0:3],v[32:35],v[64:67],a[0:3]\n" WV"(12) lgkmcnt(0)\n""s_barrier\n" SA"s72,s52,20971520\n""s_addc_u32 s73,s53,0\n""s_mov_b32 s74,2097152\n"
"s_mov_b32 s75,0x20000\n""s_lshr_b32 s58,s96,5\n""s_sub_u32 s59,15,s58\n""s_bitcmp1_b32 s36,1\n""s_cselect_b32 s58,s59,s58\n""s_lshl_b32 s58,s58,17\n""s_bfe_u32 s59,s96,0x20003\n"
"s_lshl_b32 s59,s59,15\n" SA"s58,s58,s59\n""s_and_b32 s59,s36,1\n""s_lshl_b32 s59,s59,14\n" SA"s58,s58,s59\n""s_mov_b32 s57,s58\n""v_mbcnt_lo_u32_b32 v28,-1,0\n""v_mbcnt_hi_u32_b32 v28,-1,v28\n"
"v_mul_u32_u24 v28,128,v28\n""buffer_load_dword v29,v28,s[72:75],s57 offen\n" MF"a[32:35],v[36:39],v[64:67],a[32:35]\n" MF"a[8:11],v[40:43],v[64:67],a[8:11]\n" DR"v[96:99],v7 offset:0\n"
 MF"a[40:43],v[44:47],v[64:67],a[40:43]\n" MF"a[16:19],v[48:51],v[64:67],a[16:19]\n" DR"v[100:103],v7 offset:2048\n" MF"a[48:51],v[52:55],v[64:67],a[48:51]\n" SA"m0,s43,49152\n" SA"s50,s41,3932160\n"
 BL"v4,s[16:19],s50 offen lds\n" MF"a[24:27],v[56:59],v[64:67],a[24:27]\n" DR"v[104:107],v7 offset:4096\n" MF"a[56:59],v[60:63],v[64:67],a[56:59]\n" MF"a[64:67],v[32:35],v[68:71],a[64:67]\n"
 DR"v[108:111],v7 offset:6144\n" MF"a[96:99],v[36:39],v[68:71],a[96:99]\n" MF"a[72:75],v[40:43],v[68:71],a[72:75]\n" DR"v[112:115],v7 offset:8192\n" MF"a[104:107],v[44:47],v[68:71],a[104:107]\n"
 MF"a[80:83],v[48:51],v[68:71],a[80:83]\n" DR"v[116:119],v7 offset:10240\n" SA"m0,s43,50176\n" SA"s50,s41,3964928\n" BL"v4,s[16:19],s50 offen lds\n" MF"a[112:115],v[52:55],v[68:71],a[112:115]\n"
 MF"a[88:91],v[56:59],v[68:71],a[88:91]\n" DR"v[120:123],v7 offset:12288\n" MF"a[120:123],v[60:63],v[68:71],a[120:123]\n" MF"a[128:131],v[32:35],v[72:75],a[128:131]\n" DR"v[124:127],v7 offset:14336\n"
 MF"a[160:163],v[36:39],v[72:75],a[160:163]\n" MF"a[136:139],v[40:43],v[72:75],a[136:139]\n" MF"a[168:171],v[44:47],v[72:75],a[168:171]\n" TR"v[128:129],v10 offset:16384\n"
 TR"v[130:131],v10 offset:17408\n" SA"m0,s43,51200\n" SA"s50,s41,3997696\n" BL"v5,s[16:19],s50 offen lds\n" MF"a[144:147],v[48:51],v[72:75],a[144:147]\n" MF"a[176:179],v[52:55],v[72:75],a[176:179]\n"
 MF"a[152:155],v[56:59],v[72:75],a[152:155]\n" MF"a[184:187],v[60:63],v[72:75],a[184:187]\n" MF"a[192:195],v[32:35],v[76:79],a[192:195]\n" TR"v[132:133],v11 offset:16384\n"
 TR"v[134:135],v11 offset:17408\n" MF"a[224:227],v[36:39],v[76:79],a[224:227]\n" MF"a[200:203],v[40:43],v[76:79],a[200:203]\n" SA"m0,s43,52224\n" SA"s50,s41,4030464\n" BL"v5,s[16:19],s50 offen lds\n"
 MF"a[232:235],v[44:47],v[76:79],a[232:235]\n" MF"a[208:211],v[48:51],v[76:79],a[208:211]\n" MF"a[240:243],v[52:55],v[76:79],a[240:243]\n" TR"v[136:137],v12 offset:16384\n"
 TR"v[138:139],v12 offset:17408\n" MF"a[216:219],v[56:59],v[76:79],a[216:219]\n" MF"a[248:251],v[60:63],v[76:79],a[248:251]\n" MF"a[4:7],v[32:35],v[80:83],a[4:7]\n"
 MF"a[36:39],v[36:39],v[80:83],a[36:39]\n" MF"a[12:15],v[40:43],v[80:83],a[12:15]\n" TR"v[140:141],v13 offset:16384\n" WL"(8)\n" TR"v[142:143],v13 offset:17408\n"
 MF"a[44:47],v[44:47],v[80:83],a[44:47]\n" MF"a[20:23],v[48:51],v[80:83],a[20:23]\n" MF"a[52:55],v[52:55],v[80:83],a[52:55]\n" MF"a[28:31],v[56:59],v[80:83],a[28:31]\n"
 MF"a[60:63],v[60:63],v[80:83],a[60:63]\n" TR"v[144:145],v14 offset:16384\n" TR"v[146:147],v14 offset:17408\n" MF"a[68:71],v[32:35],v[84:87],a[68:71]\n" MF"a[100:103],v[36:39],v[84:87],a[100:103]\n"
 MF"a[76:79],v[40:43],v[84:87],a[76:79]\n" MF"a[108:111],v[44:47],v[84:87],a[108:111]\n" MF"a[84:87],v[48:51],v[84:87],a[84:87]\n" TR"v[148:149],v15 offset:16384\n" TR"v[150:151],v15 offset:17408\n"
 MF"a[116:119],v[52:55],v[84:87],a[116:119]\n" MF"a[92:95],v[56:59],v[84:87],a[92:95]\n" MF"a[124:127],v[60:63],v[84:87],a[124:127]\n" MF"a[132:135],v[32:35],v[88:91],a[132:135]\n"
 MF"a[164:167],v[36:39],v[88:91],a[164:167]\n" TR"v[152:153],v16 offset:16384\n" TR"v[154:155],v16 offset:17408\n" MF"a[140:143],v[40:43],v[88:91],a[140:143]\n"
 MF"a[172:175],v[44:47],v[88:91],a[172:175]\n" MF"a[148:151],v[48:51],v[88:91],a[148:151]\n" MF"a[180:183],v[52:55],v[88:91],a[180:183]\n" MF"a[156:159],v[56:59],v[88:91],a[156:159]\n" WL"(8)\n"
 TR"v[156:157],v17 offset:16384\n" TR"v[158:159],v17 offset:17408\n" MF"a[188:191],v[60:63],v[88:91],a[188:191]\n" MF"a[196:199],v[32:35],v[92:95],a[196:199]\n"
 MF"a[228:231],v[36:39],v[92:95],a[228:231]\n" MF"a[204:207],v[40:43],v[92:95],a[204:207]\n" MF"a[236:239],v[44:47],v[92:95],a[236:239]\n" MF"a[212:215],v[48:51],v[92:95],a[212:215]\n"
 MF"a[244:247],v[52:55],v[92:95],a[244:247]\n" MF"a[220:223],v[56:59],v[92:95],a[220:223]\n" MF"a[252:255],v[60:63],v[92:95],a[252:255]\n" MF"a[0:3],v[96:99],v[128:131],a[0:3]\n" WV"(5) lgkmcnt(0)\n"
"s_barrier\n" SA"s57,s58,8192\n""v_mbcnt_lo_u32_b32 v28,-1,0\n""v_mbcnt_hi_u32_b32 v28,-1,v28\n""v_mul_u32_u24 v28,128,v28\n""buffer_load_dword v29,v28,s[72:75],s57 offen\n" SA"s76,s52,3145728\n"
"s_addc_u32 s77,s53,0\n""s_mov_b32 s78,2097152\n""s_mov_b32 s79,0x20000\n""s_lshr_b32 s60,s96,5\n""s_and_b32 s61,s96,7\n""s_lshl_b32 s61,s61,2\n""s_bfe_u32 s57,s96,0x20003\n" SA"s61,s61,s57\n"
"s_lshl_b32 s62,s61,2\n" SA"s62,s62,s36\n""s_lshl_b32 s57,s61,16\n""s_lshl_b32 s58,s60,13\n" SA"s57,s57,s58\n""v_mbcnt_lo_u32_b32 v28,-1,0\n""v_mbcnt_hi_u32_b32 v28,-1,v28\n"
"v_mul_u32_u24 v28,128,v28\n""buffer_load_dword v29,v28,s[76:79],s57 offen\n" MF"a[32:35],v[100:103],v[128:131],a[32:35]\n" MF"a[8:11],v[104:107],v[128:131],a[8:11]\n" DR"v[32:35],v6 offset:32768\n"
 MF"a[40:43],v[108:111],v[128:131],a[40:43]\n" MF"a[16:19],v[112:115],v[128:131],a[16:19]\n" DR"v[36:39],v6 offset:34816\n" MF"a[48:51],v[116:119],v[128:131],a[48:51]\n"
 MF"a[24:27],v[120:123],v[128:131],a[24:27]\n" DR"v[40:43],v6 offset:36864\n" MF"a[56:59],v[124:127],v[128:131],a[56:59]\n" MF"a[64:67],v[96:99],v[132:135],a[64:67]\n" DR"v[44:47],v6 offset:38912\n"
 MF"a[96:99],v[100:103],v[132:135],a[96:99]\n" MF"a[72:75],v[104:107],v[132:135],a[72:75]\n" DR"v[48:51],v6 offset:40960\n" MF"a[104:107],v[108:111],v[132:135],a[104:107]\n"
 MF"a[80:83],v[112:115],v[132:135],a[80:83]\n" DR"v[52:55],v6 offset:43008\n" MF"a[112:115],v[116:119],v[132:135],a[112:115]\n" MF"a[88:91],v[120:123],v[132:135],a[88:91]\n"
 DR"v[56:59],v6 offset:45056\n" MF"a[120:123],v[124:127],v[132:135],a[120:123]\n" MF"a[128:131],v[96:99],v[136:139],a[128:131]\n" DR"v[60:63],v6 offset:47104\n"
 MF"a[160:163],v[100:103],v[136:139],a[160:163]\n" MF"a[136:139],v[104:107],v[136:139],a[136:139]\n" MF"a[168:171],v[108:111],v[136:139],a[168:171]\n" TR"v[64:65],v10 offset:32768\n"
 TR"v[66:67],v10 offset:33792\n" MF"a[144:147],v[112:115],v[136:139],a[144:147]\n" MF"a[176:179],v[116:119],v[136:139],a[176:179]\n" MF"a[152:155],v[120:123],v[136:139],a[152:155]\n"
 MF"a[184:187],v[124:127],v[136:139],a[184:187]\n" MF"a[192:195],v[96:99],v[140:143],a[192:195]\n" TR"v[68:69],v11 offset:32768\n" TR"v[70:71],v11 offset:33792\n"
 MF"a[224:227],v[100:103],v[140:143],a[224:227]\n" MF"a[200:203],v[104:107],v[140:143],a[200:203]\n" MF"a[232:235],v[108:111],v[140:143],a[232:235]\n" MF"a[208:211],v[112:115],v[140:143],a[208:211]\n"
 MF"a[240:243],v[116:119],v[140:143],a[240:243]\n" TR"v[72:73],v12 offset:32768\n" TR"v[74:75],v12 offset:33792\n" MF"a[216:219],v[120:123],v[140:143],a[216:219]\n"
 MF"a[248:251],v[124:127],v[140:143],a[248:251]\n" MF"a[4:7],v[96:99],v[144:147],a[4:7]\n" MF"a[36:39],v[100:103],v[144:147],a[36:39]\n" MF"a[12:15],v[104:107],v[144:147],a[12:15]\n"
 TR"v[76:77],v13 offset:32768\n" WL"(8)\n" TR"v[78:79],v13 offset:33792\n" MF"a[44:47],v[108:111],v[144:147],a[44:47]\n" MF"a[20:23],v[112:115],v[144:147],a[20:23]\n"
 MF"a[52:55],v[116:119],v[144:147],a[52:55]\n" MF"a[28:31],v[120:123],v[144:147],a[28:31]\n" MF"a[60:63],v[124:127],v[144:147],a[60:63]\n" TR"v[80:81],v14 offset:32768\n"
 TR"v[82:83],v14 offset:33792\n" MF"a[68:71],v[96:99],v[148:151],a[68:71]\n" MF"a[100:103],v[100:103],v[148:151],a[100:103]\n" MF"a[76:79],v[104:107],v[148:151],a[76:79]\n"
 MF"a[108:111],v[108:111],v[148:151],a[108:111]\n" MF"a[84:87],v[112:115],v[148:151],a[84:87]\n" TR"v[84:85],v15 offset:32768\n" TR"v[86:87],v15 offset:33792\n"
 MF"a[116:119],v[116:119],v[148:151],a[116:119]\n" MF"a[92:95],v[120:123],v[148:151],a[92:95]\n" MF"a[124:127],v[124:127],v[148:151],a[124:127]\n" MF"a[132:135],v[96:99],v[152:155],a[132:135]\n"
 MF"a[164:167],v[100:103],v[152:155],a[164:167]\n" TR"v[88:89],v16 offset:32768\n" TR"v[90:91],v16 offset:33792\n" MF"a[140:143],v[104:107],v[152:155],a[140:143]\n"
 MF"a[172:175],v[108:111],v[152:155],a[172:175]\n" MF"a[148:151],v[112:115],v[152:155],a[148:151]\n" MF"a[180:183],v[116:119],v[152:155],a[180:183]\n" MF"a[156:159],v[120:123],v[152:155],a[156:159]\n"
 WL"(8)\n" TR"v[92:93],v17 offset:32768\n" TR"v[94:95],v17 offset:33792\n" MF"a[188:191],v[124:127],v[152:155],a[188:191]\n" MF"a[196:199],v[96:99],v[156:159],a[196:199]\n"
 MF"a[228:231],v[100:103],v[156:159],a[228:231]\n" MF"a[204:207],v[104:107],v[156:159],a[204:207]\n" MF"a[236:239],v[108:111],v[156:159],a[236:239]\n" MF"a[212:215],v[112:115],v[156:159],a[212:215]\n"
 MF"a[244:247],v[116:119],v[156:159],a[244:247]\n" MF"a[220:223],v[120:123],v[156:159],a[220:223]\n" MF"a[252:255],v[124:127],v[156:159],a[252:255]\n" MF"a[0:3],v[32:35],v[64:67],a[0:3]\n"
 WV"(2) lgkmcnt(0)\n""s_barrier\n" MF"a[32:35],v[36:39],v[64:67],a[32:35]\n" MF"a[8:11],v[40:43],v[64:67],a[8:11]\n" DR"v[96:99],v7 offset:32768\n" MF"a[40:43],v[44:47],v[64:67],a[40:43]\n"
 MF"a[16:19],v[48:51],v[64:67],a[16:19]\n" DR"v[100:103],v7 offset:34816\n" MF"a[48:51],v[52:55],v[64:67],a[48:51]\n" MF"a[24:27],v[56:59],v[64:67],a[24:27]\n" DR"v[104:107],v7 offset:36864\n"
 MF"a[56:59],v[60:63],v[64:67],a[56:59]\n" MF"a[64:67],v[32:35],v[68:71],a[64:67]\n" DR"v[108:111],v7 offset:38912\n" MF"a[96:99],v[36:39],v[68:71],a[96:99]\n"
 MF"a[72:75],v[40:43],v[68:71],a[72:75]\n" DR"v[112:115],v7 offset:40960\n" MF"a[104:107],v[44:47],v[68:71],a[104:107]\n" MF"a[80:83],v[48:51],v[68:71],a[80:83]\n" DR"v[116:119],v7 offset:43008\n"
 MF"a[112:115],v[52:55],v[68:71],a[112:115]\n" MF"a[88:91],v[56:59],v[68:71],a[88:91]\n" DR"v[120:123],v7 offset:45056\n" MF"a[120:123],v[60:63],v[68:71],a[120:123]\n"
 MF"a[128:131],v[32:35],v[72:75],a[128:131]\n" DR"v[124:127],v7 offset:47104\n" MF"a[160:163],v[36:39],v[72:75],a[160:163]\n" MF"a[136:139],v[40:43],v[72:75],a[136:139]\n"
 MF"a[168:171],v[44:47],v[72:75],a[168:171]\n" TR"v[128:129],v10 offset:49152\n" TR"v[130:131],v10 offset:50176\n" MF"a[144:147],v[48:51],v[72:75],a[144:147]\n"
 MF"a[176:179],v[52:55],v[72:75],a[176:179]\n" MF"a[152:155],v[56:59],v[72:75],a[152:155]\n" MF"a[184:187],v[60:63],v[72:75],a[184:187]\n" MF"a[192:195],v[32:35],v[76:79],a[192:195]\n"
 TR"v[132:133],v11 offset:49152\n" TR"v[134:135],v11 offset:50176\n" MF"a[224:227],v[36:39],v[76:79],a[224:227]\n" MF"a[200:203],v[40:43],v[76:79],a[200:203]\n"
 MF"a[232:235],v[44:47],v[76:79],a[232:235]\n" MF"a[208:211],v[48:51],v[76:79],a[208:211]\n" MF"a[240:243],v[52:55],v[76:79],a[240:243]\n" TR"v[136:137],v12 offset:49152\n"
 TR"v[138:139],v12 offset:50176\n" MF"a[216:219],v[56:59],v[76:79],a[216:219]\n" MF"a[248:251],v[60:63],v[76:79],a[248:251]\n" MF"a[4:7],v[32:35],v[80:83],a[4:7]\n"
 MF"a[36:39],v[36:39],v[80:83],a[36:39]\n" MF"a[12:15],v[40:43],v[80:83],a[12:15]\n" TR"v[140:141],v13 offset:49152\n" WL"(8)\n" TR"v[142:143],v13 offset:50176\n"
 MF"a[44:47],v[44:47],v[80:83],a[44:47]\n" MF"a[20:23],v[48:51],v[80:83],a[20:23]\n" MF"a[52:55],v[52:55],v[80:83],a[52:55]\n" MF"a[28:31],v[56:59],v[80:83],a[28:31]\n"
 MF"a[60:63],v[60:63],v[80:83],a[60:63]\n" TR"v[144:145],v14 offset:49152\n" TR"v[146:147],v14 offset:50176\n" MF"a[68:71],v[32:35],v[84:87],a[68:71]\n" MF"a[100:103],v[36:39],v[84:87],a[100:103]\n"
 MF"a[76:79],v[40:43],v[84:87],a[76:79]\n" MF"a[108:111],v[44:47],v[84:87],a[108:111]\n" MF"a[84:87],v[48:51],v[84:87],a[84:87]\n" TR"v[148:149],v15 offset:49152\n" TR"v[150:151],v15 offset:50176\n"
 MF"a[116:119],v[52:55],v[84:87],a[116:119]\n" MF"a[92:95],v[56:59],v[84:87],a[92:95]\n" MF"a[124:127],v[60:63],v[84:87],a[124:127]\n" MF"a[132:135],v[32:35],v[88:91],a[132:135]\n"
 MF"a[164:167],v[36:39],v[88:91],a[164:167]\n" TR"v[152:153],v16 offset:49152\n" TR"v[154:155],v16 offset:50176\n" MF"a[140:143],v[40:43],v[88:91],a[140:143]\n"
 MF"a[172:175],v[44:47],v[88:91],a[172:175]\n" MF"a[148:151],v[48:51],v[88:91],a[148:151]\n" MF"a[180:183],v[52:55],v[88:91],a[180:183]\n" MF"a[156:159],v[56:59],v[88:91],a[156:159]\n" WL"(8)\n"
 TR"v[156:157],v17 offset:49152\n" TR"v[158:159],v17 offset:50176\n" MF"a[188:191],v[60:63],v[88:91],a[188:191]\n" MF"a[196:199],v[32:35],v[92:95],a[196:199]\n"
 MF"a[228:231],v[36:39],v[92:95],a[228:231]\n" MF"a[204:207],v[40:43],v[92:95],a[204:207]\n" MF"a[236:239],v[44:47],v[92:95],a[236:239]\n" MF"a[212:215],v[48:51],v[92:95],a[212:215]\n"
 MF"a[244:247],v[52:55],v[92:95],a[244:247]\n" MF"a[220:223],v[56:59],v[92:95],a[220:223]\n" MF"a[252:255],v[60:63],v[92:95],a[252:255]\n" MF"a[128:131],v[96:99],v[136:139],a[128:131]\n" WL"(0)\n"
"s_barrier\n" MF"a[160:163],v[100:103],v[136:139],a[160:163]\n""v_mbcnt_lo_u32_b32 v20,-1,0\n""v_mbcnt_hi_u32_b32 v20,-1,v20\n""v_lshlrev_b32 v0,4,v20\n""v_lshrrev_b32 v21,3,v20\n"
"v_mul_u32_u24 v21,1024,v21\n""v_lshrrev_b32 v22,4,v20\n""v_lshlrev_b32 v23,1,v22\n""v_and_b32 v2,7,v20\n""v_xor_b32 v23,v23,v2\n""v_lshl_add_u32 v2,v23,4,v21\n" VV"v3,v0\n""s_lshl_b32 s63,s36,12\n"
"s_lshr_b32 s38,s96,5\n""s_and_b32 s39,s96,7\n""s_lshl_b32 s39,s39,2\n""s_bfe_u32 s57,s96,0x20003\n" SA"s39,s39,s57\n""s_lshl_b32 s42,s39,2\n" SA"s42,s42,s36\n""v_readlane_b32 s72,v254,12\n"
"v_readlane_b32 s73,v254,13\n""s_mov_b32 s74,524288\n""s_mov_b32 s75,0x20000\n" SA"s76,s52,3145728\n""s_addc_u32 s77,s53,0\n""s_mov_b32 s78,2097152\n""s_mov_b32 s79,0x20000\n""s_lshl_b32 s40,s36,15\n"
"s_lshl_b32 s57,s38,7\n" SA"s40,s40,s57\n""s_lshl_b32 s41,s42,14\n" MF"a[136:139],v[104:107],v[136:139],a[136:139]\n" SA"m0,s63,98304\n" SA"s58,s40,0\n" BL"v2,s[72:75],s58 offen lds\n"
 MF"a[168:171],v[108:111],v[136:139],a[168:171]\n" SA"m0,s63,99328\n" SA"s58,s40,8192\n" BL"v2,s[72:75],s58 offen lds\n" MF"a[144:147],v[112:115],v[136:139],a[144:147]\n" SA"m0,s63,100352\n"
 SA"s58,s40,16384\n" BL"v2,s[72:75],s58 offen lds\n" MF"a[176:179],v[116:119],v[136:139],a[176:179]\n" SA"m0,s63,101376\n" SA"s58,s40,24576\n" BL"v2,s[72:75],s58 offen lds\n"
 MF"a[152:155],v[120:123],v[136:139],a[152:155]\n" SA"s59,s41,0\n" BL"v[32:35],v3,s[76:79],s59 offen offset:0 sc1\n" MF"a[184:187],v[124:127],v[136:139],a[184:187]\n"
 BL"v[36:39],v3,s[76:79],s59 offen offset:1024 sc1\n" MF"a[192:195],v[96:99],v[140:143],a[192:195]\n" BL"v[40:43],v3,s[76:79],s59 offen offset:2048 sc1\n"
 MF"a[224:227],v[100:103],v[140:143],a[224:227]\n" BL"v[44:47],v3,s[76:79],s59 offen offset:3072 sc1\n" MF"a[200:203],v[104:107],v[140:143],a[200:203]\n" SA"m0,s63,114688\n" SA"s58,s40,131072\n"
 BL"v2,s[72:75],s58 offen lds\n" MF"a[232:235],v[108:111],v[140:143],a[232:235]\n" SA"m0,s63,115712\n" SA"s58,s40,139264\n" BL"v2,s[72:75],s58 offen lds\n"
 MF"a[208:211],v[112:115],v[140:143],a[208:211]\n" SA"m0,s63,116736\n" SA"s58,s40,147456\n" BL"v2,s[72:75],s58 offen lds\n" MF"a[240:243],v[116:119],v[140:143],a[240:243]\n" SA"m0,s63,117760\n"
 SA"s58,s40,155648\n" BL"v2,s[72:75],s58 offen lds\n" MF"a[216:219],v[120:123],v[140:143],a[216:219]\n" SA"s59,s41,4096\n" BL"v[48:51],v3,s[76:79],s59 offen offset:0 sc1\n"
 MF"a[248:251],v[124:127],v[140:143],a[248:251]\n" BL"v[52:55],v3,s[76:79],s59 offen offset:1024 sc1\n" MF"a[132:135],v[96:99],v[152:155],a[132:135]\n"
 BL"v[56:59],v3,s[76:79],s59 offen offset:2048 sc1\n" MF"a[164:167],v[100:103],v[152:155],a[164:167]\n" BL"v[60:63],v3,s[76:79],s59 offen offset:3072 sc1\n"
 MF"a[140:143],v[104:107],v[152:155],a[140:143]\n" SA"m0,s63,131072\n" SA"s58,s40,262144\n" BL"v2,s[72:75],s58 offen lds\n" MF"a[172:175],v[108:111],v[152:155],a[172:175]\n" SA"m0,s63,132096\n"
 SA"s58,s40,270336\n" BL"v2,s[72:75],s58 offen lds\n" MF"a[148:151],v[112:115],v[152:155],a[148:151]\n" SA"m0,s63,133120\n" SA"s58,s40,278528\n" BL"v2,s[72:75],s58 offen lds\n"
 MF"a[180:183],v[116:119],v[152:155],a[180:183]\n" SA"m0,s63,134144\n" SA"s58,s40,286720\n" BL"v2,s[72:75],s58 offen lds\n" MF"a[156:159],v[120:123],v[152:155],a[156:159]\n" SA"s59,s41,8192\n"
 BL"v[64:67],v3,s[76:79],s59 offen offset:0 sc1\n" MF"a[188:191],v[124:127],v[152:155],a[188:191]\n" BL"v[68:71],v3,s[76:79],s59 offen offset:1024 sc1\n"
 MF"a[196:199],v[96:99],v[156:159],a[196:199]\n" BL"v[72:75],v3,s[76:79],s59 offen offset:2048 sc1\n" MF"a[228:231],v[100:103],v[156:159],a[228:231]\n"
 BL"v[76:79],v3,s[76:79],s59 offen offset:3072 sc1\n" MF"a[204:207],v[104:107],v[156:159],a[204:207]\n" SA"m0,s63,147456\n" SA"s58,s40,393216\n" BL"v2,s[72:75],s58 offen lds\n"
 MF"a[236:239],v[108:111],v[156:159],a[236:239]\n" SA"m0,s63,148480\n" SA"s58,s40,401408\n" BL"v2,s[72:75],s58 offen lds\n" MF"a[212:215],v[112:115],v[156:159],a[212:215]\n" SA"m0,s63,149504\n"
 SA"s58,s40,409600\n" BL"v2,s[72:75],s58 offen lds\n" MF"a[244:247],v[116:119],v[156:159],a[244:247]\n" SA"m0,s63,150528\n" SA"s58,s40,417792\n" BL"v2,s[72:75],s58 offen lds\n"
 MF"a[220:223],v[120:123],v[156:159],a[220:223]\n" SA"s59,s41,12288\n" BL"v[80:83],v3,s[76:79],s59 offen offset:0 sc1\n" MF"a[252:255],v[124:127],v[156:159],a[252:255]\n"
 BL"v[84:87],v3,s[76:79],s59 offen offset:1024 sc1\n" MF"a[0:3],v[96:99],v[128:131],a[0:3]\n" BL"v[88:91],v3,s[76:79],s59 offen offset:2048 sc1\n" MF"a[32:35],v[100:103],v[128:131],a[32:35]\n"
 BL"v[92:95],v3,s[76:79],s59 offen offset:3072 sc1\n" MF"a[8:11],v[104:107],v[128:131],a[8:11]\n""v_mbcnt_lo_u32_b32 v20,-1,0\n""v_mbcnt_hi_u32_b32 v20,-1,v20\n""v_lshrrev_b32 v21,4,v20\n"
"v_and_b32 v22,15,v20\n""v_add_u32 v23,0,v21\n""v_lshlrev_b32 v23,1,v23\n""v_xor_b32 v23,v23,v22\n""v_lshlrev_b32 v23,4,v23\n""v_lshl_add_u32 v28,v21,13,v23\n""v_add_u32 v23,4,v21\n"
"v_lshlrev_b32 v23,1,v23\n""v_xor_b32 v23,v23,v22\n""v_lshlrev_b32 v23,4,v23\n""v_lshl_add_u32 v29,v21,13,v23\n""s_and_b32 s37,s96,31\n""s_lshr_b32 s47,s96,5\n""s_sub_u32 s55,15,s47\n"
"v_readlane_b32 s16,v254,4\n""v_readlane_b32 s17,v254,5\n""s_mov_b32 s18,4194304\n""s_mov_b32 s19,0x20000\n" SA"s28,s52,20971520\n""s_addc_u32 s29,s53,0\n""s_mov_b32 s30,2097152\n"
"s_mov_b32 s31,0x20000\n""s_lshl_b32 s57,s47,3\n" SA"s68,s57,s36\n" SA"s69,s57,7\n""s_sub_u32 s69,s69,s36\n""s_lshl_b32 s57,s55,3\n" SA"s70,s57,s36\n" SA"s71,s57,7\n""s_sub_u32 s71,s71,s36\n"
"s_lshl_b32 s68,s68,14\n""s_lshl_b32 s69,s69,14\n""s_lshl_b32 s70,s70,14\n""s_lshl_b32 s71,s71,14\n""s_lshl_b32 s57,s37,8\n""s_lshl_b32 s62,s36,18\n" SA"s62,s62,s57\n""s_lshl_b32 s61,s36,13\n"
 SA"s61,s61,32768\n" MF"a[40:43],v[108:111],v[128:131],a[40:43]\n" SA"m0,s61,0\n" SA"s50,s62,0\n" BL"v28,s[16:19],s50 offen lds\n" MF"a[16:19],v[112:115],v[128:131],a[16:19]\n" SA"m0,s61,1024\n"
 SA"s50,s62,32768\n" BL"v29,s[16:19],s50 offen lds\n" MF"a[48:51],v[116:119],v[128:131],a[48:51]\n" SA"m0,s61,2048\n" SA"s50,s62,65536\n" BL"v28,s[16:19],s50 offen lds\n"
 MF"a[24:27],v[120:123],v[128:131],a[24:27]\n" SA"m0,s61,3072\n" SA"s50,s62,98304\n" BL"v29,s[16:19],s50 offen lds\n" MF"a[56:59],v[124:127],v[128:131],a[56:59]\n" SA"m0,s61,4096\n"
 SA"s50,s62,131072\n" BL"v28,s[16:19],s50 offen lds\n" MF"a[64:67],v[96:99],v[132:135],a[64:67]\n" SA"m0,s61,5120\n" SA"s50,s62,163840\n" BL"v29,s[16:19],s50 offen lds\n"
 MF"a[96:99],v[100:103],v[132:135],a[96:99]\n" SA"m0,s61,6144\n" SA"s50,s62,196608\n" BL"v28,s[16:19],s50 offen lds\n" MF"a[72:75],v[104:107],v[132:135],a[72:75]\n" SA"m0,s61,7168\n"
 SA"s50,s62,229376\n" BL"v29,s[16:19],s50 offen lds\n" MF"a[104:107],v[108:111],v[132:135],a[104:107]\n" MF"a[80:83],v[112:115],v[132:135],a[80:83]\n" MF"a[112:115],v[116:119],v[132:135],a[112:115]\n"
 MF"a[88:91],v[120:123],v[132:135],a[88:91]\n" MF"a[120:123],v[124:127],v[132:135],a[120:123]\n" MF"a[4:7],v[96:99],v[144:147],a[4:7]\n" MF"a[36:39],v[100:103],v[144:147],a[36:39]\n"
 MF"a[12:15],v[104:107],v[144:147],a[12:15]\n" MF"a[44:47],v[108:111],v[144:147],a[44:47]\n" MF"a[20:23],v[112:115],v[144:147],a[20:23]\n" MF"a[52:55],v[116:119],v[144:147],a[52:55]\n"
 MF"a[28:31],v[120:123],v[144:147],a[28:31]\n" MF"a[60:63],v[124:127],v[144:147],a[60:63]\n" MF"a[68:71],v[96:99],v[148:151],a[68:71]\n" MF"a[100:103],v[100:103],v[148:151],a[100:103]\n"
 MF"a[76:79],v[104:107],v[148:151],a[76:79]\n" MF"a[108:111],v[108:111],v[148:151],a[108:111]\n" MF"a[84:87],v[112:115],v[148:151],a[84:87]\n" MF"a[116:119],v[116:119],v[148:151],a[116:119]\n"
 MF"a[92:95],v[120:123],v[148:151],a[92:95]\n" MF"a[124:127],v[124:127],v[148:151],a[124:127]\n""Lkve_%=:\n""s_mov_b64 s[80:81],s[24:25]\n""s_mov_b64 s[82:83],s[26:27]\n""s_mov_b32 s56,s44\n"
"v_mbcnt_lo_u32_b32 v12,-1,0\n""v_mbcnt_hi_u32_b32 v12,-1,v12\n""v_lshrrev_b32 v14,4,v12\n""v_bfe_u32 v15,v12,2,2\n""v_lshl_add_u32 v15,v14,2,v15\n""v_lshlrev_b32 v13,7,v15\n""v_and_b32 v14,3,v12\n"
"v_lshl_add_u32 v13,v14,3,v13\n""v_bfe_u32 v15,v15,1,2\n""v_xor_b32 v14,0,v15\n""v_lshl_add_u32 v4,v14,5,v13\n""v_add_u32 v8,0x10000,v4\n""v_add_u32 v16,0x20000,v4\n""v_xor_b32 v14,1,v15\n"
"v_lshl_add_u32 v5,v14,5,v13\n""v_add_u32 v9,0x10000,v5\n""v_add_u32 v17,0x20000,v5\n""v_xor_b32 v14,2,v15\n""v_lshl_add_u32 v6,v14,5,v13\n""v_add_u32 v10,0x10000,v6\n""v_add_u32 v18,0x20000,v6\n"
"v_xor_b32 v14,3,v15\n""v_lshl_add_u32 v7,v14,5,v13\n""v_add_u32 v11,0x10000,v7\n""v_add_u32 v19,0x20000,v7\n" WV"(32)\n""s_barrier\n" TR"v[96:97],v8 offset:32768\n" TR"v[98:99],v8 offset:34816\n"
 TR"v[100:101],v9 offset:32768\n" TR"v[102:103],v9 offset:34816\n" TR"v[104:105],v10 offset:32768\n" TR"v[106:107],v10 offset:34816\n" TR"v[108:109],v11 offset:32768\n"
 TR"v[110:111],v11 offset:34816\n" TR"v[112:113],v8 offset:36864\n" TR"v[114:115],v8 offset:38912\n" TR"v[116:117],v9 offset:36864\n" TR"v[118:119],v9 offset:38912\n" TR"v[120:121],v10 offset:36864\n"
 TR"v[122:123],v10 offset:38912\n" TR"v[124:125],v11 offset:36864\n" WL"(8)\n" TR"v[126:127],v11 offset:38912\n" TR"v[128:129],v8 offset:40960\n" TR"v[130:131],v8 offset:43008\n"
 TR"v[132:133],v9 offset:40960\n" TR"v[134:135],v9 offset:43008\n" TR"v[136:137],v10 offset:40960\n" TR"v[138:139],v10 offset:43008\n" WL"(8)\n" TR"v[140:141],v11 offset:40960\n"
 TR"v[142:143],v11 offset:43008\n" TR"v[144:145],v8 offset:45056\n" TR"v[146:147],v8 offset:47104\n" TR"v[148:149],v9 offset:45056\n" TR"v[150:151],v9 offset:47104\n" TR"v[152:153],v10 offset:45056\n"
 WL"(8)\n" TR"v[154:155],v10 offset:47104\n" TR"v[156:157],v11 offset:45056\n" TR"v[158:159],v11 offset:47104\n" WV"(24)\n""s_barrier\n" MF"v[224:227],v[96:99],v[32:35],0\n"
 TR"v[160:161],v8 offset:49152\n" TR"v[162:163],v8 offset:51200\n" AR"v240,a128\n" AR"v241,a129\n" AR"v242,a130\n" AR"v243,a131\n" MF"v[228:231],v[100:103],v[32:35],0\n"
 TR"v[164:165],v9 offset:49152\n" TR"v[166:167],v9 offset:51200\n" AR"v244,a160\n" AR"v245,a161\n" AR"v246,a162\n" AR"v247,a163\n" MF"v[232:235],v[104:107],v[32:35],0\n" WL"(8)\n"
 TR"v[168:169],v10 offset:49152\n" TR"v[170:171],v10 offset:51200\n" CV"v248,v240,v241\n" CV"v249,v242,v243\n" CV"v250,v244,v245\n" CV"v251,v246,v247\n" MF"v[236:239],v[108:111],v[32:35],0\n"
 TR"v[172:173],v11 offset:49152\n" TR"v[174:175],v11 offset:51200\n" SA"s50,s44,0\n" BS"v[248:251],v0,s[24:27],s50 offen offset:2048 sc1\n" AR"v240,a132\n" AR"v241,a133\n"
 MF"v[224:227],v[112:115],v[36:39],v[224:227]\n" TR"v[176:177],v8 offset:53248\n" TR"v[178:179],v8 offset:55296\n" AR"v242,a134\n" AR"v243,a135\n" AR"v244,a164\n" AR"v245,a165\n"
 MF"v[228:231],v[116:119],v[36:39],v[228:231]\n" TR"v[180:181],v9 offset:53248\n" WL"(8)\n" TR"v[182:183],v9 offset:55296\n" AR"v246,a166\n" AR"v247,a167\n" CV"v248,v240,v241\n" CV"v249,v242,v243\n"
 MF"v[232:235],v[120:123],v[36:39],v[232:235]\n" TR"v[184:185],v10 offset:53248\n" TR"v[186:187],v10 offset:55296\n" CV"v250,v244,v245\n" CV"v251,v246,v247\n" SA"s50,s44,4096\n"
 BS"v[248:251],v0,s[24:27],s50 offen offset:2048 sc1\n" MF"v[236:239],v[124:127],v[36:39],v[236:239]\n" TR"v[188:189],v11 offset:53248\n" TR"v[190:191],v11 offset:55296\n" AR"v240,a136\n"
 AR"v241,a137\n" AR"v242,a138\n" AR"v243,a139\n" MF"v[224:227],v[128:131],v[40:43],v[224:227]\n" TR"v[192:193],v8 offset:57344\n" TR"v[194:195],v8 offset:59392\n" AR"v244,a168\n" AR"v245,a169\n"
 AR"v246,a170\n" AR"v247,a171\n" MF"v[228:231],v[132:135],v[40:43],v[228:231]\n" WL"(8)\n" TR"v[196:197],v9 offset:57344\n" TR"v[198:199],v9 offset:59392\n" CV"v248,v240,v241\n" CV"v249,v242,v243\n"
 CV"v250,v244,v245\n" CV"v251,v246,v247\n" MF"v[232:235],v[136:139],v[40:43],v[232:235]\n" TR"v[200:201],v10 offset:57344\n" TR"v[202:203],v10 offset:59392\n" SA"s50,s44,8192\n"
 BS"v[248:251],v0,s[24:27],s50 offen offset:2048 sc1\n" AR"v240,a140\n" AR"v241,a141\n" MF"v[236:239],v[140:143],v[40:43],v[236:239]\n" TR"v[204:205],v11 offset:57344\n"
 TR"v[206:207],v11 offset:59392\n" AR"v242,a142\n" AR"v243,a143\n" AR"v244,a172\n" AR"v245,a173\n" MF"v[224:227],v[144:147],v[44:47],v[224:227]\n" TR"v[208:209],v8 offset:61440\n" WL"(8)\n"
 TR"v[210:211],v8 offset:63488\n" AR"v246,a174\n" AR"v247,a175\n" CV"v248,v240,v241\n" CV"v249,v242,v243\n" MF"v[228:231],v[148:151],v[44:47],v[228:231]\n" TR"v[212:213],v9 offset:61440\n"
 TR"v[214:215],v9 offset:63488\n" CV"v250,v244,v245\n" CV"v251,v246,v247\n" SA"s50,s44,12288\n" BS"v[248:251],v0,s[24:27],s50 offen offset:2048 sc1\n" MF"v[232:235],v[152:155],v[44:47],v[232:235]\n"
 TR"v[216:217],v10 offset:61440\n" TR"v[218:219],v10 offset:63488\n" AR"v240,a144\n" AR"v241,a145\n" AR"v242,a146\n" AR"v243,a147\n" MF"v[236:239],v[156:159],v[44:47],v[236:239]\n"
 TR"v[220:221],v11 offset:61440\n" TR"v[222:223],v11 offset:63488\n" AR"v244,a176\n" AR"v245,a177\n" AR"v246,a178\n" AR"v247,a179\n" WV"(20)\n""s_barrier\n"
 MF"v[224:227],v[160:163],v[48:51],v[224:227]\n" WL"(8)\n" TR"v[96:97],v16 offset:0\n" TR"v[98:99],v16 offset:2048\n" CV"v248,v240,v241\n" CV"v249,v242,v243\n" CV"v250,v244,v245\n"
 CV"v251,v246,v247\n" MF"v[228:231],v[164:167],v[48:51],v[228:231]\n" TR"v[100:101],v17 offset:0\n" TR"v[102:103],v17 offset:2048\n" SA"s50,s44,16384\n"
 BS"v[248:251],v0,s[24:27],s50 offen offset:2048 sc1\n" AR"v240,a148\n" AR"v241,a149\n" MF"v[232:235],v[168:171],v[48:51],v[232:235]\n" TR"v[104:105],v18 offset:0\n" TR"v[106:107],v18 offset:2048\n"
 AR"v242,a150\n" AR"v243,a151\n" AR"v244,a180\n" AR"v245,a181\n" MF"v[236:239],v[172:175],v[48:51],v[236:239]\n" TR"v[108:109],v19 offset:0\n" WL"(8)\n" TR"v[110:111],v19 offset:2048\n"
 AR"v246,a182\n" AR"v247,a183\n" CV"v248,v240,v241\n" CV"v249,v242,v243\n" MF"v[224:227],v[176:179],v[52:55],v[224:227]\n" TR"v[112:113],v16 offset:4096\n" TR"v[114:115],v16 offset:6144\n"
 CV"v250,v244,v245\n" CV"v251,v246,v247\n" SA"s50,s44,20480\n" BS"v[248:251],v0,s[24:27],s50 offen offset:2048 sc1\n" MF"v[228:231],v[180:183],v[52:55],v[228:231]\n" TR"v[116:117],v17 offset:4096\n"
 TR"v[118:119],v17 offset:6144\n" AR"v240,a152\n" AR"v241,a153\n" AR"v242,a154\n" AR"v243,a155\n" MF"v[232:235],v[184:187],v[52:55],v[232:235]\n" TR"v[120:121],v18 offset:4096\n"
 TR"v[122:123],v18 offset:6144\n" AR"v244,a184\n" AR"v245,a185\n" AR"v246,a186\n" AR"v247,a187\n" MF"v[236:239],v[188:191],v[52:55],v[236:239]\n" WL"(8)\n" TR"v[124:125],v19 offset:4096\n"
 TR"v[126:127],v19 offset:6144\n" CV"v248,v240,v241\n" CV"v249,v242,v243\n" CV"v250,v244,v245\n" CV"v251,v246,v247\n" MF"v[224:227],v[192:195],v[56:59],v[224:227]\n" TR"v[128:129],v16 offset:8192\n"
 TR"v[130:131],v16 offset:10240\n" SA"s50,s44,24576\n" BS"v[248:251],v0,s[24:27],s50 offen offset:2048 sc1\n" AR"v240,a156\n" AR"v241,a157\n" MF"v[228:231],v[196:199],v[56:59],v[228:231]\n"
 TR"v[132:133],v17 offset:8192\n" TR"v[134:135],v17 offset:10240\n" AR"v242,a158\n" AR"v243,a159\n" AR"v244,a188\n" AR"v245,a189\n" MF"v[232:235],v[200:203],v[56:59],v[232:235]\n"
 TR"v[136:137],v18 offset:8192\n" WL"(8)\n" TR"v[138:139],v18 offset:10240\n" AR"v246,a190\n" AR"v247,a191\n" CV"v248,v240,v241\n" CV"v249,v242,v243\n" MF"v[236:239],v[204:207],v[56:59],v[236:239]\n"
 TR"v[140:141],v19 offset:8192\n" TR"v[142:143],v19 offset:10240\n" CV"v250,v244,v245\n" CV"v251,v246,v247\n" SA"s50,s44,28672\n" BS"v[248:251],v0,s[24:27],s50 offen offset:2048 sc1\n"
 MF"v[224:227],v[208:211],v[60:63],v[224:227]\n" TR"v[144:145],v16 offset:12288\n" TR"v[146:147],v16 offset:14336\n" AR"v240,a192\n" AR"v241,a193\n" AR"v242,a194\n" AR"v243,a195\n"
 MF"v[228:231],v[212:215],v[60:63],v[228:231]\n" TR"v[148:149],v17 offset:12288\n" TR"v[150:151],v17 offset:14336\n" AR"v244,a224\n" AR"v245,a225\n" AR"v246,a226\n" AR"v247,a227\n"
 MF"v[232:235],v[216:219],v[60:63],v[232:235]\n" WL"(8)\n" TR"v[152:153],v18 offset:12288\n" TR"v[154:155],v18 offset:14336\n" CV"v248,v240,v241\n" CV"v249,v242,v243\n" CV"v250,v244,v245\n"
 CV"v251,v246,v247\n" MF"v[236:239],v[220:223],v[60:63],v[236:239]\n" TR"v[156:157],v19 offset:12288\n" TR"v[158:159],v19 offset:14336\n" SA"s50,s44,0\n"
 BS"v[248:251],v0,s[24:27],s50 offen offset:3072 sc1\n" AR"v240,a196\n" AR"v241,a197\n" WV"(17)\n""s_barrier\n" MF"v[224:227],v[96:99],v[64:67],v[224:227]\n" TR"v[160:161],v16 offset:16384\n"
 TR"v[162:163],v16 offset:18432\n" AR"v242,a198\n" AR"v243,a199\n" AR"v244,a228\n" AR"v245,a229\n" MF"v[228:231],v[100:103],v[64:67],v[228:231]\n" TR"v[164:165],v17 offset:16384\n" WL"(8)\n"
 TR"v[166:167],v17 offset:18432\n" SA"s51,s68,0\n" BL"a[128:131],v0,s[28:31],s51 offen offset:0 sc1\n" AR"v246,a230\n" AR"v247,a231\n" CV"v248,v240,v241\n" CV"v249,v242,v243\n"
 MF"v[232:235],v[104:107],v[64:67],v[232:235]\n" TR"v[168:169],v18 offset:16384\n" TR"v[170:171],v18 offset:18432\n" CV"v250,v244,v245\n" CV"v251,v246,v247\n" SA"s50,s44,4096\n"
 BS"v[248:251],v0,s[24:27],s50 offen offset:3072 sc1\n" MF"v[236:239],v[108:111],v[64:67],v[236:239]\n" TR"v[172:173],v19 offset:16384\n" TR"v[174:175],v19 offset:18432\n"
 BL"a[132:135],v0,s[28:31],s51 offen offset:1024 sc1\n" AR"v240,a200\n" AR"v241,a201\n" AR"v242,a202\n" AR"v243,a203\n" MF"v[224:227],v[112:115],v[68:71],v[224:227]\n"
 TR"v[176:177],v16 offset:20480\n" TR"v[178:179],v16 offset:22528\n" AR"v244,a232\n" AR"v245,a233\n" AR"v246,a234\n" AR"v247,a235\n" MF"v[228:231],v[116:119],v[68:71],v[228:231]\n" WL"(8)\n"
 TR"v[180:181],v17 offset:20480\n" TR"v[182:183],v17 offset:22528\n" BL"a[136:139],v0,s[28:31],s51 offen offset:2048 sc1\n" CV"v248,v240,v241\n" CV"v249,v242,v243\n" CV"v250,v244,v245\n"
 CV"v251,v246,v247\n" MF"v[232:235],v[120:123],v[68:71],v[232:235]\n" TR"v[184:185],v18 offset:20480\n" TR"v[186:187],v18 offset:22528\n" SA"s50,s44,8192\n"
 BS"v[248:251],v0,s[24:27],s50 offen offset:3072 sc1\n" AR"v240,a204\n" AR"v241,a205\n" MF"v[236:239],v[124:127],v[68:71],v[236:239]\n" TR"v[188:189],v19 offset:20480\n"
 TR"v[190:191],v19 offset:22528\n" BL"a[140:143],v0,s[28:31],s51 offen offset:3072 sc1\n" AR"v242,a206\n" AR"v243,a207\n" AR"v244,a236\n" AR"v245,a237\n"
 MF"v[224:227],v[128:131],v[72:75],v[224:227]\n" TR"v[192:193],v16 offset:24576\n" WL"(8)\n" TR"v[194:195],v16 offset:26624\n" AR"v246,a238\n" AR"v247,a239\n" CV"v248,v240,v241\n" CV"v249,v242,v243\n"
 MF"v[228:231],v[132:135],v[72:75],v[228:231]\n" TR"v[196:197],v17 offset:24576\n" TR"v[198:199],v17 offset:26624\n" SA"s51,s69,0\n" BL"a[144:147],v0,s[28:31],s51 offen offset:0 sc1\n"
 CV"v250,v244,v245\n" CV"v251,v246,v247\n" SA"s50,s44,12288\n" BS"v[248:251],v0,s[24:27],s50 offen offset:3072 sc1\n" MF"v[232:235],v[136:139],v[72:75],v[232:235]\n" TR"v[200:201],v18 offset:24576\n"
 TR"v[202:203],v18 offset:26624\n" AR"v240,a208\n" AR"v241,a209\n" AR"v242,a210\n" AR"v243,a211\n" MF"v[236:239],v[140:143],v[72:75],v[236:239]\n" TR"v[204:205],v19 offset:24576\n"
 TR"v[206:207],v19 offset:26624\n" BL"a[148:151],v0,s[28:31],s51 offen offset:1024 sc1\n" AR"v244,a240\n" AR"v245,a241\n" AR"v246,a242\n" AR"v247,a243\n"
 MF"v[224:227],v[144:147],v[76:79],v[224:227]\n" WL"(8)\n" TR"v[208:209],v16 offset:28672\n" TR"v[210:211],v16 offset:30720\n" CV"v248,v240,v241\n" CV"v249,v242,v243\n" CV"v250,v244,v245\n"
 CV"v251,v246,v247\n" MF"v[228:231],v[148:151],v[76:79],v[228:231]\n" TR"v[212:213],v17 offset:28672\n" TR"v[214:215],v17 offset:30720\n" BL"a[152:155],v0,s[28:31],s51 offen offset:2048 sc1\n"
 SA"s50,s44,16384\n" BS"v[248:251],v0,s[24:27],s50 offen offset:3072 sc1\n" AR"v240,a212\n" AR"v241,a213\n" MF"v[232:235],v[152:155],v[76:79],v[232:235]\n" TR"v[216:217],v18 offset:28672\n"
 TR"v[218:219],v18 offset:30720\n" AR"v242,a214\n" AR"v243,a215\n" AR"v244,a244\n" AR"v245,a245\n" MF"v[236:239],v[156:159],v[76:79],v[236:239]\n" TR"v[220:221],v19 offset:28672\n" WL"(8)\n"
 TR"v[222:223],v19 offset:30720\n" BL"a[156:159],v0,s[28:31],s51 offen offset:3072 sc1\n" AR"v246,a246\n" AR"v247,a247\n" CV"v248,v240,v241\n" CV"v249,v242,v243\n"
 MF"v[224:227],v[160:163],v[80:83],v[224:227]\n" CV"v250,v244,v245\n" CV"v251,v246,v247\n" SA"s50,s44,20480\n" BS"v[248:251],v0,s[24:27],s50 offen offset:3072 sc1\n"
 MF"v[228:231],v[164:167],v[80:83],v[228:231]\n" SA"s51,s70,0\n" BL"a[160:163],v0,s[28:31],s51 offen offset:0 sc1\n" AR"v240,a216\n" AR"v241,a217\n" AR"v242,a218\n" AR"v243,a219\n"
 MF"v[232:235],v[168:171],v[80:83],v[232:235]\n" AR"v244,a248\n" AR"v245,a249\n" AR"v246,a250\n" AR"v247,a251\n" MF"v[236:239],v[172:175],v[80:83],v[236:239]\n"
 BL"a[164:167],v0,s[28:31],s51 offen offset:1024 sc1\n" CV"v248,v240,v241\n" CV"v249,v242,v243\n" CV"v250,v244,v245\n" CV"v251,v246,v247\n" MF"v[224:227],v[176:179],v[84:87],v[224:227]\n"
 SA"s50,s44,24576\n" BS"v[248:251],v0,s[24:27],s50 offen offset:3072 sc1\n" AR"v240,a220\n" AR"v241,a221\n" MF"v[228:231],v[180:183],v[84:87],v[228:231]\n"
 BL"a[168:171],v0,s[28:31],s51 offen offset:2048 sc1\n" AR"v242,a222\n" AR"v243,a223\n" AR"v244,a252\n" AR"v245,a253\n" MF"v[232:235],v[184:187],v[84:87],v[232:235]\n" AR"v246,a254\n" AR"v247,a255\n"
 CV"v248,v240,v241\n" CV"v249,v242,v243\n" MF"v[236:239],v[188:191],v[84:87],v[236:239]\n" BL"a[172:175],v0,s[28:31],s51 offen offset:3072 sc1\n" CV"v250,v244,v245\n" CV"v251,v246,v247\n"
 SA"s50,s44,28672\n" BS"v[248:251],v0,s[24:27],s50 offen offset:3072 sc1\n" MF"v[224:227],v[192:195],v[88:91],v[224:227]\n" MF"v[228:231],v[196:199],v[88:91],v[228:231]\n" SA"s51,s71,0\n"
 BL"a[176:179],v0,s[28:31],s51 offen offset:0 sc1\n" MF"v[232:235],v[200:203],v[88:91],v[232:235]\n" WL"(8)\n" MF"v[236:239],v[204:207],v[88:91],v[236:239]\n"
 BL"a[180:183],v0,s[28:31],s51 offen offset:1024 sc1\n" WL"(6)\n" MF"v[224:227],v[208:211],v[92:95],v[224:227]\n" WL"(4)\n" MF"v[228:231],v[212:215],v[92:95],v[228:231]\n"
 BL"a[184:187],v0,s[28:31],s51 offen offset:2048 sc1\n" WL"(2)\n" MF"v[232:235],v[216:219],v[92:95],v[232:235]\n" WL"(0)\n" MF"v[236:239],v[220:223],v[92:95],v[236:239]\n"
 BL"a[188:191],v0,s[28:31],s51 offen offset:3072 sc1\n""s_nop 7\n""s_mov_b32 s60,0xbdd2d3e8\n" VV"v14,0xc0135761\n" VM"v160,v224,v224\n" VM"v161,v225,v225\n" VM"v162,v226,v226\n" VM"v163,v227,v227\n"
 VM"v164,v228,v228\n" VM"v165,v229,v229\n" VM"v166,v230,v230\n" VM"v167,v231,v231\n" VM"v168,v232,v232\n" VM"v169,v233,v233\n" VM"v170,v234,v234\n" VM"v171,v235,v235\n" VM"v172,v236,v236\n"
 VM"v173,v237,v237\n" VM"v174,v238,v238\n" VM"v175,v239,v239\n""v_fma_f32 v160,v160,s60,v14\n""v_fma_f32 v161,v161,s60,v14\n""v_fma_f32 v162,v162,s60,v14\n""v_fma_f32 v163,v163,s60,v14\n"
"v_fma_f32 v164,v164,s60,v14\n""v_fma_f32 v165,v165,s60,v14\n""v_fma_f32 v166,v166,s60,v14\n""v_fma_f32 v167,v167,s60,v14\n""v_fma_f32 v168,v168,s60,v14\n""v_fma_f32 v169,v169,s60,v14\n"
"v_fma_f32 v170,v170,s60,v14\n""v_fma_f32 v171,v171,s60,v14\n""v_fma_f32 v172,v172,s60,v14\n""v_fma_f32 v173,v173,s60,v14\n""v_fma_f32 v174,v174,s60,v14\n""v_fma_f32 v175,v175,s60,v14\n"
 VM"v160,v160,v224\n" VM"v161,v161,v225\n" VM"v162,v162,v226\n" VM"v163,v163,v227\n" VM"v164,v164,v228\n" VM"v165,v165,v229\n" VM"v166,v166,v230\n" VM"v167,v167,v231\n" VM"v168,v168,v232\n"
 VM"v169,v169,v233\n" VM"v170,v170,v234\n" VM"v171,v171,v235\n" VM"v172,v172,v236\n" VM"v173,v173,v237\n" VM"v174,v174,v238\n" VM"v175,v175,v239\n" EX"v160,v160\n" EX"v161,v161\n" EX"v162,v162\n"
 EX"v163,v163\n" EX"v164,v164\n" EX"v165,v165\n" EX"v166,v166\n" EX"v167,v167\n" EX"v168,v168\n" EX"v169,v169\n" EX"v170,v170\n" EX"v171,v171\n" EX"v172,v172\n" EX"v173,v173\n" EX"v174,v174\n"
 EX"v175,v175\n""v_add_f32 v160,1.0,v160\n""v_add_f32 v161,1.0,v161\n""v_add_f32 v162,1.0,v162\n""v_add_f32 v163,1.0,v163\n""v_add_f32 v164,1.0,v164\n""v_add_f32 v165,1.0,v165\n"
"v_add_f32 v166,1.0,v166\n""v_add_f32 v167,1.0,v167\n""v_add_f32 v168,1.0,v168\n""v_add_f32 v169,1.0,v169\n""v_add_f32 v170,1.0,v170\n""v_add_f32 v171,1.0,v171\n""v_add_f32 v172,1.0,v172\n"
"v_add_f32 v173,1.0,v173\n""v_add_f32 v174,1.0,v174\n""v_add_f32 v175,1.0,v175\n""v_rcp_f32 v160,v160\n""v_rcp_f32 v161,v161\n""v_rcp_f32 v162,v162\n""v_rcp_f32 v163,v163\n""v_rcp_f32 v164,v164\n"
"v_rcp_f32 v165,v165\n""v_rcp_f32 v166,v166\n""v_rcp_f32 v167,v167\n""v_rcp_f32 v168,v168\n""v_rcp_f32 v169,v169\n""v_rcp_f32 v170,v170\n""v_rcp_f32 v171,v171\n""v_rcp_f32 v172,v172\n"
"v_rcp_f32 v173,v173\n""v_rcp_f32 v174,v174\n""v_rcp_f32 v175,v175\n" VM"v224,v224,v160\n" VM"v225,v225,v161\n" VM"v226,v226,v162\n" VM"v227,v227,v163\n" VM"v228,v228,v164\n" VM"v229,v229,v165\n"
 VM"v230,v230,v166\n" VM"v231,v231,v167\n" VM"v232,v232,v168\n" VM"v233,v233,v169\n" VM"v234,v234,v170\n" VM"v235,v235,v171\n" VM"v236,v236,v172\n" VM"v237,v237,v173\n" VM"v238,v238,v174\n"
 VM"v239,v239,v175\n" CV"v96,v224,v225\n" CV"v97,v226,v227\n" CV"v98,v228,v229\n" CV"v99,v230,v231\n" CV"v100,v232,v233\n" CV"v101,v234,v235\n" CV"v102,v236,v237\n" CV"v103,v238,v239\n"
 SA"s88,s52,11534336\n""s_addc_u32 s89,s53,0\n""s_mov_b32 s90,2105344\n""s_mov_b32 s91,0x20000\n""v_lshrrev_b32 v13,8,v0\n""v_mul_u32_u24 v13,2056,v13\n""v_bfe_u32 v15,v0,4,4\n"
"v_add_u32 v13,v13,v15\n""v_lshlrev_b32 v13,4,v13\n""s_mul_i32 s58,s38,263168\n""s_lshl_b32 s59,s42,8\n" SA"s58,s58,s59\n" SA"s58,s58,32\n" BS"v[96:99],v13,s[88:91],s58 offen sc1\n"
 SA"s58,s58,131584\n" BS"v[100:103],v13,s[88:91],s58 offen sc1\n""v_mbcnt_lo_u32_b32 v248,-1,0\n""v_mbcnt_hi_u32_b32 v248,-1,v248\n""v_lshlrev_b32 v0,4,v248\n""v_add_u32 v1,4096,v0\n"
"v_lshrrev_b32 v249,4,v248\n""v_and_b32 v250,15,v248\n""v_add_u32 v251,0,v249\n""v_lshlrev_b32 v251,1,v251\n""v_xor_b32 v251,v251,v250\n""v_lshlrev_b32 v251,4,v251\n""v_lshl_add_u32 v6,v249,13,v251\n"
"v_add_u32 v251,4,v249\n""v_lshlrev_b32 v251,1,v251\n""v_xor_b32 v251,v251,v250\n""v_lshlrev_b32 v251,4,v251\n""v_lshl_add_u32 v7,v249,13,v251\n""v_bfe_u32 v250,v248,2,2\n"
"v_lshl_add_u32 v251,v249,2,v250\n""v_lshlrev_b32 v252,8,v251\n""v_and_b32 v253,3,v248\n""v_lshl_add_u32 v252,v253,3,v252\n""v_add_u32 v252,32768,v252\n""v_and_b32 v251,7,v251\n"
"v_xor_b32 v253,0,v251\n""v_lshl_add_u32 v8,v253,5,v252\n""v_xor_b32 v253,1,v251\n""v_lshl_add_u32 v9,v253,5,v252\n""v_xor_b32 v253,2,v251\n""v_lshl_add_u32 v10,v253,5,v252\n""v_xor_b32 v253,3,v251\n"
"v_lshl_add_u32 v11,v253,5,v252\n""v_xor_b32 v253,4,v251\n""v_lshl_add_u32 v12,v253,5,v252\n""v_xor_b32 v253,5,v251\n""v_lshl_add_u32 v13,v253,5,v252\n""v_xor_b32 v253,6,v251\n"
"v_lshl_add_u32 v14,v253,5,v252\n""v_xor_b32 v253,7,v251\n""v_lshl_add_u32 v15,v253,5,v252\n""s_lshl_b32 s50,s37,20\n" SA"s50,s50,33554432\n" SA"s20,s52,s50\n""s_addc_u32 s21,s53,0\n"
"s_mov_b32 s23,0x20000\n" SA"s24,s20,516096\n""s_addc_u32 s25,s21,0\n""s_mov_b32 s27,0x20000\n""s_lshl_b32 s46,s36,13\n" SA"s51,s55,1\n""s_lshl_b32 s22,s51,15\n" SA"s26,s22,8192\n"
"s_mov_b32 s40,s46\n" SA"s50,s47,1\n""s_lshl_b32 s50,s50,15\n" SA"s48,s50,s46\n""s_cmp_eq_u32 s36,0\n""s_cselect_b32 s58,32768,s46\n""s_cselect_b32 s51,32768,0\n" SA"s49,s48,s51\n" SA"s41,s46,32768\n"
"s_cmp_eq_u32 s41,s49\n""s_cselect_b32 s41,s58,s41\n""s_cselect_b32 s49,-1,s49\n" WV"(2)\n""s_barrier\n" TR"v[16:17],v8 offset:0\n" TR"v[18:19],v8 offset:4096\n" TR"v[20:21],v9 offset:0\n"
 TR"v[22:23],v9 offset:4096\n" TR"v[24:25],v10 offset:0\n" TR"v[26:27],v10 offset:4096\n" WL"(4)\n" MF"v[48:51],v[16:19],a[128:131],0\n" TR"v[28:29],v11 offset:0\n" TR"v[30:31],v11 offset:4096\n"
 SA"s51,s68,4096\n" BL"a[192:195],v0,s[28:31],s51 offen offset:0 sc1\n" AR"v128,a0\n" MF"v[52:55],v[16:19],a[144:147],0\n" AR"v129,a1\n" AR"v130,a2\n" MF"v[56:59],v[16:19],a[160:163],0\n"
 AR"v131,a3\n" AR"v132,a32\n" MF"v[60:63],v[16:19],a[176:179],0\n" BL"a[196:199],v0,s[28:31],s51 offen offset:1024 sc1\n" AR"v133,a33\n" AR"v134,a34\n" WL"(4)\n" MF"v[64:67],v[20:23],a[128:131],0\n"
 TR"v[32:33],v12 offset:0\n" TR"v[34:35],v12 offset:4096\n" AR"v135,a35\n" MF"v[68:71],v[20:23],a[144:147],0\n" CV"v136,v128,v129\n" CV"v137,v130,v131\n" MF"v[72:75],v[20:23],a[160:163],0\n"
 BL"a[200:203],v0,s[28:31],s51 offen offset:2048 sc1\n" CV"v138,v132,v133\n" CV"v139,v134,v135\n" MF"v[76:79],v[20:23],a[176:179],0\n" SA"s97,s56,0\n"
 BS"v[136:139],v0,s[80:83],s97 offen offset:0 sc1\n" WL"(4)\n" MF"v[80:83],v[24:27],a[128:131],0\n" TR"v[36:37],v13 offset:0\n" TR"v[38:39],v13 offset:4096\n" AR"v140,a4\n"
 MF"v[84:87],v[24:27],a[144:147],0\n" BL"a[204:207],v0,s[28:31],s51 offen offset:3072 sc1\n" AR"v141,a5\n" AR"v142,a6\n" MF"v[88:91],v[24:27],a[160:163],0\n" AR"v143,a7\n" AR"v144,a36\n"
 MF"v[92:95],v[24:27],a[176:179],0\n" AR"v145,a37\n" AR"v146,a38\n" WL"(4)\n" MF"v[96:99],v[28:31],a[128:131],0\n" TR"v[40:41],v14 offset:0\n" TR"v[42:43],v14 offset:4096\n" AR"v147,a39\n"
 MF"v[100:103],v[28:31],a[144:147],0\n" SA"s51,s69,4096\n" BL"a[208:211],v0,s[28:31],s51 offen offset:0 sc1\n" CV"v148,v140,v141\n" CV"v149,v142,v143\n" MF"v[104:107],v[28:31],a[160:163],0\n"
 CV"v150,v144,v145\n" CV"v151,v146,v147\n" MF"v[108:111],v[28:31],a[176:179],0\n" SA"s97,s56,4096\n" BS"v[148:151],v0,s[80:83],s97 offen offset:0 sc1\n" WL"(4)\n"
 MF"v[112:115],v[32:35],a[128:131],0\n" TR"v[44:45],v15 offset:0\n" TR"v[46:47],v15 offset:4096\n" BL"a[212:215],v0,s[28:31],s51 offen offset:1024 sc1\n" AR"v152,a8\n"
 MF"v[116:119],v[32:35],a[144:147],0\n" AR"v153,a9\n" AR"v154,a10\n" MF"v[120:123],v[32:35],a[160:163],0\n" AR"v155,a11\n" AR"v156,a40\n" MF"v[124:127],v[32:35],a[176:179],0\n"
 BL"a[216:219],v0,s[28:31],s51 offen offset:2048 sc1\n" AR"v157,a41\n" AR"v158,a42\n" WL"(4)\n" MF"v[200:203],v[36:39],a[128:131],0\n" TR"v[16:17],v8 offset:8192\n" TR"v[18:19],v8 offset:12288\n"
 AR"v159,a43\n" MF"v[204:207],v[36:39],a[144:147],0\n" CV"v160,v152,v153\n" CV"v161,v154,v155\n" MF"v[208:211],v[36:39],a[160:163],0\n" BL"a[220:223],v0,s[28:31],s51 offen offset:3072 sc1\n"
 CV"v162,v156,v157\n" CV"v163,v158,v159\n" MF"v[212:215],v[36:39],a[176:179],0\n" SA"s97,s56,8192\n" BS"v[160:163],v0,s[80:83],s97 offen offset:0 sc1\n" WL"(4)\n"
 MF"v[216:219],v[40:43],a[128:131],0\n" TR"v[20:21],v9 offset:8192\n" TR"v[22:23],v9 offset:12288\n" AR"v164,a12\n" MF"v[220:223],v[40:43],a[144:147],0\n" AR"v165,a13\n" AR"v166,a14\n"
 MF"v[224:227],v[40:43],a[160:163],0\n" SA"s51,s70,4096\n" BL"a[224:227],v0,s[28:31],s51 offen offset:0 sc1\n" AR"v167,a15\n" AR"v168,a44\n" MF"v[228:231],v[40:43],a[176:179],0\n" AR"v169,a45\n"
 AR"v170,a46\n" WL"(4)\n" MF"v[232:235],v[44:47],a[128:131],0\n" TR"v[24:25],v10 offset:8192\n" TR"v[26:27],v10 offset:12288\n" AR"v171,a47\n" MF"v[236:239],v[44:47],a[144:147],0\n"
 BL"a[228:231],v0,s[28:31],s51 offen offset:1024 sc1\n" CV"v172,v164,v165\n" CV"v173,v166,v167\n" MF"v[240:243],v[44:47],a[160:163],0\n" CV"v174,v168,v169\n" CV"v175,v170,v171\n"
 MF"v[244:247],v[44:47],a[176:179],0\n" SA"s97,s56,12288\n" BS"v[172:175],v0,s[80:83],s97 offen offset:0 sc1\n" WL"(4)\n" MF"v[48:51],v[16:19],a[132:135],v[48:51]\n" TR"v[28:29],v11 offset:8192\n"
 TR"v[30:31],v11 offset:12288\n" BL"a[232:235],v0,s[28:31],s51 offen offset:2048 sc1\n" AR"v128,a16\n" MF"v[52:55],v[16:19],a[148:151],v[52:55]\n" AR"v129,a17\n" AR"v130,a18\n"
 MF"v[56:59],v[16:19],a[164:167],v[56:59]\n" AR"v131,a19\n" AR"v132,a48\n" MF"v[60:63],v[16:19],a[180:183],v[60:63]\n" BL"a[236:239],v0,s[28:31],s51 offen offset:3072 sc1\n" AR"v133,a49\n"
 AR"v134,a50\n" WL"(4)\n" MF"v[64:67],v[20:23],a[132:135],v[64:67]\n" TR"v[32:33],v12 offset:8192\n" TR"v[34:35],v12 offset:12288\n" AR"v135,a51\n" MF"v[68:71],v[20:23],a[148:151],v[68:71]\n"
 CV"v136,v128,v129\n" CV"v137,v130,v131\n" MF"v[72:75],v[20:23],a[164:167],v[72:75]\n" CV"v138,v132,v133\n" CV"v139,v134,v135\n" MF"v[76:79],v[20:23],a[180:183],v[76:79]\n" SA"s51,s71,4096\n"
 BL"a[240:243],v0,s[28:31],s51 offen offset:0 sc1\n" SA"s97,s56,16384\n" BS"v[136:139],v0,s[80:83],s97 offen offset:0 sc1\n" WL"(4)\n" MF"v[80:83],v[24:27],a[132:135],v[80:83]\n"
 TR"v[36:37],v13 offset:8192\n" TR"v[38:39],v13 offset:12288\n" AR"v140,a20\n" MF"v[84:87],v[24:27],a[148:151],v[84:87]\n" AR"v141,a21\n" AR"v142,a22\n" MF"v[88:91],v[24:27],a[164:167],v[88:91]\n"
 BL"a[244:247],v0,s[28:31],s51 offen offset:1024 sc1\n" AR"v143,a23\n" AR"v144,a52\n" MF"v[92:95],v[24:27],a[180:183],v[92:95]\n" AR"v145,a53\n" AR"v146,a54\n" WL"(4)\n"
 MF"v[96:99],v[28:31],a[132:135],v[96:99]\n" TR"v[40:41],v14 offset:8192\n" TR"v[42:43],v14 offset:12288\n" AR"v147,a55\n" MF"v[100:103],v[28:31],a[148:151],v[100:103]\n"
 BL"a[248:251],v0,s[28:31],s51 offen offset:2048 sc1\n" CV"v148,v140,v141\n" CV"v149,v142,v143\n" MF"v[104:107],v[28:31],a[164:167],v[104:107]\n" CV"v150,v144,v145\n" CV"v151,v146,v147\n"
 MF"v[108:111],v[28:31],a[180:183],v[108:111]\n" SA"s97,s56,20480\n" BS"v[148:151],v0,s[80:83],s97 offen offset:0 sc1\n" WL"(4)\n" MF"v[112:115],v[32:35],a[132:135],v[112:115]\n"
 TR"v[44:45],v15 offset:8192\n" TR"v[46:47],v15 offset:12288\n" BL"a[252:255],v0,s[28:31],s51 offen offset:3072 sc1\n" MF"v[116:119],v[32:35],a[148:151],v[116:119]\n"
 MF"v[120:123],v[32:35],a[164:167],v[120:123]\n" MF"v[124:127],v[32:35],a[180:183],v[124:127]\n" WL"(4)\n" MF"v[200:203],v[36:39],a[132:135],v[200:203]\n" TR"v[16:17],v8 offset:16384\n"
 TR"v[18:19],v8 offset:20480\n" SA"m0,s61,32768\n" SA"s50,s62,1048576\n" BL"v6,s[16:19],s50 offen lds\n" MF"v[204:207],v[36:39],a[148:151],v[204:207]\n" MF"v[208:211],v[36:39],a[164:167],v[208:211]\n"
 MF"v[212:215],v[36:39],a[180:183],v[212:215]\n" SA"m0,s61,33792\n" SA"s50,s62,1081344\n" BL"v7,s[16:19],s50 offen lds\n" WL"(4)\n" MF"v[216:219],v[40:43],a[132:135],v[216:219]\n"
 TR"v[20:21],v9 offset:16384\n" TR"v[22:23],v9 offset:20480\n" MF"v[220:223],v[40:43],a[148:151],v[220:223]\n" MF"v[224:227],v[40:43],a[164:167],v[224:227]\n" SA"m0,s61,34816\n" SA"s50,s62,1114112\n"
 BL"v6,s[16:19],s50 offen lds\n" MF"v[228:231],v[40:43],a[180:183],v[228:231]\n" WL"(4)\n" MF"v[232:235],v[44:47],a[132:135],v[232:235]\n" TR"v[24:25],v10 offset:16384\n"
 TR"v[26:27],v10 offset:20480\n" MF"v[236:239],v[44:47],a[148:151],v[236:239]\n" SA"m0,s61,35840\n" SA"s50,s62,1146880\n" BL"v7,s[16:19],s50 offen lds\n"
 MF"v[240:243],v[44:47],a[164:167],v[240:243]\n" MF"v[244:247],v[44:47],a[180:183],v[244:247]\n" WL"(4)\n" MF"v[48:51],v[16:19],a[136:139],v[48:51]\n" TR"v[28:29],v11 offset:16384\n"
 TR"v[30:31],v11 offset:20480\n" MF"v[52:55],v[16:19],a[152:155],v[52:55]\n" SA"m0,s61,36864\n" SA"s50,s62,1179648\n" BL"v6,s[16:19],s50 offen lds\n" MF"v[56:59],v[16:19],a[168:171],v[56:59]\n"
 MF"v[60:63],v[16:19],a[184:187],v[60:63]\n" WL"(4)\n" MF"v[64:67],v[20:23],a[136:139],v[64:67]\n" TR"v[32:33],v12 offset:16384\n" TR"v[34:35],v12 offset:20480\n" SA"m0,s61,37888\n"
 SA"s50,s62,1212416\n" BL"v7,s[16:19],s50 offen lds\n" MF"v[68:71],v[20:23],a[152:155],v[68:71]\n" MF"v[72:75],v[20:23],a[168:171],v[72:75]\n" MF"v[76:79],v[20:23],a[184:187],v[76:79]\n"
 SA"m0,s61,38912\n" SA"s50,s62,1245184\n" BL"v6,s[16:19],s50 offen lds\n" WL"(4)\n" MF"v[80:83],v[24:27],a[136:139],v[80:83]\n" TR"v[36:37],v13 offset:16384\n" TR"v[38:39],v13 offset:20480\n"
 MF"v[84:87],v[24:27],a[152:155],v[84:87]\n" MF"v[88:91],v[24:27],a[168:171],v[88:91]\n" SA"m0,s61,39936\n" SA"s50,s62,1277952\n" BL"v7,s[16:19],s50 offen lds\n"
 MF"v[92:95],v[24:27],a[184:187],v[92:95]\n" WL"(4)\n" MF"v[96:99],v[28:31],a[136:139],v[96:99]\n" TR"v[40:41],v14 offset:16384\n" TR"v[42:43],v14 offset:20480\n"
 MF"v[100:103],v[28:31],a[152:155],v[100:103]\n" MF"v[104:107],v[28:31],a[168:171],v[104:107]\n" SA"m0,s61,65536\n" SA"s50,s62,2097152\n" BL"v6,s[16:19],s50 offen lds\n"
 MF"v[108:111],v[28:31],a[184:187],v[108:111]\n" WL"(4)\n" MF"v[112:115],v[32:35],a[136:139],v[112:115]\n" TR"v[44:45],v15 offset:16384\n" TR"v[46:47],v15 offset:20480\n"
 MF"v[116:119],v[32:35],a[152:155],v[116:119]\n" SA"m0,s61,66560\n" SA"s50,s62,2129920\n" BL"v7,s[16:19],s50 offen lds\n" MF"v[120:123],v[32:35],a[168:171],v[120:123]\n"
 MF"v[124:127],v[32:35],a[184:187],v[124:127]\n" WL"(4)\n" MF"v[200:203],v[36:39],a[136:139],v[200:203]\n" TR"v[16:17],v8 offset:24576\n" TR"v[18:19],v8 offset:28672\n" SA"m0,s61,67584\n"
 SA"s50,s62,2162688\n" BL"v6,s[16:19],s50 offen lds\n" MF"v[204:207],v[36:39],a[152:155],v[204:207]\n" MF"v[208:211],v[36:39],a[168:171],v[208:211]\n" MF"v[212:215],v[36:39],a[184:187],v[212:215]\n"
 SA"m0,s61,68608\n" SA"s50,s62,2195456\n" BL"v7,s[16:19],s50 offen lds\n" WL"(4)\n" MF"v[216:219],v[40:43],a[136:139],v[216:219]\n" TR"v[20:21],v9 offset:24576\n" TR"v[22:23],v9 offset:28672\n"
 MF"v[220:223],v[40:43],a[152:155],v[220:223]\n" MF"v[224:227],v[40:43],a[168:171],v[224:227]\n" MF"v[228:231],v[40:43],a[184:187],v[228:231]\n" SA"m0,s61,69632\n" SA"s50,s62,2228224\n"
 BL"v6,s[16:19],s50 offen lds\n" WL"(4)\n" MF"v[232:235],v[44:47],a[136:139],v[232:235]\n" TR"v[24:25],v10 offset:24576\n" TR"v[26:27],v10 offset:28672\n"
 MF"v[236:239],v[44:47],a[152:155],v[236:239]\n" MF"v[240:243],v[44:47],a[168:171],v[240:243]\n" SA"m0,s61,70656\n" SA"s50,s62,2260992\n" BL"v7,s[16:19],s50 offen lds\n"
 MF"v[244:247],v[44:47],a[184:187],v[244:247]\n" WL"(4)\n" MF"v[48:51],v[16:19],a[140:143],v[48:51]\n" TR"v[28:29],v11 offset:24576\n" TR"v[30:31],v11 offset:28672\n"
 MF"v[52:55],v[16:19],a[156:159],v[52:55]\n" SA"m0,s61,71680\n" SA"s50,s62,2293760\n" BL"v6,s[16:19],s50 offen lds\n" MF"v[56:59],v[16:19],a[172:175],v[56:59]\n"
 MF"v[60:63],v[16:19],a[188:191],v[60:63]\n" WL"(4)\n" MF"v[64:67],v[20:23],a[140:143],v[64:67]\n" TR"v[32:33],v12 offset:24576\n" TR"v[34:35],v12 offset:28672\n" SA"m0,s61,72704\n"
 SA"s50,s62,2326528\n" BL"v7,s[16:19],s50 offen lds\n" MF"v[68:71],v[20:23],a[156:159],v[68:71]\n" MF"v[72:75],v[20:23],a[172:175],v[72:75]\n" MF"v[76:79],v[20:23],a[188:191],v[76:79]\n" WL"(4)\n"
 MF"v[80:83],v[24:27],a[140:143],v[80:83]\n" TR"v[36:37],v13 offset:24576\n" TR"v[38:39],v13 offset:28672\n" MF"v[84:87],v[24:27],a[156:159],v[84:87]\n" TR"v[40:41],v14 offset:24576\n"
 TR"v[42:43],v14 offset:28672\n" MF"v[88:91],v[24:27],a[172:175],v[88:91]\n" TR"v[44:45],v15 offset:24576\n" TR"v[46:47],v15 offset:28672\n" MF"v[92:95],v[24:27],a[188:191],v[92:95]\n" WL"(8)\n"
 MF"v[96:99],v[28:31],a[140:143],v[96:99]\n" MF"v[100:103],v[28:31],a[156:159],v[100:103]\n" MF"v[104:107],v[28:31],a[172:175],v[104:107]\n" MF"v[108:111],v[28:31],a[188:191],v[108:111]\n" WL"(6)\n"
 MF"v[112:115],v[32:35],a[140:143],v[112:115]\n" WV"(8) lgkmcnt(0)\n""s_barrier\n""s_lshr_b32 s38,s96,5\n""s_and_b32 s39,s96,7\n""s_lshl_b32 s39,s39,2\n""s_bfe_u32 s57,s96,0x20003\n" SA"s39,s39,s57\n"
"s_lshl_b32 s42,s39,2\n" SA"s42,s42,s36\n""s_lshl_b32 s57,s39,3\n" SA"s57,s57,s38\n""s_lshl_b32 s57,s57,3\n" SA"s57,s57,12288\n" VV"v250,s98\n" VV"v251,s99\n" VV"v252,0\n"
"buffer_store_dwordx2 v[250:251],v252,s[92:95],s57 offen sc1\n" MF"v[116:119],v[32:35],a[156:159],v[116:119]\n" MF"v[120:123],v[32:35],a[172:175],v[120:123]\n"
 MF"v[124:127],v[32:35],a[188:191],v[124:127]\n" MF"v[200:203],v[36:39],a[140:143],v[200:203]\n" TR"v[16:17],v8 offset:32768\n" TR"v[18:19],v8 offset:36864\n"
 MF"v[204:207],v[36:39],a[156:159],v[204:207]\n" MF"v[208:211],v[36:39],a[172:175],v[208:211]\n" MF"v[212:215],v[36:39],a[188:191],v[212:215]\n" MF"v[216:219],v[40:43],a[140:143],v[216:219]\n"
 TR"v[20:21],v9 offset:32768\n" TR"v[22:23],v9 offset:36864\n" MF"v[220:223],v[40:43],a[156:159],v[220:223]\n" MF"v[224:227],v[40:43],a[172:175],v[224:227]\n"
 MF"v[228:231],v[40:43],a[188:191],v[228:231]\n" MF"v[232:235],v[44:47],a[140:143],v[232:235]\n" TR"v[24:25],v10 offset:32768\n" TR"v[26:27],v10 offset:36864\n"
 MF"v[236:239],v[44:47],a[156:159],v[236:239]\n" MF"v[240:243],v[44:47],a[172:175],v[240:243]\n" MF"v[244:247],v[44:47],a[188:191],v[244:247]\n" WL"(4)\n" MF"v[48:51],v[16:19],a[192:195],v[48:51]\n"
 TR"v[28:29],v11 offset:32768\n" TR"v[30:31],v11 offset:36864\n" SA"s51,s68,8192\n" BL"a[128:131],v0,s[28:31],s51 offen offset:0 sc1\n" AR"v152,a24\n" MF"v[52:55],v[16:19],a[208:211],v[52:55]\n"
 AR"v153,a25\n" AR"v154,a26\n" MF"v[56:59],v[16:19],a[224:227],v[56:59]\n" AR"v155,a27\n" AR"v156,a56\n" MF"v[60:63],v[16:19],a[240:243],v[60:63]\n" AR"v157,a57\n" AR"v158,a58\n" WL"(4)\n"
 MF"v[64:67],v[20:23],a[192:195],v[64:67]\n" TR"v[32:33],v12 offset:32768\n" TR"v[34:35],v12 offset:36864\n" BL"a[132:135],v0,s[28:31],s51 offen offset:1024 sc1\n" AR"v159,a59\n"
 MF"v[68:71],v[20:23],a[208:211],v[68:71]\n" CV"v160,v152,v153\n" CV"v161,v154,v155\n" MF"v[72:75],v[20:23],a[224:227],v[72:75]\n" CV"v162,v156,v157\n" CV"v163,v158,v159\n"
 MF"v[76:79],v[20:23],a[240:243],v[76:79]\n" SA"s97,s56,24576\n" BS"v[160:163],v0,s[80:83],s97 offen offset:0 sc1\n" WL"(4)\n" MF"v[80:83],v[24:27],a[192:195],v[80:83]\n"
 TR"v[36:37],v13 offset:32768\n" TR"v[38:39],v13 offset:36864\n" BL"a[136:139],v0,s[28:31],s51 offen offset:2048 sc1\n" AR"v164,a28\n" MF"v[84:87],v[24:27],a[208:211],v[84:87]\n" AR"v165,a29\n"
 AR"v166,a30\n" MF"v[88:91],v[24:27],a[224:227],v[88:91]\n" AR"v167,a31\n" AR"v168,a60\n" MF"v[92:95],v[24:27],a[240:243],v[92:95]\n" AR"v169,a61\n" AR"v170,a62\n" WL"(4)\n"
 MF"v[96:99],v[28:31],a[192:195],v[96:99]\n" TR"v[40:41],v14 offset:32768\n" TR"v[42:43],v14 offset:36864\n" BL"a[140:143],v0,s[28:31],s51 offen offset:3072 sc1\n" AR"v171,a63\n"
 MF"v[100:103],v[28:31],a[208:211],v[100:103]\n" CV"v172,v164,v165\n" CV"v173,v166,v167\n" MF"v[104:107],v[28:31],a[224:227],v[104:107]\n" CV"v174,v168,v169\n" CV"v175,v170,v171\n"
 MF"v[108:111],v[28:31],a[240:243],v[108:111]\n" SA"s97,s56,28672\n" BS"v[172:175],v0,s[80:83],s97 offen offset:0 sc1\n" WL"(4)\n" MF"v[112:115],v[32:35],a[192:195],v[112:115]\n"
 TR"v[44:45],v15 offset:32768\n" TR"v[46:47],v15 offset:36864\n" SA"s51,s69,8192\n" BL"a[144:147],v0,s[28:31],s51 offen offset:0 sc1\n" AR"v128,a64\n" MF"v[116:119],v[32:35],a[208:211],v[116:119]\n"
 AR"v129,a65\n" AR"v130,a66\n" MF"v[120:123],v[32:35],a[224:227],v[120:123]\n" AR"v131,a67\n" AR"v132,a96\n" MF"v[124:127],v[32:35],a[240:243],v[124:127]\n" AR"v133,a97\n" AR"v134,a98\n" WL"(4)\n"
 MF"v[200:203],v[36:39],a[192:195],v[200:203]\n" TR"v[16:17],v8 offset:40960\n" TR"v[18:19],v8 offset:45056\n" BL"a[148:151],v0,s[28:31],s51 offen offset:1024 sc1\n" AR"v135,a99\n"
 MF"v[204:207],v[36:39],a[208:211],v[204:207]\n" CV"v136,v128,v129\n" CV"v137,v130,v131\n" MF"v[208:211],v[36:39],a[224:227],v[208:211]\n" CV"v138,v132,v133\n" CV"v139,v134,v135\n"
 MF"v[212:215],v[36:39],a[240:243],v[212:215]\n" SA"s97,s56,0\n" BS"v[136:139],v0,s[80:83],s97 offen offset:1024 sc1\n" WL"(4)\n" MF"v[216:219],v[40:43],a[192:195],v[216:219]\n"
 TR"v[20:21],v9 offset:40960\n" TR"v[22:23],v9 offset:45056\n" BL"a[152:155],v0,s[28:31],s51 offen offset:2048 sc1\n" AR"v140,a68\n" MF"v[220:223],v[40:43],a[208:211],v[220:223]\n" AR"v141,a69\n"
 AR"v142,a70\n" MF"v[224:227],v[40:43],a[224:227],v[224:227]\n" AR"v143,a71\n" AR"v144,a100\n" MF"v[228:231],v[40:43],a[240:243],v[228:231]\n" AR"v145,a101\n" AR"v146,a102\n" WL"(4)\n"
 MF"v[232:235],v[44:47],a[192:195],v[232:235]\n" TR"v[24:25],v10 offset:40960\n" TR"v[26:27],v10 offset:45056\n" AR"v147,a103\n" MF"v[236:239],v[44:47],a[208:211],v[236:239]\n"
 BL"a[156:159],v0,s[28:31],s51 offen offset:3072 sc1\n" CV"v148,v140,v141\n" CV"v149,v142,v143\n" MF"v[240:243],v[44:47],a[224:227],v[240:243]\n" CV"v150,v144,v145\n" CV"v151,v146,v147\n"
 MF"v[244:247],v[44:47],a[240:243],v[244:247]\n" SA"s97,s56,4096\n" BS"v[148:151],v0,s[80:83],s97 offen offset:1024 sc1\n" WL"(4)\n" MF"v[48:51],v[16:19],a[196:199],v[48:51]\n"
 TR"v[28:29],v11 offset:40960\n" TR"v[30:31],v11 offset:45056\n" AR"v152,a72\n" MF"v[52:55],v[16:19],a[212:215],v[52:55]\n" SA"s51,s70,8192\n" BL"a[160:163],v0,s[28:31],s51 offen offset:0 sc1\n"
 AR"v153,a73\n" AR"v154,a74\n" MF"v[56:59],v[16:19],a[228:231],v[56:59]\n" AR"v155,a75\n" AR"v156,a104\n" MF"v[60:63],v[16:19],a[244:247],v[60:63]\n" AR"v157,a105\n" AR"v158,a106\n" WL"(4)\n"
 MF"v[64:67],v[20:23],a[196:199],v[64:67]\n" TR"v[32:33],v12 offset:40960\n" TR"v[34:35],v12 offset:45056\n" AR"v159,a107\n" MF"v[68:71],v[20:23],a[212:215],v[68:71]\n"
 BL"a[164:167],v0,s[28:31],s51 offen offset:1024 sc1\n" CV"v160,v152,v153\n" CV"v161,v154,v155\n" MF"v[72:75],v[20:23],a[228:231],v[72:75]\n" CV"v162,v156,v157\n" CV"v163,v158,v159\n"
 MF"v[76:79],v[20:23],a[244:247],v[76:79]\n" SA"s97,s56,8192\n" BS"v[160:163],v0,s[80:83],s97 offen offset:1024 sc1\n" WL"(4)\n" MF"v[80:83],v[24:27],a[196:199],v[80:83]\n"
 TR"v[36:37],v13 offset:40960\n" TR"v[38:39],v13 offset:45056\n" MF"v[84:87],v[24:27],a[212:215],v[84:87]\n" BL"a[168:171],v0,s[28:31],s51 offen offset:2048 sc1\n"
 MF"v[88:91],v[24:27],a[228:231],v[88:91]\n" MF"v[92:95],v[24:27],a[244:247],v[92:95]\n" WL"(4)\n" MF"v[96:99],v[28:31],a[196:199],v[96:99]\n" TR"v[40:41],v14 offset:40960\n"
 TR"v[42:43],v14 offset:45056\n" MF"v[100:103],v[28:31],a[212:215],v[100:103]\n" BL"a[172:175],v0,s[28:31],s51 offen offset:3072 sc1\n" MF"v[104:107],v[28:31],a[228:231],v[104:107]\n"
 MF"v[108:111],v[28:31],a[244:247],v[108:111]\n" WL"(4)\n" MF"v[112:115],v[32:35],a[196:199],v[112:115]\n" TR"v[44:45],v15 offset:40960\n" TR"v[46:47],v15 offset:45056\n"
 MF"v[116:119],v[32:35],a[212:215],v[116:119]\n" SA"s51,s71,8192\n" BL"a[176:179],v0,s[28:31],s51 offen offset:0 sc1\n" MF"v[120:123],v[32:35],a[228:231],v[120:123]\n"
 MF"v[124:127],v[32:35],a[244:247],v[124:127]\n" WL"(4)\n" MF"v[200:203],v[36:39],a[196:199],v[200:203]\n" TR"v[16:17],v8 offset:49152\n" TR"v[18:19],v8 offset:53248\n"
 MF"v[204:207],v[36:39],a[212:215],v[204:207]\n" MF"v[208:211],v[36:39],a[228:231],v[208:211]\n" BL"a[180:183],v0,s[28:31],s51 offen offset:1024 sc1\n" MF"v[212:215],v[36:39],a[244:247],v[212:215]\n"
 WL"(4)\n" MF"v[216:219],v[40:43],a[196:199],v[216:219]\n" TR"v[20:21],v9 offset:49152\n" TR"v[22:23],v9 offset:53248\n" MF"v[220:223],v[40:43],a[212:215],v[220:223]\n"
 MF"v[224:227],v[40:43],a[228:231],v[224:227]\n" BL"a[184:187],v0,s[28:31],s51 offen offset:2048 sc1\n" MF"v[228:231],v[40:43],a[244:247],v[228:231]\n" WL"(4)\n"
 MF"v[232:235],v[44:47],a[196:199],v[232:235]\n" TR"v[24:25],v10 offset:49152\n" TR"v[26:27],v10 offset:53248\n" MF"v[236:239],v[44:47],a[212:215],v[236:239]\n"
 MF"v[240:243],v[44:47],a[228:231],v[240:243]\n" BL"a[188:191],v0,s[28:31],s51 offen offset:3072 sc1\n" MF"v[244:247],v[44:47],a[244:247],v[244:247]\n" WL"(4)\n"
 MF"v[48:51],v[16:19],a[200:203],v[48:51]\n" TR"v[28:29],v11 offset:49152\n" TR"v[30:31],v11 offset:53248\n" MF"v[52:55],v[16:19],a[216:219],v[52:55]\n" MF"v[56:59],v[16:19],a[232:235],v[56:59]\n"
 SA"m0,s61,98304\n" SA"s50,s62,3145728\n" BL"v6,s[16:19],s50 offen lds\n" MF"v[60:63],v[16:19],a[248:251],v[60:63]\n" WL"(4)\n" MF"v[64:67],v[20:23],a[200:203],v[64:67]\n"
 TR"v[32:33],v12 offset:49152\n" TR"v[34:35],v12 offset:53248\n" MF"v[68:71],v[20:23],a[216:219],v[68:71]\n" MF"v[72:75],v[20:23],a[232:235],v[72:75]\n" SA"m0,s61,99328\n" SA"s50,s62,3178496\n"
 BL"v7,s[16:19],s50 offen lds\n" MF"v[76:79],v[20:23],a[248:251],v[76:79]\n" WL"(4)\n" MF"v[80:83],v[24:27],a[200:203],v[80:83]\n" TR"v[36:37],v13 offset:49152\n" TR"v[38:39],v13 offset:53248\n"
 MF"v[84:87],v[24:27],a[216:219],v[84:87]\n" MF"v[88:91],v[24:27],a[232:235],v[88:91]\n" SA"m0,s61,100352\n" SA"s50,s62,3211264\n" BL"v6,s[16:19],s50 offen lds\n"
 MF"v[92:95],v[24:27],a[248:251],v[92:95]\n" WL"(4)\n" MF"v[96:99],v[28:31],a[200:203],v[96:99]\n" TR"v[40:41],v14 offset:49152\n" TR"v[42:43],v14 offset:53248\n"
 MF"v[100:103],v[28:31],a[216:219],v[100:103]\n" MF"v[104:107],v[28:31],a[232:235],v[104:107]\n" MF"v[108:111],v[28:31],a[248:251],v[108:111]\n" SA"m0,s61,101376\n" SA"s50,s62,3244032\n"
 BL"v7,s[16:19],s50 offen lds\n" WL"(4)\n" MF"v[112:115],v[32:35],a[200:203],v[112:115]\n" TR"v[44:45],v15 offset:49152\n" TR"v[46:47],v15 offset:53248\n"
 MF"v[116:119],v[32:35],a[216:219],v[116:119]\n" MF"v[120:123],v[32:35],a[232:235],v[120:123]\n" MF"v[124:127],v[32:35],a[248:251],v[124:127]\n" SA"m0,s61,102400\n" SA"s50,s62,3276800\n"
 BL"v6,s[16:19],s50 offen lds\n" WL"(4)\n" MF"v[200:203],v[36:39],a[200:203],v[200:203]\n" TR"v[16:17],v8 offset:57344\n" TR"v[18:19],v8 offset:61440\n" MF"v[204:207],v[36:39],a[216:219],v[204:207]\n"
 MF"v[208:211],v[36:39],a[232:235],v[208:211]\n" MF"v[212:215],v[36:39],a[248:251],v[212:215]\n" SA"m0,s61,103424\n" SA"s50,s62,3309568\n" BL"v7,s[16:19],s50 offen lds\n" WL"(4)\n"
 MF"v[216:219],v[40:43],a[200:203],v[216:219]\n" TR"v[20:21],v9 offset:57344\n" TR"v[22:23],v9 offset:61440\n" MF"v[220:223],v[40:43],a[216:219],v[220:223]\n"
 MF"v[224:227],v[40:43],a[232:235],v[224:227]\n" MF"v[228:231],v[40:43],a[248:251],v[228:231]\n" SA"m0,s61,104448\n" SA"s50,s62,3342336\n" BL"v6,s[16:19],s50 offen lds\n" WL"(4)\n"
 MF"v[232:235],v[44:47],a[200:203],v[232:235]\n" TR"v[24:25],v10 offset:57344\n" TR"v[26:27],v10 offset:61440\n" MF"v[236:239],v[44:47],a[216:219],v[236:239]\n"
 MF"v[240:243],v[44:47],a[232:235],v[240:243]\n" MF"v[244:247],v[44:47],a[248:251],v[244:247]\n" SA"m0,s61,105472\n" SA"s50,s62,3375104\n" BL"v7,s[16:19],s50 offen lds\n" WL"(4)\n"
 MF"v[48:51],v[16:19],a[204:207],v[48:51]\n" TR"v[28:29],v11 offset:57344\n" TR"v[30:31],v11 offset:61440\n" MF"v[52:55],v[16:19],a[220:223],v[52:55]\n" MF"v[56:59],v[16:19],a[236:239],v[56:59]\n"
 MF"v[60:63],v[16:19],a[252:255],v[60:63]\n""v_mbcnt_lo_u32_b32 v251,-1,0\n""v_mbcnt_hi_u32_b32 v251,-1,v251\n""v_min_u32 v251,24,v251\n""s_lshr_b32 s38,s96,5\n""s_and_b32 s39,s96,7\n"
"s_lshl_b32 s39,s39,2\n""s_bfe_u32 s57,s96,0x20003\n" SA"s39,s39,s57\n""s_lshl_b32 s42,s39,2\n" SA"s42,s42,s36\n""s_sub_u32 s57,s39,1\n""s_max_i32 s57,s57,0\n""s_min_u32 s57,s57,29\n"
"s_lshl_b32 s57,s57,6\n" SA"s57,s57,12288\n" VV"v248,s57\n""s_mov_b32 s57,4096\n""s_sub_u32 s57,s57,192\n" VV"v249,s57\n""v_cmp_le_u32 vcc,24,v251\n""s_nop 1\n""v_cndmask_b32 v248,v248,v249,vcc\n"
"v_lshl_add_u32 v248,v251,3,v248\n""buffer_load_dwordx2 v[252:253],v248,s[92:95],0 offen sc1\n" WL"(4)\n" MF"v[64:67],v[20:23],a[204:207],v[64:67]\n" TR"v[32:33],v12 offset:57344\n"
 TR"v[34:35],v12 offset:61440\n" MF"v[68:71],v[20:23],a[220:223],v[68:71]\n" MF"v[72:75],v[20:23],a[236:239],v[72:75]\n" MF"v[76:79],v[20:23],a[252:255],v[76:79]\n" WL"(4)\n"
 MF"v[80:83],v[24:27],a[204:207],v[80:83]\n" TR"v[36:37],v13 offset:57344\n" TR"v[38:39],v13 offset:61440\n" MF"v[84:87],v[24:27],a[220:223],v[84:87]\n" TR"v[40:41],v14 offset:57344\n"
 TR"v[42:43],v14 offset:61440\n" MF"v[88:91],v[24:27],a[236:239],v[88:91]\n" TR"v[44:45],v15 offset:57344\n" TR"v[46:47],v15 offset:61440\n" MF"v[92:95],v[24:27],a[252:255],v[92:95]\n" WL"(8)\n"
 MF"v[96:99],v[28:31],a[204:207],v[96:99]\n" MF"v[100:103],v[28:31],a[220:223],v[100:103]\n" MF"v[104:107],v[28:31],a[236:239],v[104:107]\n" MF"v[108:111],v[28:31],a[252:255],v[108:111]\n" WL"(6)\n"
 MF"v[112:115],v[32:35],a[204:207],v[112:115]\n" WV"(0) lgkmcnt(0)\n""s_barrier\n""v_add_u32 v8,0x10000,v8\n""v_add_u32 v9,0x10000,v9\n""v_add_u32 v10,0x10000,v10\n""v_add_u32 v11,0x10000,v11\n"
"v_add_u32 v12,0x10000,v12\n""v_add_u32 v13,0x10000,v13\n""v_add_u32 v14,0x10000,v14\n""v_add_u32 v15,0x10000,v15\n""v_cmp_ne_u64 vcc,s[98:99],v[252:253]\n""s_nop 1\n""s_cmp_eq_u64 vcc,0\n"
"s_cbranch_scc1 Lokp2_%=\n""s_mov_b32 s57,32768\n""Lspp2_%=:\n""s_sleep 2\n""buffer_load_dwordx2 v[252:253],v248,s[92:95],0 offen sc1\n" WV"(0)\n""v_cmp_ne_u64 vcc,s[98:99],v[252:253]\n""s_nop 1\n"
"s_cmp_eq_u64 vcc,0\n""s_cbranch_scc1 Lokp2_%=\n""s_sub_u32 s57,s57,1\n""s_cmp_lg_u32 s57,0\n""s_cbranch_scc1 Lspp2_%=\n""Lokp2_%=:\n" MF"v[116:119],v[32:35],a[220:223],v[116:119]\n"
 MF"v[120:123],v[32:35],a[236:239],v[120:123]\n" MF"v[124:127],v[32:35],a[252:255],v[124:127]\n" MF"v[200:203],v[36:39],a[204:207],v[200:203]\n" TR"v[16:17],v8 offset:0\n"
 TR"v[18:19],v8 offset:4096\n" MF"v[204:207],v[36:39],a[220:223],v[204:207]\n" MF"v[208:211],v[36:39],a[236:239],v[208:211]\n" MF"v[212:215],v[36:39],a[252:255],v[212:215]\n"
 MF"v[216:219],v[40:43],a[204:207],v[216:219]\n" TR"v[20:21],v9 offset:0\n" TR"v[22:23],v9 offset:4096\n" MF"v[220:223],v[40:43],a[220:223],v[220:223]\n"
 MF"v[224:227],v[40:43],a[236:239],v[224:227]\n" MF"v[228:231],v[40:43],a[252:255],v[228:231]\n" MF"v[232:235],v[44:47],a[204:207],v[232:235]\n" TR"v[24:25],v10 offset:0\n"
 TR"v[26:27],v10 offset:4096\n" MF"v[236:239],v[44:47],a[220:223],v[236:239]\n" MF"v[240:243],v[44:47],a[236:239],v[240:243]\n" MF"v[244:247],v[44:47],a[252:255],v[244:247]\n" WL"(4)\n"
 MF"v[48:51],v[16:19],a[128:131],v[48:51]\n" TR"v[28:29],v11 offset:0\n" TR"v[30:31],v11 offset:4096\n" SA"s51,s68,12288\n" BL"a[192:195],v0,s[28:31],s51 offen offset:0 sc1\n" AR"v164,a76\n"
 MF"v[52:55],v[16:19],a[144:147],v[52:55]\n" AR"v165,a77\n" AR"v166,a78\n" MF"v[56:59],v[16:19],a[160:163],v[56:59]\n" AR"v167,a79\n" AR"v168,a108\n" MF"v[60:63],v[16:19],a[176:179],v[60:63]\n"
 AR"v169,a109\n" AR"v170,a110\n" WL"(4)\n" MF"v[64:67],v[20:23],a[128:131],v[64:67]\n" TR"v[32:33],v12 offset:0\n" TR"v[34:35],v12 offset:4096\n" AR"v171,a111\n"
 MF"v[68:71],v[20:23],a[144:147],v[68:71]\n" CV"v172,v164,v165\n" CV"v173,v166,v167\n" MF"v[72:75],v[20:23],a[160:163],v[72:75]\n" BL"a[196:199],v0,s[28:31],s51 offen offset:1024 sc1\n"
 CV"v174,v168,v169\n" CV"v175,v170,v171\n" MF"v[76:79],v[20:23],a[176:179],v[76:79]\n" SA"s97,s56,12288\n" BS"v[172:175],v0,s[80:83],s97 offen offset:1024 sc1\n" WL"(4)\n"
 MF"v[80:83],v[24:27],a[128:131],v[80:83]\n" TR"v[36:37],v13 offset:0\n" TR"v[38:39],v13 offset:4096\n" AR"v128,a80\n" MF"v[84:87],v[24:27],a[144:147],v[84:87]\n" AR"v129,a81\n" AR"v130,a82\n"
 MF"v[88:91],v[24:27],a[160:163],v[88:91]\n" AR"v131,a83\n" AR"v132,a112\n" MF"v[92:95],v[24:27],a[176:179],v[92:95]\n" AR"v133,a113\n" AR"v134,a114\n" WL"(4)\n"
 MF"v[96:99],v[28:31],a[128:131],v[96:99]\n" TR"v[40:41],v14 offset:0\n" TR"v[42:43],v14 offset:4096\n" AR"v135,a115\n" MF"v[100:103],v[28:31],a[144:147],v[100:103]\n"
 BL"a[200:203],v0,s[28:31],s51 offen offset:2048 sc1\n" CV"v136,v128,v129\n" CV"v137,v130,v131\n" MF"v[104:107],v[28:31],a[160:163],v[104:107]\n" CV"v138,v132,v133\n" CV"v139,v134,v135\n"
 MF"v[108:111],v[28:31],a[176:179],v[108:111]\n" SA"s97,s56,16384\n" BS"v[136:139],v0,s[80:83],s97 offen offset:1024 sc1\n" WL"(4)\n" MF"v[112:115],v[32:35],a[128:131],v[112:115]\n"
 TR"v[44:45],v15 offset:0\n" TR"v[46:47],v15 offset:4096\n" AR"v140,a84\n" MF"v[116:119],v[32:35],a[144:147],v[116:119]\n" AR"v141,a85\n" AR"v142,a86\n" MF"v[120:123],v[32:35],a[160:163],v[120:123]\n"
 AR"v143,a87\n" AR"v144,a116\n" MF"v[124:127],v[32:35],a[176:179],v[124:127]\n" BL"a[204:207],v0,s[28:31],s51 offen offset:3072 sc1\n" AR"v145,a117\n" AR"v146,a118\n" WL"(4)\n"
 MF"v[200:203],v[36:39],a[128:131],v[200:203]\n" TR"v[16:17],v8 offset:8192\n" TR"v[18:19],v8 offset:12288\n" AR"v147,a119\n" MF"v[204:207],v[36:39],a[144:147],v[204:207]\n" CV"v148,v140,v141\n"
 CV"v149,v142,v143\n" MF"v[208:211],v[36:39],a[160:163],v[208:211]\n" CV"v150,v144,v145\n" CV"v151,v146,v147\n" MF"v[212:215],v[36:39],a[176:179],v[212:215]\n" SA"s97,s56,20480\n"
 BS"v[148:151],v0,s[80:83],s97 offen offset:1024 sc1\n" WL"(4)\n" MF"v[216:219],v[40:43],a[128:131],v[216:219]\n" TR"v[20:21],v9 offset:8192\n" TR"v[22:23],v9 offset:12288\n" AR"v152,a88\n"
 MF"v[220:223],v[40:43],a[144:147],v[220:223]\n" AR"v153,a89\n" AR"v154,a90\n" MF"v[224:227],v[40:43],a[160:163],v[224:227]\n" SA"s51,s69,12288\n" BL"a[208:211],v0,s[28:31],s51 offen offset:0 sc1\n"
 AR"v155,a91\n" AR"v156,a120\n" MF"v[228:231],v[40:43],a[176:179],v[228:231]\n" AR"v157,a121\n" AR"v158,a122\n" WL"(4)\n" MF"v[232:235],v[44:47],a[128:131],v[232:235]\n" TR"v[24:25],v10 offset:8192\n"
 TR"v[26:27],v10 offset:12288\n" AR"v159,a123\n" MF"v[236:239],v[44:47],a[144:147],v[236:239]\n" CV"v160,v152,v153\n" CV"v161,v154,v155\n" MF"v[240:243],v[44:47],a[160:163],v[240:243]\n"
 CV"v162,v156,v157\n" CV"v163,v158,v159\n" MF"v[244:247],v[44:47],a[176:179],v[244:247]\n" SA"s97,s56,24576\n" BS"v[160:163],v0,s[80:83],s97 offen offset:1024 sc1\n" WL"(4)\n"
 MF"v[48:51],v[16:19],a[132:135],v[48:51]\n" TR"v[28:29],v11 offset:8192\n" TR"v[30:31],v11 offset:12288\n" BL"a[212:215],v0,s[28:31],s51 offen offset:1024 sc1\n" AR"v164,a92\n"
 MF"v[52:55],v[16:19],a[148:151],v[52:55]\n" AR"v165,a93\n" AR"v166,a94\n" MF"v[56:59],v[16:19],a[164:167],v[56:59]\n" AR"v167,a95\n" AR"v168,a124\n" MF"v[60:63],v[16:19],a[180:183],v[60:63]\n"
 AR"v169,a125\n" AR"v170,a126\n" WL"(4)\n" MF"v[64:67],v[20:23],a[132:135],v[64:67]\n" TR"v[32:33],v12 offset:8192\n" TR"v[34:35],v12 offset:12288\n" AR"v171,a127\n"
 MF"v[68:71],v[20:23],a[148:151],v[68:71]\n" CV"v172,v164,v165\n" CV"v173,v166,v167\n" MF"v[72:75],v[20:23],a[164:167],v[72:75]\n" CV"v174,v168,v169\n" CV"v175,v170,v171\n"
 MF"v[76:79],v[20:23],a[180:183],v[76:79]\n" BL"a[216:219],v0,s[28:31],s51 offen offset:2048 sc1\n" SA"s97,s56,28672\n" BS"v[172:175],v0,s[80:83],s97 offen offset:1024 sc1\n" WL"(4)\n"
 MF"v[80:83],v[24:27],a[132:135],v[80:83]\n" TR"v[36:37],v13 offset:8192\n" TR"v[38:39],v13 offset:12288\n" MF"v[84:87],v[24:27],a[148:151],v[84:87]\n" MF"v[88:91],v[24:27],a[164:167],v[88:91]\n"
 MF"v[92:95],v[24:27],a[180:183],v[92:95]\n" WL"(4)\n" MF"v[96:99],v[28:31],a[132:135],v[96:99]\n" TR"v[40:41],v14 offset:8192\n" TR"v[42:43],v14 offset:12288\n"
 MF"v[100:103],v[28:31],a[148:151],v[100:103]\n" BL"a[220:223],v0,s[28:31],s51 offen offset:3072 sc1\n" MF"v[104:107],v[28:31],a[164:167],v[104:107]\n" MF"v[108:111],v[28:31],a[180:183],v[108:111]\n"
 WL"(4)\n" MF"v[112:115],v[32:35],a[132:135],v[112:115]\n" TR"v[44:45],v15 offset:8192\n" TR"v[46:47],v15 offset:12288\n" MF"v[116:119],v[32:35],a[148:151],v[116:119]\n"
 MF"v[120:123],v[32:35],a[164:167],v[120:123]\n" MF"v[124:127],v[32:35],a[180:183],v[124:127]\n" WL"(4)\n" MF"v[200:203],v[36:39],a[132:135],v[200:203]\n" TR"v[16:17],v8 offset:16384\n"
 TR"v[18:19],v8 offset:20480\n" SA"s51,s70,12288\n" BL"a[224:227],v0,s[28:31],s51 offen offset:0 sc1\n" MF"v[204:207],v[36:39],a[148:151],v[204:207]\n" MF"v[208:211],v[36:39],a[164:167],v[208:211]\n"
 MF"v[212:215],v[36:39],a[180:183],v[212:215]\n" WL"(4)\n" MF"v[216:219],v[40:43],a[132:135],v[216:219]\n" TR"v[20:21],v9 offset:16384\n" TR"v[22:23],v9 offset:20480\n"
 MF"v[220:223],v[40:43],a[148:151],v[220:223]\n" MF"v[224:227],v[40:43],a[164:167],v[224:227]\n" BL"a[228:231],v0,s[28:31],s51 offen offset:1024 sc1\n" MF"v[228:231],v[40:43],a[180:183],v[228:231]\n"
 WL"(4)\n" MF"v[232:235],v[44:47],a[132:135],v[232:235]\n" TR"v[24:25],v10 offset:16384\n" TR"v[26:27],v10 offset:20480\n" MF"v[236:239],v[44:47],a[148:151],v[236:239]\n"
 MF"v[240:243],v[44:47],a[164:167],v[240:243]\n" MF"v[244:247],v[44:47],a[180:183],v[244:247]\n" WL"(4)\n" MF"v[48:51],v[16:19],a[136:139],v[48:51]\n" TR"v[28:29],v11 offset:16384\n"
 TR"v[30:31],v11 offset:20480\n" MF"v[52:55],v[16:19],a[152:155],v[52:55]\n" BL"a[232:235],v0,s[28:31],s51 offen offset:2048 sc1\n" MF"v[56:59],v[16:19],a[168:171],v[56:59]\n"
 MF"v[60:63],v[16:19],a[184:187],v[60:63]\n" WL"(4)\n" MF"v[64:67],v[20:23],a[136:139],v[64:67]\n" TR"v[32:33],v12 offset:16384\n" TR"v[34:35],v12 offset:20480\n"
 MF"v[68:71],v[20:23],a[152:155],v[68:71]\n" MF"v[72:75],v[20:23],a[168:171],v[72:75]\n" MF"v[76:79],v[20:23],a[184:187],v[76:79]\n" BL"a[236:239],v0,s[28:31],s51 offen offset:3072 sc1\n" WL"(4)\n"
 MF"v[80:83],v[24:27],a[136:139],v[80:83]\n" TR"v[36:37],v13 offset:16384\n" TR"v[38:39],v13 offset:20480\n" MF"v[84:87],v[24:27],a[152:155],v[84:87]\n" MF"v[88:91],v[24:27],a[168:171],v[88:91]\n"
 MF"v[92:95],v[24:27],a[184:187],v[92:95]\n" WL"(4)\n" MF"v[96:99],v[28:31],a[136:139],v[96:99]\n" TR"v[40:41],v14 offset:16384\n" TR"v[42:43],v14 offset:20480\n"
 MF"v[100:103],v[28:31],a[152:155],v[100:103]\n" MF"v[104:107],v[28:31],a[168:171],v[104:107]\n" SA"s51,s71,12288\n" BL"a[240:243],v0,s[28:31],s51 offen offset:0 sc1\n"
 MF"v[108:111],v[28:31],a[184:187],v[108:111]\n" WL"(4)\n" MF"v[112:115],v[32:35],a[136:139],v[112:115]\n" TR"v[44:45],v15 offset:16384\n" TR"v[46:47],v15 offset:20480\n"
 MF"v[116:119],v[32:35],a[152:155],v[116:119]\n" MF"v[120:123],v[32:35],a[168:171],v[120:123]\n" MF"v[124:127],v[32:35],a[184:187],v[124:127]\n" WL"(4)\n"
 MF"v[200:203],v[36:39],a[136:139],v[200:203]\n" TR"v[16:17],v8 offset:24576\n" TR"v[18:19],v8 offset:28672\n" BL"a[244:247],v0,s[28:31],s51 offen offset:1024 sc1\n"
 MF"v[204:207],v[36:39],a[152:155],v[204:207]\n" MF"v[208:211],v[36:39],a[168:171],v[208:211]\n" MF"v[212:215],v[36:39],a[184:187],v[212:215]\n" WL"(4)\n"
 MF"v[216:219],v[40:43],a[136:139],v[216:219]\n" TR"v[20:21],v9 offset:24576\n" TR"v[22:23],v9 offset:28672\n" MF"v[220:223],v[40:43],a[152:155],v[220:223]\n"
 MF"v[224:227],v[40:43],a[168:171],v[224:227]\n" MF"v[228:231],v[40:43],a[184:187],v[228:231]\n" BL"a[248:251],v0,s[28:31],s51 offen offset:2048 sc1\n" WL"(4)\n"
 MF"v[232:235],v[44:47],a[136:139],v[232:235]\n" TR"v[24:25],v10 offset:24576\n" TR"v[26:27],v10 offset:28672\n" MF"v[236:239],v[44:47],a[152:155],v[236:239]\n"
 MF"v[240:243],v[44:47],a[168:171],v[240:243]\n" MF"v[244:247],v[44:47],a[184:187],v[244:247]\n" WL"(4)\n" MF"v[48:51],v[16:19],a[140:143],v[48:51]\n" TR"v[28:29],v11 offset:24576\n"
 TR"v[30:31],v11 offset:28672\n" MF"v[52:55],v[16:19],a[156:159],v[52:55]\n" BL"a[252:255],v0,s[28:31],s51 offen offset:3072 sc1\n" MF"v[56:59],v[16:19],a[172:175],v[56:59]\n"
 MF"v[60:63],v[16:19],a[188:191],v[60:63]\n" WL"(4)\n" MF"v[64:67],v[20:23],a[140:143],v[64:67]\n" TR"v[32:33],v12 offset:24576\n" TR"v[34:35],v12 offset:28672\n"
 MF"v[68:71],v[20:23],a[156:159],v[68:71]\n" MF"v[72:75],v[20:23],a[172:175],v[72:75]\n" MF"v[76:79],v[20:23],a[188:191],v[76:79]\n" WL"(4)\n" MF"v[80:83],v[24:27],a[140:143],v[80:83]\n"
 TR"v[36:37],v13 offset:24576\n" TR"v[38:39],v13 offset:28672\n" MF"v[84:87],v[24:27],a[156:159],v[84:87]\n" TR"v[40:41],v14 offset:24576\n" TR"v[42:43],v14 offset:28672\n"
 MF"v[88:91],v[24:27],a[172:175],v[88:91]\n" TR"v[44:45],v15 offset:24576\n" TR"v[46:47],v15 offset:28672\n" MF"v[92:95],v[24:27],a[188:191],v[92:95]\n" WL"(8)\n"
 MF"v[96:99],v[28:31],a[140:143],v[96:99]\n" MF"v[100:103],v[28:31],a[156:159],v[100:103]\n" MF"v[104:107],v[28:31],a[172:175],v[104:107]\n" MF"v[108:111],v[28:31],a[188:191],v[108:111]\n" WL"(6)\n"
 MF"v[112:115],v[32:35],a[140:143],v[112:115]\n" WV"(0) lgkmcnt(0)\n""s_barrier\n""s_lshl_b32 s57,s37,3\n" SA"s57,s57,s47\n""s_lshl_b32 s57,s57,3\n" SA"s57,s57,8192\n" VV"v250,s98\n" VV"v251,s99\n"
 VV"v252,0\n""buffer_store_dwordx2 v[250:251],v252,s[92:95],s57 offen sc1\n" MF"v[116:119],v[32:35],a[156:159],v[116:119]\n" MF"v[120:123],v[32:35],a[172:175],v[120:123]\n"
 MF"v[124:127],v[32:35],a[188:191],v[124:127]\n" MF"v[200:203],v[36:39],a[140:143],v[200:203]\n" TR"v[16:17],v8 offset:32768\n" TR"v[18:19],v8 offset:36864\n"
 MF"v[204:207],v[36:39],a[156:159],v[204:207]\n" MF"v[208:211],v[36:39],a[172:175],v[208:211]\n" MF"v[212:215],v[36:39],a[188:191],v[212:215]\n" MF"v[216:219],v[40:43],a[140:143],v[216:219]\n"
 TR"v[20:21],v9 offset:32768\n" TR"v[22:23],v9 offset:36864\n" MF"v[220:223],v[40:43],a[156:159],v[220:223]\n" MF"v[224:227],v[40:43],a[172:175],v[224:227]\n"
 MF"v[228:231],v[40:43],a[188:191],v[228:231]\n" MF"v[232:235],v[44:47],a[140:143],v[232:235]\n" TR"v[24:25],v10 offset:32768\n" TR"v[26:27],v10 offset:36864\n"
 MF"v[236:239],v[44:47],a[156:159],v[236:239]\n" MF"v[240:243],v[44:47],a[172:175],v[240:243]\n" MF"v[244:247],v[44:47],a[188:191],v[244:247]\n" WL"(4)\n" MF"v[48:51],v[16:19],a[192:195],v[48:51]\n"
 TR"v[28:29],v11 offset:32768\n" TR"v[30:31],v11 offset:36864\n""v_mbcnt_lo_u32_b32 v250,-1,0\n""v_mbcnt_hi_u32_b32 v250,-1,v250\n""v_lshlrev_b32 v0,4,v250\n""v_lshrrev_b32 v251,3,v250\n"
"v_mul_u32_u24 v251,1024,v251\n""v_lshrrev_b32 v252,4,v250\n""v_lshlrev_b32 v253,1,v252\n""v_and_b32 v2,7,v250\n""v_xor_b32 v253,v253,v2\n""v_lshl_add_u32 v2,v253,4,v251\n"
"v_mul_u32_u24 v251,2056,v252\n""v_and_b32 v253,15,v250\n""v_add_u32 v251,v251,v253\n""v_lshlrev_b32 v3,4,v251\n""s_lshl_b32 s63,s36,12\n""s_lshr_b32 s38,s96,5\n""s_and_b32 s39,s96,7\n"
"s_lshl_b32 s39,s39,2\n""s_bfe_u32 s57,s96,0x20003\n" SA"s39,s39,s57\n""s_lshl_b32 s42,s39,2\n" SA"s42,s42,s36\n""v_readlane_b32 s72,v254,14\n""v_readlane_b32 s73,v254,15\n""s_mov_b32 s74,524288\n"
"s_mov_b32 s75,0x20000\n" SA"s76,s52,11534336\n""s_addc_u32 s77,s53,0\n""s_mov_b32 s78,2105344\n""s_mov_b32 s79,0x20000\n""s_lshl_b32 s44,s36,15\n""s_lshl_b32 s57,s38,7\n" SA"s44,s44,s57\n"
"s_lshl_b32 s45,s42,8\n" MF"v[52:55],v[16:19],a[208:211],v[52:55]\n" MF"v[56:59],v[16:19],a[224:227],v[56:59]\n" SA"m0,s63,65536\n" SA"s58,s44,0\n" BL"v2,s[72:75],s58 offen lds\n"
 MF"v[60:63],v[16:19],a[240:243],v[60:63]\n" WL"(4)\n" MF"v[64:67],v[20:23],a[192:195],v[64:67]\n" TR"v[32:33],v12 offset:32768\n" TR"v[34:35],v12 offset:36864\n"
 MF"v[68:71],v[20:23],a[208:211],v[68:71]\n" SA"m0,s63,66560\n" SA"s58,s44,8192\n" BL"v2,s[72:75],s58 offen lds\n" MF"v[72:75],v[20:23],a[224:227],v[72:75]\n"
 MF"v[76:79],v[20:23],a[240:243],v[76:79]\n" SA"m0,s63,67584\n" SA"s58,s44,16384\n" BL"v2,s[72:75],s58 offen lds\n" WL"(4)\n" MF"v[80:83],v[24:27],a[192:195],v[80:83]\n"
 TR"v[36:37],v13 offset:32768\n" TR"v[38:39],v13 offset:36864\n" MF"v[84:87],v[24:27],a[208:211],v[84:87]\n" MF"v[88:91],v[24:27],a[224:227],v[88:91]\n" SA"m0,s63,68608\n" SA"s58,s44,24576\n"
 BL"v2,s[72:75],s58 offen lds\n" MF"v[92:95],v[24:27],a[240:243],v[92:95]\n" WL"(4)\n" MF"v[96:99],v[28:31],a[192:195],v[96:99]\n" TR"v[40:41],v14 offset:32768\n" TR"v[42:43],v14 offset:36864\n"
 MF"v[100:103],v[28:31],a[208:211],v[100:103]\n" SA"m0,s63,81920\n" SA"s58,s44,131072\n" BL"v2,s[72:75],s58 offen lds\n" MF"v[104:107],v[28:31],a[224:227],v[104:107]\n"
 MF"v[108:111],v[28:31],a[240:243],v[108:111]\n" SA"m0,s63,82944\n" SA"s58,s44,139264\n" BL"v2,s[72:75],s58 offen lds\n" WL"(4)\n" MF"v[112:115],v[32:35],a[192:195],v[112:115]\n"
 TR"v[44:45],v15 offset:32768\n" TR"v[46:47],v15 offset:36864\n" MF"v[116:119],v[32:35],a[208:211],v[116:119]\n" MF"v[120:123],v[32:35],a[224:227],v[120:123]\n" SA"m0,s63,83968\n" SA"s58,s44,147456\n"
 BL"v2,s[72:75],s58 offen lds\n" MF"v[124:127],v[32:35],a[240:243],v[124:127]\n" WL"(4)\n" MF"v[200:203],v[36:39],a[192:195],v[200:203]\n" TR"v[16:17],v8 offset:40960\n" TR"v[18:19],v8 offset:45056\n"
 MF"v[204:207],v[36:39],a[208:211],v[204:207]\n" SA"m0,s63,84992\n" SA"s58,s44,155648\n" BL"v2,s[72:75],s58 offen lds\n" MF"v[208:211],v[36:39],a[224:227],v[208:211]\n"
 MF"v[212:215],v[36:39],a[240:243],v[212:215]\n" SA"m0,s63,98304\n" SA"s58,s44,262144\n" BL"v2,s[72:75],s58 offen lds\n" WL"(4)\n" MF"v[216:219],v[40:43],a[192:195],v[216:219]\n"
 TR"v[20:21],v9 offset:40960\n" TR"v[22:23],v9 offset:45056\n" MF"v[220:223],v[40:43],a[208:211],v[220:223]\n" MF"v[224:227],v[40:43],a[224:227],v[224:227]\n" SA"m0,s63,99328\n" SA"s58,s44,270336\n"
 BL"v2,s[72:75],s58 offen lds\n" MF"v[228:231],v[40:43],a[240:243],v[228:231]\n" WL"(4)\n" MF"v[232:235],v[44:47],a[192:195],v[232:235]\n" TR"v[24:25],v10 offset:40960\n"
 TR"v[26:27],v10 offset:45056\n" MF"v[236:239],v[44:47],a[208:211],v[236:239]\n" SA"m0,s63,100352\n" SA"s58,s44,278528\n" BL"v2,s[72:75],s58 offen lds\n"
 MF"v[240:243],v[44:47],a[224:227],v[240:243]\n" MF"v[244:247],v[44:47],a[240:243],v[244:247]\n" SA"m0,s63,101376\n" SA"s58,s44,286720\n" BL"v2,s[72:75],s58 offen lds\n" WL"(4)\n"
 MF"v[48:51],v[16:19],a[196:199],v[48:51]\n" TR"v[28:29],v11 offset:40960\n" TR"v[30:31],v11 offset:45056\n" MF"v[52:55],v[16:19],a[212:215],v[52:55]\n" MF"v[56:59],v[16:19],a[228:231],v[56:59]\n"
 SA"m0,s63,114688\n" SA"s58,s44,393216\n" BL"v2,s[72:75],s58 offen lds\n" MF"v[60:63],v[16:19],a[244:247],v[60:63]\n" WL"(4)\n" MF"v[64:67],v[20:23],a[196:199],v[64:67]\n"
 TR"v[32:33],v12 offset:40960\n" TR"v[34:35],v12 offset:45056\n" MF"v[68:71],v[20:23],a[212:215],v[68:71]\n" SA"m0,s63,115712\n" SA"s58,s44,401408\n" BL"v2,s[72:75],s58 offen lds\n"
 MF"v[72:75],v[20:23],a[228:231],v[72:75]\n" MF"v[76:79],v[20:23],a[244:247],v[76:79]\n" SA"m0,s63,116736\n" SA"s58,s44,409600\n" BL"v2,s[72:75],s58 offen lds\n" WL"(4)\n"
 MF"v[80:83],v[24:27],a[196:199],v[80:83]\n" TR"v[36:37],v13 offset:40960\n" TR"v[38:39],v13 offset:45056\n" MF"v[84:87],v[24:27],a[212:215],v[84:87]\n" MF"v[88:91],v[24:27],a[228:231],v[88:91]\n"
 SA"m0,s63,117760\n" SA"s58,s44,417792\n" BL"v2,s[72:75],s58 offen lds\n" MF"v[92:95],v[24:27],a[244:247],v[92:95]\n" WL"(4)\n" MF"v[96:99],v[28:31],a[196:199],v[96:99]\n"
 TR"v[40:41],v14 offset:40960\n" TR"v[42:43],v14 offset:45056\n" MF"v[100:103],v[28:31],a[212:215],v[100:103]\n" SA"s59,s45,16\n" BL"a[0:3],v3,s[76:79],s59 offen offset:0 sc1\n"
 MF"v[104:107],v[28:31],a[228:231],v[104:107]\n" MF"v[108:111],v[28:31],a[244:247],v[108:111]\n" SA"s59,s45,131600\n" BL"a[4:7],v3,s[76:79],s59 offen offset:0 sc1\n" WL"(4)\n"
 MF"v[112:115],v[32:35],a[196:199],v[112:115]\n" TR"v[44:45],v15 offset:40960\n" TR"v[46:47],v15 offset:45056\n" MF"v[116:119],v[32:35],a[212:215],v[116:119]\n"
 MF"v[120:123],v[32:35],a[228:231],v[120:123]\n" SA"s59,s45,263184\n" BL"a[8:11],v3,s[76:79],s59 offen offset:0 sc1\n" MF"v[124:127],v[32:35],a[244:247],v[124:127]\n" WL"(4)\n"
 MF"v[200:203],v[36:39],a[196:199],v[200:203]\n" TR"v[16:17],v8 offset:49152\n" TR"v[18:19],v8 offset:53248\n" SA"s59,s45,394768\n" BL"a[12:15],v3,s[76:79],s59 offen offset:0 sc1\n"
 MF"v[204:207],v[36:39],a[212:215],v[204:207]\n" MF"v[208:211],v[36:39],a[228:231],v[208:211]\n" MF"v[212:215],v[36:39],a[244:247],v[212:215]\n" SA"s59,s45,526384\n"
 BL"a[16:19],v3,s[76:79],s59 offen offset:0 sc1\n" WL"(4)\n" MF"v[216:219],v[40:43],a[196:199],v[216:219]\n" TR"v[20:21],v9 offset:49152\n" TR"v[22:23],v9 offset:53248\n"
 MF"v[220:223],v[40:43],a[212:215],v[220:223]\n" MF"v[224:227],v[40:43],a[228:231],v[224:227]\n" SA"s59,s45,657968\n" BL"a[20:23],v3,s[76:79],s59 offen offset:0 sc1\n"
 MF"v[228:231],v[40:43],a[244:247],v[228:231]\n" WL"(4)\n" MF"v[232:235],v[44:47],a[196:199],v[232:235]\n" TR"v[24:25],v10 offset:49152\n" TR"v[26:27],v10 offset:53248\n" SA"s59,s45,789552\n"
 BL"a[24:27],v3,s[76:79],s59 offen offset:0 sc1\n" MF"v[236:239],v[44:47],a[212:215],v[236:239]\n" MF"v[240:243],v[44:47],a[228:231],v[240:243]\n" MF"v[244:247],v[44:47],a[244:247],v[244:247]\n"
 SA"s59,s45,921136\n" BL"a[28:31],v3,s[76:79],s59 offen offset:0 sc1\n" WL"(4)\n" MF"v[48:51],v[16:19],a[200:203],v[48:51]\n" TR"v[28:29],v11 offset:49152\n" TR"v[30:31],v11 offset:53248\n"
 MF"v[52:55],v[16:19],a[216:219],v[52:55]\n" MF"v[56:59],v[16:19],a[232:235],v[56:59]\n" SA"s59,s45,1052672\n" BL"a[32:35],v3,s[76:79],s59 offen offset:0 sc1\n"
 MF"v[60:63],v[16:19],a[248:251],v[60:63]\n" WL"(4)\n" MF"v[64:67],v[20:23],a[200:203],v[64:67]\n" TR"v[32:33],v12 offset:49152\n" TR"v[34:35],v12 offset:53248\n" SA"s59,s45,1184256\n"
 BL"a[36:39],v3,s[76:79],s59 offen offset:0 sc1\n" MF"v[68:71],v[20:23],a[216:219],v[68:71]\n" MF"v[72:75],v[20:23],a[232:235],v[72:75]\n" MF"v[76:79],v[20:23],a[248:251],v[76:79]\n"
 SA"s59,s45,1315840\n" BL"a[40:43],v3,s[76:79],s59 offen offset:0 sc1\n" WL"(4)\n" MF"v[80:83],v[24:27],a[200:203],v[80:83]\n" TR"v[36:37],v13 offset:49152\n" TR"v[38:39],v13 offset:53248\n"
 MF"v[84:87],v[24:27],a[216:219],v[84:87]\n" MF"v[88:91],v[24:27],a[232:235],v[88:91]\n" SA"s59,s45,1447424\n" BL"a[44:47],v3,s[76:79],s59 offen offset:0 sc1\n"
 MF"v[92:95],v[24:27],a[248:251],v[92:95]\n" WL"(4)\n" MF"v[96:99],v[28:31],a[200:203],v[96:99]\n" TR"v[40:41],v14 offset:49152\n" TR"v[42:43],v14 offset:53248\n" SA"s59,s45,1579072\n"
 BL"a[48:51],v3,s[76:79],s59 offen offset:0 sc1\n" MF"v[100:103],v[28:31],a[216:219],v[100:103]\n" MF"v[104:107],v[28:31],a[232:235],v[104:107]\n" MF"v[108:111],v[28:31],a[248:251],v[108:111]\n"
 SA"s59,s45,1710656\n" BL"a[52:55],v3,s[76:79],s59 offen offset:0 sc1\n" WL"(4)\n" MF"v[112:115],v[32:35],a[200:203],v[112:115]\n" TR"v[44:45],v15 offset:49152\n" TR"v[46:47],v15 offset:53248\n"
 MF"v[116:119],v[32:35],a[216:219],v[116:119]\n" MF"v[120:123],v[32:35],a[232:235],v[120:123]\n" SA"s59,s45,1842240\n" BL"a[56:59],v3,s[76:79],s59 offen offset:0 sc1\n"
 MF"v[124:127],v[32:35],a[248:251],v[124:127]\n" WL"(4)\n" MF"v[200:203],v[36:39],a[200:203],v[200:203]\n" TR"v[16:17],v8 offset:57344\n" TR"v[18:19],v8 offset:61440\n" SA"s59,s45,1973824\n"
 BL"a[60:63],v3,s[76:79],s59 offen offset:0 sc1\n" MF"v[204:207],v[36:39],a[216:219],v[204:207]\n" MF"v[208:211],v[36:39],a[232:235],v[208:211]\n" MF"v[212:215],v[36:39],a[248:251],v[212:215]\n"
"v_mbcnt_lo_u32_b32 v251,-1,0\n""v_mbcnt_hi_u32_b32 v251,-1,v251\n""v_min_u32 v251,7,v251\n""s_lshl_b32 s57,s37,6\n" SA"s57,s57,8192\n" VV"v248,s57\n""v_lshl_add_u32 v248,v251,3,v248\n"
"buffer_load_dwordx2 v[252:253],v248,s[92:95],0 offen sc1\n" WL"(4)\n" MF"v[216:219],v[40:43],a[200:203],v[216:219]\n" TR"v[20:21],v9 offset:57344\n" TR"v[22:23],v9 offset:61440\n"
 MF"v[220:223],v[40:43],a[216:219],v[220:223]\n" MF"v[224:227],v[40:43],a[232:235],v[224:227]\n" MF"v[228:231],v[40:43],a[248:251],v[228:231]\n" WL"(4)\n"
 MF"v[232:235],v[44:47],a[200:203],v[232:235]\n" TR"v[24:25],v10 offset:57344\n" TR"v[26:27],v10 offset:61440\n" MF"v[236:239],v[44:47],a[216:219],v[236:239]\n"
 MF"v[240:243],v[44:47],a[232:235],v[240:243]\n" MF"v[244:247],v[44:47],a[248:251],v[244:247]\n" WL"(4)\n" MF"v[48:51],v[16:19],a[204:207],v[48:51]\n" TR"v[28:29],v11 offset:57344\n"
 TR"v[30:31],v11 offset:61440\n" MF"v[52:55],v[16:19],a[220:223],v[52:55]\n" MF"v[56:59],v[16:19],a[236:239],v[56:59]\n" MF"v[60:63],v[16:19],a[252:255],v[60:63]\n" WL"(4)\n"
 MF"v[64:67],v[20:23],a[204:207],v[64:67]\n" TR"v[32:33],v12 offset:57344\n" TR"v[34:35],v12 offset:61440\n" MF"v[68:71],v[20:23],a[220:223],v[68:71]\n" MF"v[72:75],v[20:23],a[236:239],v[72:75]\n"
 MF"v[76:79],v[20:23],a[252:255],v[76:79]\n" WL"(4)\n" MF"v[80:83],v[24:27],a[204:207],v[80:83]\n" TR"v[36:37],v13 offset:57344\n" TR"v[38:39],v13 offset:61440\n"
 MF"v[84:87],v[24:27],a[220:223],v[84:87]\n" TR"v[40:41],v14 offset:57344\n" TR"v[42:43],v14 offset:61440\n" MF"v[88:91],v[24:27],a[236:239],v[88:91]\n" TR"v[44:45],v15 offset:57344\n"
 TR"v[46:47],v15 offset:61440\n" MF"v[92:95],v[24:27],a[252:255],v[92:95]\n" CV"v128,v48,v49\n" CV"v129,v50,v51\n" WL"(8)\n" MF"v[96:99],v[28:31],a[204:207],v[96:99]\n" CV"v130,v64,v65\n"
 CV"v131,v66,v67\n" MF"v[100:103],v[28:31],a[220:223],v[100:103]\n" CV"v144,v52,v53\n" CV"v145,v54,v55\n" MF"v[104:107],v[28:31],a[236:239],v[104:107]\n" CV"v146,v68,v69\n" CV"v147,v70,v71\n"
 MF"v[108:111],v[28:31],a[252:255],v[108:111]\n" CV"v160,v56,v57\n" CV"v161,v58,v59\n" WL"(6)\n" MF"v[112:115],v[32:35],a[204:207],v[112:115]\n" CV"v162,v72,v73\n" CV"v163,v74,v75\n"
 MF"v[116:119],v[32:35],a[220:223],v[116:119]\n" CV"v176,v60,v61\n" CV"v177,v62,v63\n" MF"v[120:123],v[32:35],a[236:239],v[120:123]\n" CV"v178,v76,v77\n" CV"v179,v78,v79\n"
 MF"v[124:127],v[32:35],a[252:255],v[124:127]\n" CV"v132,v80,v81\n" CV"v133,v82,v83\n" WL"(4)\n" MF"v[200:203],v[36:39],a[204:207],v[200:203]\n" CV"v134,v96,v97\n" CV"v135,v98,v99\n"
 MF"v[204:207],v[36:39],a[220:223],v[204:207]\n" CV"v148,v84,v85\n" CV"v149,v86,v87\n" MF"v[208:211],v[36:39],a[236:239],v[208:211]\n" CV"v150,v100,v101\n" CV"v151,v102,v103\n"
 MF"v[212:215],v[36:39],a[252:255],v[212:215]\n" CV"v164,v88,v89\n" CV"v165,v90,v91\n" WL"(2)\n" MF"v[216:219],v[40:43],a[204:207],v[216:219]\n" CV"v166,v104,v105\n" CV"v167,v106,v107\n"
 MF"v[220:223],v[40:43],a[220:223],v[220:223]\n" CV"v180,v92,v93\n" CV"v181,v94,v95\n" MF"v[224:227],v[40:43],a[236:239],v[224:227]\n" CV"v182,v108,v109\n" CV"v183,v110,v111\n"
 MF"v[228:231],v[40:43],a[252:255],v[228:231]\n" CV"v136,v112,v113\n" CV"v137,v114,v115\n" WL"(0)\n" MF"v[232:235],v[44:47],a[204:207],v[232:235]\n" CV"v138,v200,v201\n" CV"v139,v202,v203\n"
 MF"v[236:239],v[44:47],a[220:223],v[236:239]\n" CV"v152,v116,v117\n" CV"v153,v118,v119\n" MF"v[240:243],v[44:47],a[236:239],v[240:243]\n" CV"v154,v204,v205\n" CV"v155,v206,v207\n"
 MF"v[244:247],v[44:47],a[252:255],v[244:247]\n" CV"v168,v120,v121\n" CV"v169,v122,v123\n""s_nop 7\n" CV"v170,v208,v209\n" CV"v171,v210,v211\n" CV"v184,v124,v125\n" CV"v185,v126,v127\n"
 CV"v186,v212,v213\n" CV"v187,v214,v215\n" CV"v140,v216,v217\n" CV"v141,v218,v219\n" CV"v142,v232,v233\n" CV"v143,v234,v235\n" CV"v156,v220,v221\n" CV"v157,v222,v223\n" CV"v158,v236,v237\n"
 CV"v159,v238,v239\n" CV"v172,v224,v225\n" CV"v173,v226,v227\n" CV"v174,v240,v241\n" CV"v175,v242,v243\n" CV"v188,v228,v229\n" CV"v189,v230,v231\n" CV"v190,v244,v245\n" CV"v191,v246,v247\n" WL"(0)\n"
"s_barrier\n" WV"(0)\n""v_cmp_ne_u64 vcc,s[98:99],v[252:253]\n""s_nop 1\n""s_cmp_eq_u64 vcc,0\n""s_cbranch_scc1 Lokpk_%=\n""s_mov_b32 s57,32768\n""Lsppk_%=:\n""s_sleep 2\n"
"buffer_load_dwordx2 v[252:253],v248,s[92:95],0 offen sc1\n" WV"(0)\n""v_cmp_ne_u64 vcc,s[98:99],v[252:253]\n""s_nop 1\n""s_cmp_eq_u64 vcc,0\n""s_cbranch_scc1 Lokpk_%=\n""s_sub_u32 s57,s57,1\n"
"s_cmp_lg_u32 s57,0\n""s_cbranch_scc1 Lsppk_%=\n""Lokpk_%=:\n""s_mov_b32 s50,s46\n""s_mov_b32 m0,s50\n""s_nop 0\n" BL"v0,s[20:23],s40 offen offset:0 sc1 lds\n"
 BL"v0,s[20:23],s40 offen offset:1024 sc1 lds\n" BL"v0,s[20:23],s40 offen offset:2048 sc1 lds\n" BL"v0,s[20:23],s40 offen offset:3072 sc1 lds\n" SA"m0,s50,4096\n""s_nop 0\n"
 BL"v1,s[20:23],s40 offen offset:0 sc1 lds\n" BL"v1,s[20:23],s40 offen offset:1024 sc1 lds\n" BL"v1,s[20:23],s40 offen offset:2048 sc1 lds\n" BL"v1,s[20:23],s40 offen offset:3072 sc1 lds\n"
 SA"s40,s40,32768\n""s_cmp_eq_u32 s40,s48\n""s_cselect_b32 s40,s46,s40\n""s_cselect_b32 s48,-1,s48\n" SA"s50,s46,32768\n""s_mov_b32 m0,s50\n""s_nop 0\n" BL"v0,s[20:23],s40 offen offset:0 sc1 lds\n"
 BL"v0,s[20:23],s40 offen offset:1024 sc1 lds\n" BL"v0,s[20:23],s40 offen offset:2048 sc1 lds\n" BL"v0,s[20:23],s40 offen offset:3072 sc1 lds\n" SA"m0,s50,4096\n""s_nop 0\n"
 BL"v1,s[20:23],s40 offen offset:0 sc1 lds\n" BL"v1,s[20:23],s40 offen offset:1024 sc1 lds\n" BL"v1,s[20:23],s40 offen offset:2048 sc1 lds\n" BL"v1,s[20:23],s40 offen offset:3072 sc1 lds\n"
 SA"s40,s40,32768\n""s_cmp_eq_u32 s40,s48\n""s_cselect_b32 s40,s46,s40\n""s_cselect_b32 s48,-1,s48\n" SA"s50,s46,131072\n""s_max_u32 s51,s46,8192\n""s_mov_b32 m0,s50\n""s_nop 0\n"
 BL"v0,s[24:27],s51 offen offset:0 sc1 lds\n" BL"v0,s[24:27],s51 offen offset:1024 sc1 lds\n" BL"v0,s[24:27],s51 offen offset:2048 sc1 lds\n" BL"v0,s[24:27],s51 offen offset:3072 sc1 lds\n"
 SA"m0,s50,4096\n""s_nop 0\n" BL"v1,s[24:27],s51 offen offset:0 sc1 lds\n" BL"v1,s[24:27],s51 offen offset:1024 sc1 lds\n" BL"v1,s[24:27],s51 offen offset:2048 sc1 lds\n"
 BL"v1,s[24:27],s51 offen offset:3072 sc1 lds\n" SA"s80,s52,1048576\n""s_addc_u32 s81,s53,0\n""s_mov_b32 s82,2097152\n""s_mov_b32 s83,0x20000\n""s_lshl_b32 s43,s42,14\n""s_lshl_b32 s57,s38,11\n"
 SA"s43,s43,s57\n" BL"v[232:235],v0,s[80:83],s43 offen sc1\n" BL"v[236:239],v0,s[80:83],s43 offen offset:1024 sc1\n""v_mbcnt_lo_u32_b32 v12,-1,0\n""v_mbcnt_hi_u32_b32 v12,-1,v12\n"
"v_lshrrev_b32 v14,4,v12\n""v_bfe_u32 v15,v12,2,2\n""v_lshl_add_u32 v15,v14,2,v15\n""v_lshlrev_b32 v13,7,v15\n""v_and_b32 v14,3,v12\n""v_lshl_add_u32 v13,v14,3,v13\n""v_bfe_u32 v15,v15,1,2\n"
"v_xor_b32 v14,0,v15\n""v_lshl_add_u32 v4,v14,5,v13\n""v_add_u32 v8,0x10000,v4\n""v_xor_b32 v14,1,v15\n""v_lshl_add_u32 v5,v14,5,v13\n""v_add_u32 v9,0x10000,v5\n""v_xor_b32 v14,2,v15\n"
"v_lshl_add_u32 v6,v14,5,v13\n""v_add_u32 v10,0x10000,v6\n""v_xor_b32 v14,3,v15\n""v_lshl_add_u32 v7,v14,5,v13\n""v_add_u32 v11,0x10000,v7\n" WV"(39)\n""s_barrier\n" TR"a[128:129],v8 offset:0\n"
 TR"a[130:131],v8 offset:2048\n" TR"a[132:133],v9 offset:0\n" TR"a[134:135],v9 offset:2048\n" TR"a[136:137],v10 offset:0\n" TR"a[138:139],v10 offset:2048\n" TR"a[140:141],v11 offset:0\n"
 TR"a[142:143],v11 offset:2048\n" TR"a[144:145],v8 offset:4096\n" TR"a[146:147],v8 offset:6144\n" TR"a[148:149],v9 offset:4096\n" TR"a[150:151],v9 offset:6144\n" TR"a[152:153],v10 offset:4096\n"
 TR"a[154:155],v10 offset:6144\n" TR"a[156:157],v11 offset:4096\n" WL"(8)\n" TR"a[158:159],v11 offset:6144\n" TR"a[160:161],v8 offset:8192\n" TR"a[162:163],v8 offset:10240\n"
 TR"a[164:165],v9 offset:8192\n" TR"a[166:167],v9 offset:10240\n" TR"a[168:169],v10 offset:8192\n" TR"a[170:171],v10 offset:10240\n" WL"(8)\n" TR"a[172:173],v11 offset:8192\n"
 TR"a[174:175],v11 offset:10240\n" TR"a[176:177],v8 offset:12288\n" TR"a[178:179],v8 offset:14336\n" TR"a[180:181],v9 offset:12288\n" TR"a[182:183],v9 offset:14336\n" TR"a[184:185],v10 offset:12288\n"
 WL"(8)\n" TR"a[186:187],v10 offset:14336\n" TR"a[188:189],v11 offset:12288\n" TR"a[190:191],v11 offset:14336\n" WV"(35)\n""s_barrier\n" MF"v[200:203],a[128:131],a[0:3],0\n"
 TR"a[192:193],v8 offset:16384\n" TR"a[194:195],v8 offset:18432\n" MF"v[204:207],a[132:135],a[0:3],0\n" TR"a[196:197],v9 offset:16384\n" TR"a[198:199],v9 offset:18432\n"
 MF"v[208:211],a[136:139],a[0:3],0\n" WL"(8)\n" TR"a[200:201],v10 offset:16384\n" TR"a[202:203],v10 offset:18432\n" MF"v[212:215],a[140:143],a[0:3],0\n" TR"a[204:205],v11 offset:16384\n"
 TR"a[206:207],v11 offset:18432\n" MF"v[200:203],a[144:147],a[4:7],v[200:203]\n" TR"a[208:209],v8 offset:20480\n" TR"a[210:211],v8 offset:22528\n" MF"v[204:207],a[148:151],a[4:7],v[204:207]\n"
 TR"a[212:213],v9 offset:20480\n" WL"(8)\n" TR"a[214:215],v9 offset:22528\n" MF"v[208:211],a[152:155],a[4:7],v[208:211]\n" TR"a[216:217],v10 offset:20480\n" TR"a[218:219],v10 offset:22528\n"
 MF"v[212:215],a[156:159],a[4:7],v[212:215]\n" TR"a[220:221],v11 offset:20480\n" TR"a[222:223],v11 offset:22528\n" MF"v[200:203],a[160:163],a[8:11],v[200:203]\n" TR"a[224:225],v8 offset:24576\n"
 TR"a[226:227],v8 offset:26624\n" MF"v[204:207],a[164:167],a[8:11],v[204:207]\n" WL"(8)\n" TR"a[228:229],v9 offset:24576\n" TR"a[230:231],v9 offset:26624\n"
 MF"v[208:211],a[168:171],a[8:11],v[208:211]\n" TR"a[232:233],v10 offset:24576\n" TR"a[234:235],v10 offset:26624\n" MF"v[212:215],a[172:175],a[8:11],v[212:215]\n" TR"a[236:237],v11 offset:24576\n"
 TR"a[238:239],v11 offset:26624\n" MF"v[200:203],a[176:179],a[12:15],v[200:203]\n" TR"a[240:241],v8 offset:28672\n" WL"(8)\n" TR"a[242:243],v8 offset:30720\n"
 MF"v[204:207],a[180:183],a[12:15],v[204:207]\n" TR"a[244:245],v9 offset:28672\n" TR"a[246:247],v9 offset:30720\n" MF"v[208:211],a[184:187],a[12:15],v[208:211]\n" TR"a[248:249],v10 offset:28672\n"
 TR"a[250:251],v10 offset:30720\n" MF"v[212:215],a[188:191],a[12:15],v[212:215]\n" TR"a[252:253],v11 offset:28672\n" TR"a[254:255],v11 offset:30720\n" WV"(31)\n""s_barrier\n"
 MF"v[200:203],a[192:195],a[16:19],v[200:203]\n" WL"(8)\n" TR"a[128:129],v8 offset:32768\n" TR"a[130:131],v8 offset:34816\n" MF"v[204:207],a[196:199],a[16:19],v[204:207]\n"
 TR"a[132:133],v9 offset:32768\n" TR"a[134:135],v9 offset:34816\n" MF"v[208:211],a[200:203],a[16:19],v[208:211]\n" TR"a[136:137],v10 offset:32768\n" TR"a[138:139],v10 offset:34816\n"
 MF"v[212:215],a[204:207],a[16:19],v[212:215]\n" TR"a[140:141],v11 offset:32768\n" WL"(8)\n" TR"a[142:143],v11 offset:34816\n" MF"v[200:203],a[208:211],a[20:23],v[200:203]\n"
 TR"a[144:145],v8 offset:36864\n" TR"a[146:147],v8 offset:38912\n" MF"v[204:207],a[212:215],a[20:23],v[204:207]\n" TR"a[148:149],v9 offset:36864\n" TR"a[150:151],v9 offset:38912\n"
 MF"v[208:211],a[216:219],a[20:23],v[208:211]\n" TR"a[152:153],v10 offset:36864\n" TR"a[154:155],v10 offset:38912\n" MF"v[212:215],a[220:223],a[20:23],v[212:215]\n" WL"(8)\n"
 TR"a[156:157],v11 offset:36864\n" TR"a[158:159],v11 offset:38912\n" MF"v[200:203],a[224:227],a[24:27],v[200:203]\n" TR"a[160:161],v8 offset:40960\n" TR"a[162:163],v8 offset:43008\n"
 MF"v[204:207],a[228:231],a[24:27],v[204:207]\n" TR"a[164:165],v9 offset:40960\n" TR"a[166:167],v9 offset:43008\n" MF"v[208:211],a[232:235],a[24:27],v[208:211]\n" TR"a[168:169],v10 offset:40960\n"
 WL"(8)\n" TR"a[170:171],v10 offset:43008\n" MF"v[212:215],a[236:239],a[24:27],v[212:215]\n" TR"a[172:173],v11 offset:40960\n" TR"a[174:175],v11 offset:43008\n"
 MF"v[200:203],a[240:243],a[28:31],v[200:203]\n" TR"a[176:177],v8 offset:45056\n" TR"a[178:179],v8 offset:47104\n" MF"v[204:207],a[244:247],a[28:31],v[204:207]\n" TR"a[180:181],v9 offset:45056\n"
 TR"a[182:183],v9 offset:47104\n" MF"v[208:211],a[248:251],a[28:31],v[208:211]\n" WL"(8)\n" TR"a[184:185],v10 offset:45056\n" TR"a[186:187],v10 offset:47104\n"
 MF"v[212:215],a[252:255],a[28:31],v[212:215]\n" TR"a[188:189],v11 offset:45056\n" TR"a[190:191],v11 offset:47104\n" WV"(27)\n""s_barrier\n" MF"v[200:203],a[128:131],a[32:35],v[200:203]\n"
 TR"a[192:193],v8 offset:49152\n" TR"a[194:195],v8 offset:51200\n" MF"v[204:207],a[132:135],a[32:35],v[204:207]\n" TR"a[196:197],v9 offset:49152\n" WL"(8)\n" TR"a[198:199],v9 offset:51200\n"
 MF"v[208:211],a[136:139],a[32:35],v[208:211]\n" TR"a[200:201],v10 offset:49152\n" TR"a[202:203],v10 offset:51200\n" MF"v[212:215],a[140:143],a[32:35],v[212:215]\n" TR"a[204:205],v11 offset:49152\n"
 TR"a[206:207],v11 offset:51200\n" MF"v[200:203],a[144:147],a[36:39],v[200:203]\n" TR"a[208:209],v8 offset:53248\n" TR"a[210:211],v8 offset:55296\n" MF"v[204:207],a[148:151],a[36:39],v[204:207]\n"
 WL"(8)\n" TR"a[212:213],v9 offset:53248\n" TR"a[214:215],v9 offset:55296\n" MF"v[208:211],a[152:155],a[36:39],v[208:211]\n" TR"a[216:217],v10 offset:53248\n" TR"a[218:219],v10 offset:55296\n"
 MF"v[212:215],a[156:159],a[36:39],v[212:215]\n" TR"a[220:221],v11 offset:53248\n" TR"a[222:223],v11 offset:55296\n" MF"v[200:203],a[160:163],a[40:43],v[200:203]\n" TR"a[224:225],v8 offset:57344\n"
 WL"(8)\n" TR"a[226:227],v8 offset:59392\n" MF"v[204:207],a[164:167],a[40:43],v[204:207]\n" TR"a[228:229],v9 offset:57344\n" TR"a[230:231],v9 offset:59392\n"
 MF"v[208:211],a[168:171],a[40:43],v[208:211]\n" TR"a[232:233],v10 offset:57344\n" TR"a[234:235],v10 offset:59392\n" MF"v[212:215],a[172:175],a[40:43],v[212:215]\n" TR"a[236:237],v11 offset:57344\n"
 TR"a[238:239],v11 offset:59392\n" MF"v[200:203],a[176:179],a[44:47],v[200:203]\n" WL"(8)\n" TR"a[240:241],v8 offset:61440\n" TR"a[242:243],v8 offset:63488\n"
 MF"v[204:207],a[180:183],a[44:47],v[204:207]\n" TR"a[244:245],v9 offset:61440\n" TR"a[246:247],v9 offset:63488\n" MF"v[208:211],a[184:187],a[44:47],v[208:211]\n" TR"a[248:249],v10 offset:61440\n"
 TR"a[250:251],v10 offset:63488\n" MF"v[212:215],a[188:191],a[44:47],v[212:215]\n" TR"a[252:253],v11 offset:61440\n" WL"(8)\n" TR"a[254:255],v11 offset:63488\n"
 MF"v[200:203],a[192:195],a[48:51],v[200:203]\n" MF"v[204:207],a[196:199],a[48:51],v[204:207]\n" MF"v[208:211],a[200:203],a[48:51],v[208:211]\n" MF"v[212:215],a[204:207],a[48:51],v[212:215]\n"
 MF"v[200:203],a[208:211],a[52:55],v[200:203]\n" MF"v[204:207],a[212:215],a[52:55],v[204:207]\n" MF"v[208:211],a[216:219],a[52:55],v[208:211]\n" MF"v[212:215],a[220:223],a[52:55],v[212:215]\n"
 MF"v[200:203],a[224:227],a[56:59],v[200:203]\n" MF"v[204:207],a[228:231],a[56:59],v[204:207]\n" MF"v[208:211],a[232:235],a[56:59],v[208:211]\n" WL"(8)\n"
 MF"v[212:215],a[236:239],a[56:59],v[212:215]\n" WL"(6)\n" MF"v[200:203],a[240:243],a[60:63],v[200:203]\n" WL"(4)\n" MF"v[204:207],a[244:247],a[60:63],v[204:207]\n" WL"(2)\n"
 MF"v[208:211],a[248:251],a[60:63],v[208:211]\n" WL"(0)\n" MF"v[212:215],a[252:255],a[60:63],v[212:215]\n" WV"(0)\n""s_nop 7\n""v_lshlrev_b32 v13,16,v232\n""v_and_b32 v15,0xffff0000,v232\n"
"v_add_f32 v200,v200,v13\n""v_add_f32 v201,v201,v15\n" CV"v64,v200,v201\n""v_lshlrev_b32 v13,16,v233\n""v_and_b32 v15,0xffff0000,v233\n""v_add_f32 v202,v202,v13\n""v_add_f32 v203,v203,v15\n"
 CV"v65,v202,v203\n""v_lshlrev_b32 v13,16,v234\n""v_and_b32 v15,0xffff0000,v234\n""v_add_f32 v204,v204,v13\n""v_add_f32 v205,v205,v15\n" CV"v66,v204,v205\n""v_lshlrev_b32 v13,16,v235\n"
"v_and_b32 v15,0xffff0000,v235\n""v_add_f32 v206,v206,v13\n""v_add_f32 v207,v207,v15\n" CV"v67,v206,v207\n""v_lshlrev_b32 v13,16,v236\n""v_and_b32 v15,0xffff0000,v236\n""v_add_f32 v208,v208,v13\n"
"v_add_f32 v209,v209,v15\n" CV"v68,v208,v209\n""v_lshlrev_b32 v13,16,v237\n""v_and_b32 v15,0xffff0000,v237\n""v_add_f32 v210,v210,v13\n""v_add_f32 v211,v211,v15\n" CV"v69,v210,v211\n"
"v_lshlrev_b32 v13,16,v238\n""v_and_b32 v15,0xffff0000,v238\n""v_add_f32 v212,v212,v13\n""v_add_f32 v213,v213,v15\n" CV"v70,v212,v213\n""v_lshlrev_b32 v13,16,v239\n""v_and_b32 v15,0xffff0000,v239\n"
"v_add_f32 v214,v214,v13\n""v_add_f32 v215,v215,v15\n" CV"v71,v214,v215\n" SA"s84,s52,7340032\n""s_addc_u32 s85,s53,0\n""s_mov_b32 s86,2097152\n""s_mov_b32 s87,0x20000\n" SA"s88,s52,23068672\n"
"s_addc_u32 s89,s53,0\n""s_mov_b32 s90,131072\n""s_mov_b32 s91,0x20000\n" BS"v[64:67],v0,s[84:87],s43 offen sc1\n" BS"v[68:71],v0,s[84:87],s43 offen offset:1024 sc1\n""v_lshlrev_b32 v32,16,v64\n"
"v_and_b32 v33,0xffff0000,v64\n""v_lshlrev_b32 v34,16,v65\n""v_and_b32 v35,0xffff0000,v65\n""v_lshlrev_b32 v36,16,v66\n""v_and_b32 v37,0xffff0000,v66\n""v_lshlrev_b32 v38,16,v67\n"
"v_and_b32 v39,0xffff0000,v67\n""v_lshlrev_b32 v40,16,v68\n""v_and_b32 v41,0xffff0000,v68\n""v_lshlrev_b32 v42,16,v69\n""v_and_b32 v43,0xffff0000,v69\n""v_lshlrev_b32 v44,16,v70\n"
"v_and_b32 v45,0xffff0000,v70\n""v_lshlrev_b32 v46,16,v71\n""v_and_b32 v47,0xffff0000,v71\n""v_add_f32 v48,v32,v33\n""v_add_f32 v48,v48,v34\n""v_add_f32 v48,v48,v35\n""v_add_f32 v48,v48,v36\n"
"v_add_f32 v48,v48,v37\n""v_add_f32 v48,v48,v38\n""v_add_f32 v48,v48,v39\n""v_add_f32 v48,v48,v40\n""v_add_f32 v48,v48,v41\n""v_add_f32 v48,v48,v42\n""v_add_f32 v48,v48,v43\n""v_add_f32 v48,v48,v44\n"
"v_add_f32 v48,v48,v45\n""v_add_f32 v48,v48,v46\n""v_add_f32 v48,v48,v47\n" VV"v13,v48\n""s_nop 1\n""v_permlane16_swap_b32 v48,v13\n""s_nop 1\n""v_add_f32 v48,v48,v13\n" VV"v13,v48\n""s_nop 1\n"
"v_permlane32_swap_b32 v48,v13\n""s_nop 1\n""v_add_f32 v48,v48,v13\n" VM"v48,0x3c800000,v48\n""v_sub_f32 v32,v32,v48\n""v_sub_f32 v33,v33,v48\n""v_sub_f32 v34,v34,v48\n""v_sub_f32 v35,v35,v48\n"
"v_sub_f32 v36,v36,v48\n""v_sub_f32 v37,v37,v48\n""v_sub_f32 v38,v38,v48\n""v_sub_f32 v39,v39,v48\n""v_sub_f32 v40,v40,v48\n""v_sub_f32 v41,v41,v48\n""v_sub_f32 v42,v42,v48\n""v_sub_f32 v43,v43,v48\n"
"v_sub_f32 v44,v44,v48\n""v_sub_f32 v45,v45,v48\n""v_sub_f32 v46,v46,v48\n""v_sub_f32 v47,v47,v48\n" VM"v49,v32,v32\n""v_fmac_f32 v49,v33,v33\n""v_fmac_f32 v49,v34,v34\n""v_fmac_f32 v49,v35,v35\n"
"v_fmac_f32 v49,v36,v36\n""v_fmac_f32 v49,v37,v37\n""v_fmac_f32 v49,v38,v38\n""v_fmac_f32 v49,v39,v39\n""v_fmac_f32 v49,v40,v40\n""v_fmac_f32 v49,v41,v41\n""v_fmac_f32 v49,v42,v42\n"
"v_fmac_f32 v49,v43,v43\n""v_fmac_f32 v49,v44,v44\n""v_fmac_f32 v49,v45,v45\n""v_fmac_f32 v49,v46,v46\n""v_fmac_f32 v49,v47,v47\n" VV"v13,v49\n""s_nop 1\n""v_permlane16_swap_b32 v49,v13\n""s_nop 1\n"
"v_add_f32 v49,v49,v13\n" VV"v13,v49\n""s_nop 1\n""v_permlane32_swap_b32 v49,v13\n""s_nop 1\n""v_add_f32 v49,v49,v13\n""v_bfe_u32 v13,v0,4,4\n""v_lshlrev_b32 v13,6,v13\n""s_lshl_b32 s58,s42,10\n"
"s_lshl_b32 s59,s38,3\n" SA"s58,s58,s59\n""buffer_store_dwordx2 v[48:49],v13,s[88:91],s58 offen sc1\n""s_cmp_eq_u32 s36,0\n""s_cselect_b32 s58,32768,s46\n""s_and_b32 s37,s96,31\n"
"s_lshr_b32 s47,s96,5\n""s_sub_u32 s55,15,s47\n" SA"s64,s52,67108864\n""s_addc_u32 s65,s53,0\n""s_mov_b32 s66,16777216\n""s_mov_b32 s67,0x20000\n""v_mbcnt_lo_u32_b32 v248,-1,0\n"
"v_mbcnt_hi_u32_b32 v248,-1,v248\n""v_lshlrev_b32 v0,4,v248\n""v_add_u32 v1,4096,v0\n""v_lshrrev_b32 v249,4,v248\n""v_and_b32 v250,15,v248\n""v_lshlrev_b32 v251,2,v249\n""v_sub_u32 v250,v250,v251\n"
"v_subrev_u32 v251,0,v250\n""v_ashrrev_i32 v251,31,v251\n""v_and_b32 v192,0xff800000,v251\n""v_subrev_u32 v251,1,v250\n""v_ashrrev_i32 v251,31,v251\n""v_and_b32 v193,0xff800000,v251\n"
"v_subrev_u32 v251,2,v250\n""v_ashrrev_i32 v251,31,v251\n""v_and_b32 v194,0xff800000,v251\n""v_subrev_u32 v251,3,v250\n""v_ashrrev_i32 v251,31,v251\n""v_and_b32 v195,0xff800000,v251\n"
"s_mov_b32 s44,0\n" SA"s42,s46,65536\n" SA"s43,s46,98304\n" VV"v2,v0\n""v_add_u32 v3,131072,v0\n""s_mov_b32 s45,0\n" VV"v8,0x3f803f80\n" VV"v9,0x3f803f80\n" VV"v10,0x3f803f80\n" VV"v11,0x3f803f80\n"
 WV"(5) lgkmcnt(0)\n""s_barrier\n" DR"v[16:19],v2 offset:0\n" DR"v[20:23],v2 offset:1024\n" DR"v[24:27],v2 offset:2048\n" DR"v[28:31],v2 offset:3072\n" DR"v[32:35],v2 offset:4096\n"
"s_cmp_eq_u32 s47,0\n""s_cbranch_scc1 Ldummy_%=\n" WL"(3)\n" MF"v[80:83],v[16:19],v[128:131],0\n" DR"v[36:39],v2 offset:5120\n" DR"v[40:43],v2 offset:6144\n" MF"v[84:87],v[16:19],v[144:147],0\n"
"s_mov_b32 m0,s43\n" MF"v[80:83],v[20:23],v[132:135],v[80:83]\n" MF"v[84:87],v[20:23],v[148:151],v[84:87]\n" BL"v0,s[24:27],s41 offen offset:0 sc1 lds\n" WL"(3)\n"
 MF"v[80:83],v[24:27],v[136:139],v[80:83]\n" DR"v[44:47],v2 offset:7168\n" DR"v[48:51],v2 offset:8192\n" MF"v[84:87],v[24:27],v[152:155],v[84:87]\n" MF"v[80:83],v[28:31],v[140:143],v[80:83]\n"
 MF"v[84:87],v[28:31],v[156:159],v[84:87]\n" WL"(4)\n" MF"v[88:91],v[32:35],v[128:131],0\n" MF"v[92:95],v[32:35],v[144:147],0\n" BL"v0,s[24:27],s41 offen offset:1024 sc1 lds\n" WL"(3)\n"
 MF"v[88:91],v[36:39],v[132:135],v[88:91]\n" MF"v[92:95],v[36:39],v[148:151],v[92:95]\n" EX"v80,v80\n" WL"(2)\n" MF"v[88:91],v[40:43],v[136:139],v[88:91]\n" EX"v81,v81\n"
 MF"v[92:95],v[40:43],v[152:155],v[92:95]\n" CV"v112,v80,v81\n" WL"(1)\n" MF"v[88:91],v[44:47],v[140:143],v[88:91]\n" EX"v82,v82\n" MF"v[92:95],v[44:47],v[156:159],v[92:95]\n"
 DR"v[52:55],v2 offset:9216\n" DR"v[56:59],v2 offset:10240\n" EX"v83,v83\n" BL"v0,s[24:27],s41 offen offset:2048 sc1 lds\n" CV"v113,v82,v83\n" EX"v84,v84\n" EX"v85,v85\n"
 DR"v[60:63],v2 offset:11264\n" DR"v[64:67],v2 offset:12288\n" CV"v116,v84,v85\n" EX"v86,v86\n" DR"v[68:71],v2 offset:13312\n" DR"v[72:75],v2 offset:14336\n" EX"v87,v87\n" WL"(6)\n"
 MF"v[96:99],v[48:51],v[128:131],0\n" EX"v88,v88\n" MF"v[100:103],v[48:51],v[144:147],0\n" BL"v0,s[24:27],s41 offen offset:3072 sc1 lds\n" CV"v117,v86,v87\n" WL"(4)\n"
 MF"v[96:99],v[52:55],v[132:135],v[96:99]\n" SA"m0,s43,4096\n" EX"v89,v89\n" MF"v[100:103],v[52:55],v[148:151],v[100:103]\n" DR"v[76:79],v2 offset:15360\n" DR"v[16:19],v3 offset:8192\n"
 MF"v[96:99],v[56:59],v[136:139],v[96:99]\n" EX"v90,v90\n" MF"v[100:103],v[56:59],v[152:155],v[100:103]\n" CV"v114,v88,v89\n" WL"(4)\n" MF"v[96:99],v[60:63],v[140:143],v[96:99]\n"
 BL"v1,s[24:27],s41 offen offset:0 sc1 lds\n" EX"v91,v91\n" MF"v[100:103],v[60:63],v[156:159],v[100:103]\n" CV"v115,v90,v91\n" EX"v92,v92\n" MF"v[104:107],v[64:67],v[128:131],0\n"
 DR"v[20:23],v3 offset:9216\n" DR"v[24:27],v3 offset:10240\n" MF"v[108:111],v[64:67],v[144:147],0\n" EX"v93,v93\n" WL"(4)\n" MF"v[104:107],v[68:71],v[132:135],v[104:107]\n"
 DR"v[28:31],v3 offset:11264\n" DR"v[32:35],v3 offset:12288\n" CV"v118,v92,v93\n" MF"v[108:111],v[68:71],v[148:151],v[108:111]\n" EX"v94,v94\n" MF"v[104:107],v[72:75],v[136:139],v[104:107]\n"
 EX"v96,v96\n" MF"v[108:111],v[72:75],v[152:155],v[108:111]\n" EX"v95,v95\n" WL"(4)\n" MF"v[104:107],v[76:79],v[140:143],v[104:107]\n" CV"v119,v94,v95\n" EX"v97,v97\n"
 MF"v[108:111],v[76:79],v[156:159],v[108:111]\n" DR"v[36:39],v3 offset:13312\n" DR"v[40:43],v3 offset:14336\n" MF"a[0:3],v[16:19],v[112:115],0\n" EX"v98,v98\n" MF"a[4:7],v[16:19],v[116:119],0\n"
 BL"v1,s[24:27],s41 offen offset:1024 sc1 lds\n" CV"v120,v96,v97\n" WL"(4)\n" MF"a[8:11],v[20:23],v[112:115],0\n" EX"v99,v99\n" MF"a[12:15],v[20:23],v[116:119],0\n" DR"v[44:47],v3 offset:15360\n"
 DR"v[48:51],v2 offset:16384\n" MF"a[16:19],v[24:27],v[112:115],0\n" EX"v100,v100\n" MF"a[20:23],v[24:27],v[116:119],0\n" BL"v1,s[24:27],s41 offen offset:2048 sc1 lds\n" CV"v121,v98,v99\n" WL"(4)\n"
 MF"a[24:27],v[28:31],v[112:115],0\n" EX"v101,v101\n" MF"a[28:31],v[28:31],v[116:119],0\n" CV"v124,v100,v101\n" MF"a[32:35],v[32:35],v[112:115],0\n" EX"v102,v102\n"
 MF"a[36:39],v[32:35],v[116:119],0\n" DR"v[52:55],v2 offset:17408\n" DR"v[56:59],v2 offset:18432\n" WL"(4)\n" MF"a[40:43],v[36:39],v[112:115],0\n" EX"v103,v103\n" MF"a[44:47],v[36:39],v[116:119],0\n"
 BL"v1,s[24:27],s41 offen offset:3072 sc1 lds\n" CV"v125,v102,v103\n" MF"a[48:51],v[40:43],v[112:115],0\n""s_mov_b32 m0,s42\n" EX"v104,v104\n" MF"a[52:55],v[40:43],v[116:119],0\n"
 DR"v[60:63],v2 offset:19456\n" DR"v[64:67],v2 offset:20480\n" WL"(4)\n" MF"a[56:59],v[44:47],v[112:115],0\n" EX"v105,v105\n" MF"a[60:63],v[44:47],v[116:119],0\n"
 BL"v0,s[20:23],s40 offen offset:0 sc1 lds\n" CV"v122,v104,v105\n" MF"a[64:67],v[8:11],v[112:115],0\n" EX"v106,v106\n" MF"a[68:71],v[8:11],v[116:119],0\n" DR"v[68:71],v2 offset:21504\n"
 DR"v[72:75],v2 offset:22528\n" MF"v[80:83],v[48:51],v[128:131],0\n" EX"v107,v107\n" MF"v[84:87],v[48:51],v[144:147],0\n" CV"v123,v106,v107\n" WL"(4)\n" MF"v[80:83],v[52:55],v[132:135],v[80:83]\n"
 EX"v108,v108\n" MF"v[84:87],v[52:55],v[148:151],v[84:87]\n" DR"v[76:79],v2 offset:23552\n" DR"v[16:19],v3 offset:16384\n" MF"v[80:83],v[56:59],v[136:139],v[80:83]\n" EX"v109,v109\n"
 MF"v[84:87],v[56:59],v[152:155],v[84:87]\n" CV"v126,v108,v109\n" WL"(4)\n" MF"v[80:83],v[60:63],v[140:143],v[80:83]\n" EX"v110,v110\n" MF"v[84:87],v[60:63],v[156:159],v[84:87]\n"
 DR"v[20:23],v3 offset:17408\n" DR"v[24:27],v3 offset:18432\n" MF"v[88:91],v[64:67],v[128:131],0\n" EX"v111,v111\n" MF"v[92:95],v[64:67],v[144:147],0\n" BL"v0,s[20:23],s40 offen offset:1024 sc1 lds\n"
 CV"v127,v110,v111\n" WL"(4)\n" MF"v[88:91],v[68:71],v[132:135],v[88:91]\n" MF"v[92:95],v[68:71],v[148:151],v[92:95]\n" DR"v[28:31],v3 offset:19456\n" DR"v[32:35],v3 offset:20480\n"
 MF"v[88:91],v[72:75],v[136:139],v[88:91]\n" EX"v80,v80\n" MF"v[92:95],v[72:75],v[152:155],v[92:95]\n" WL"(4)\n" MF"v[88:91],v[76:79],v[140:143],v[88:91]\n" EX"v81,v81\n"
 MF"v[92:95],v[76:79],v[156:159],v[92:95]\n" DR"v[36:39],v3 offset:21504\n" DR"v[40:43],v3 offset:22528\n" MF"a[0:3],v[16:19],v[120:123],a[0:3]\n" EX"v82,v82\n" MF"a[4:7],v[16:19],v[124:127],a[4:7]\n"
 BL"v0,s[20:23],s40 offen offset:2048 sc1 lds\n" CV"v112,v80,v81\n" WL"(4)\n" MF"a[8:11],v[20:23],v[120:123],a[8:11]\n" EX"v83,v83\n" MF"a[12:15],v[20:23],v[124:127],a[12:15]\n"
 DR"v[44:47],v3 offset:23552\n" DR"v[48:51],v2 offset:24576\n" MF"a[16:19],v[24:27],v[120:123],a[16:19]\n" EX"v84,v84\n" MF"a[20:23],v[24:27],v[124:127],a[20:23]\n" CV"v113,v82,v83\n" WL"(4)\n"
 MF"a[24:27],v[28:31],v[120:123],a[24:27]\n" EX"v85,v85\n" MF"a[28:31],v[28:31],v[124:127],a[28:31]\n" CV"v116,v84,v85\n" MF"a[32:35],v[32:35],v[120:123],a[32:35]\n" EX"v86,v86\n"
 MF"a[36:39],v[32:35],v[124:127],a[36:39]\n" DR"v[52:55],v2 offset:25600\n" DR"v[56:59],v2 offset:26624\n" WL"(4)\n" MF"a[40:43],v[36:39],v[120:123],a[40:43]\n" EX"v87,v87\n"
 MF"a[44:47],v[36:39],v[124:127],a[44:47]\n" BL"v0,s[20:23],s40 offen offset:3072 sc1 lds\n" CV"v117,v86,v87\n" MF"a[48:51],v[40:43],v[120:123],a[48:51]\n" SA"m0,s42,4096\n" EX"v88,v88\n"
 MF"a[52:55],v[40:43],v[124:127],a[52:55]\n" DR"v[60:63],v2 offset:27648\n" DR"v[64:67],v2 offset:28672\n" WL"(4)\n" MF"a[56:59],v[44:47],v[120:123],a[56:59]\n" SA"s50,s44,32768\n" EX"v89,v89\n"
 MF"a[60:63],v[44:47],v[124:127],a[60:63]\n""s_cmp_eq_u32 s50,98304\n" CV"v114,v88,v89\n" MF"a[64:67],v[8:11],v[120:123],a[64:67]\n""s_cselect_b32 s50,0,s50\n" EX"v90,v90\n"
 MF"a[68:71],v[8:11],v[124:127],a[68:71]\n" DR"v[68:71],v2 offset:29696\n" DR"v[72:75],v2 offset:30720\n" MF"v[96:99],v[48:51],v[128:131],0\n""s_xor_b32 s43,s43,0x38000\n" EX"v91,v91\n"
 MF"v[100:103],v[48:51],v[144:147],0\n" BL"v1,s[20:23],s40 offen offset:0 sc1 lds\n" CV"v115,v90,v91\n" WL"(4)\n" MF"v[96:99],v[52:55],v[132:135],v[96:99]\n" SA"s41,s41,32768\n" EX"v92,v92\n"
 MF"v[100:103],v[52:55],v[148:151],v[100:103]\n" DR"v[76:79],v2 offset:31744\n" DR"v[16:19],v3 offset:24576\n" MF"v[96:99],v[56:59],v[136:139],v[96:99]\n""s_cmp_eq_u32 s41,s49\n" EX"v93,v93\n"
 MF"v[100:103],v[56:59],v[152:155],v[100:103]\n""s_cselect_b32 s41,s58,s41\n" CV"v118,v92,v93\n" WL"(4)\n" MF"v[96:99],v[60:63],v[140:143],v[96:99]\n""s_cselect_b32 s49,-1,s49\n" EX"v94,v94\n"
 MF"v[100:103],v[60:63],v[156:159],v[100:103]\n" DR"v[20:23],v3 offset:25600\n" DR"v[24:27],v3 offset:26624\n" MF"v[104:107],v[64:67],v[128:131],0\n" EX"v95,v95\n"
 MF"v[108:111],v[64:67],v[144:147],0\n" BL"v1,s[20:23],s40 offen offset:1024 sc1 lds\n" CV"v119,v94,v95\n" WL"(4)\n" MF"v[104:107],v[68:71],v[132:135],v[104:107]\n"
 MF"v[108:111],v[68:71],v[148:151],v[108:111]\n" DR"v[36:39],v3 offset:27648\n" DR"v[40:43],v3 offset:28672\n" MF"v[104:107],v[72:75],v[136:139],v[104:107]\n" EX"v96,v96\n"
 MF"v[108:111],v[72:75],v[152:155],v[108:111]\n" WL"(4)\n" MF"v[104:107],v[76:79],v[140:143],v[104:107]\n" EX"v97,v97\n" MF"v[108:111],v[76:79],v[156:159],v[108:111]\n" DR"v[44:47],v3 offset:29696\n"
 DR"v[48:51],v3 offset:30720\n" MF"a[0:3],v[16:19],v[112:115],a[0:3]\n" EX"v98,v98\n" MF"a[4:7],v[16:19],v[116:119],a[4:7]\n" DR"v[52:55],v3 offset:31744\n" CV"v120,v96,v97\n" WL"(5)\n"
 MF"a[8:11],v[20:23],v[112:115],a[8:11]\n" EX"v99,v99\n" MF"a[12:15],v[20:23],v[116:119],a[12:15]\n" CV"v121,v98,v99\n""v_add_u32 v2,s50,v0\n" MF"a[16:19],v[24:27],v[112:115],a[16:19]\n"
 SA"s42,s44,s46\n" EX"v100,v100\n" MF"a[20:23],v[24:27],v[116:119],a[20:23]\n""s_mov_b32 s44,s50\n""v_xor_b32 v3,0x38000,v3\n" WL"(3)\n" MF"a[24:27],v[36:39],v[112:115],a[24:27]\n"
 WV"(6) lgkmcnt(0)\n""s_barrier\n" EX"v101,v101\n" MF"a[28:31],v[36:39],v[116:119],a[28:31]\n" DR"v[16:19],v2 offset:0\n" DR"v[20:23],v2 offset:1024\n" CV"v124,v100,v101\n"
 MF"a[32:35],v[40:43],v[112:115],a[32:35]\n" BL"v1,s[20:23],s40 offen offset:2048 sc1 lds\n" EX"v102,v102\n" MF"a[36:39],v[40:43],v[116:119],a[36:39]\n" DR"v[24:27],v2 offset:2048\n"
 DR"v[28:31],v2 offset:3072\n" BL"v1,s[20:23],s40 offen offset:3072 sc1 lds\n" MF"a[40:43],v[44:47],v[112:115],a[40:43]\n" SA"s40,s40,32768\n" EX"v103,v103\n"
 MF"a[44:47],v[44:47],v[116:119],a[44:47]\n" DR"v[32:35],v2 offset:4096\n" CV"v125,v102,v103\n" MF"a[48:51],v[48:51],v[112:115],a[48:51]\n""s_cmp_eq_u32 s40,s48\n" EX"v104,v104\n"
 MF"a[52:55],v[48:51],v[116:119],a[52:55]\n""s_cselect_b32 s40,s46,s40\n""s_cselect_b32 s48,-1,s48\n" MF"a[56:59],v[52:55],v[112:115],a[56:59]\n" EX"v105,v105\n"
 MF"a[60:63],v[52:55],v[116:119],a[60:63]\n" CV"v122,v104,v105\n" MF"a[64:67],v[8:11],v[112:115],a[64:67]\n" EX"v106,v106\n" MF"a[68:71],v[8:11],v[116:119],a[68:71]\n""s_sub_u32 s39,s47,1\n"
"s_cmp_eq_u32 s39,0\n""s_cbranch_scc1 Ltail_%=\n""Lloop_%=:\n" WL"(3)\n" MF"v[80:83],v[16:19],v[128:131],0\n""s_mov_b32 m0,s43\n" EX"v107,v107\n" MF"v[84:87],v[16:19],v[144:147],0\n"
 DR"v[36:39],v2 offset:5120\n" DR"v[40:43],v2 offset:6144\n" MF"v[80:83],v[20:23],v[132:135],v[80:83]\n" EX"v108,v108\n" MF"v[84:87],v[20:23],v[148:151],v[84:87]\n" DR"v[44:47],v2 offset:7168\n"
 DR"v[48:51],v3 offset:0\n" WL"(5)\n" MF"v[80:83],v[24:27],v[136:139],v[80:83]\n" EX"v109,v109\n" MF"v[84:87],v[24:27],v[152:155],v[84:87]\n" CV"v123,v106,v107\n" CV"v126,v108,v109\n"
 MF"v[80:83],v[28:31],v[140:143],v[80:83]\n" EX"v110,v110\n" MF"v[84:87],v[28:31],v[156:159],v[84:87]\n" DR"v[52:55],v3 offset:1024\n" DR"v[56:59],v3 offset:2048\n" WL"(6)\n"
 MF"v[88:91],v[32:35],v[128:131],0\n" EX"v111,v111\n" MF"v[92:95],v[32:35],v[144:147],0\n" BL"v0,s[24:27],s41 offen offset:0 sc1 lds\n" CV"v127,v110,v111\n" WL"(4)\n"
 MF"v[88:91],v[36:39],v[132:135],v[88:91]\n" BL"v0,s[24:27],s41 offen offset:1024 sc1 lds\n" MF"v[92:95],v[36:39],v[148:151],v[92:95]\n" DR"v[60:63],v3 offset:3072\n" DR"v[64:67],v3 offset:4096\n"
 MF"v[88:91],v[40:43],v[136:139],v[88:91]\n" EX"v80,v80\n" MF"v[92:95],v[40:43],v[152:155],v[92:95]\n" WL"(4)\n" MF"v[88:91],v[44:47],v[140:143],v[88:91]\n" EX"v81,v81\n"
 MF"v[92:95],v[44:47],v[156:159],v[92:95]\n" DR"v[68:71],v3 offset:5120\n" DR"v[72:75],v3 offset:6144\n" MF"a[0:3],v[48:51],v[120:123],a[0:3]\n" EX"v82,v82\n" MF"a[4:7],v[48:51],v[124:127],a[4:7]\n"
 CV"v112,v80,v81\n" WL"(4)\n" MF"a[8:11],v[52:55],v[120:123],a[8:11]\n" EX"v83,v83\n" MF"a[12:15],v[52:55],v[124:127],a[12:15]\n" DR"v[76:79],v3 offset:7168\n" DR"v[16:19],v2 offset:8192\n"
 MF"a[16:19],v[56:59],v[120:123],a[16:19]\n" EX"v84,v84\n" MF"a[20:23],v[56:59],v[124:127],a[20:23]\n" BL"v0,s[24:27],s41 offen offset:2048 sc1 lds\n" CV"v113,v82,v83\n" WL"(4)\n"
 MF"a[24:27],v[60:63],v[120:123],a[24:27]\n" EX"v85,v85\n" MF"a[28:31],v[60:63],v[124:127],a[28:31]\n" CV"v116,v84,v85\n" MF"a[32:35],v[64:67],v[120:123],a[32:35]\n" EX"v86,v86\n"
 MF"a[36:39],v[64:67],v[124:127],a[36:39]\n" DR"v[20:23],v2 offset:9216\n" DR"v[24:27],v2 offset:10240\n" WL"(4)\n" MF"a[40:43],v[68:71],v[120:123],a[40:43]\n" EX"v87,v87\n"
 MF"a[44:47],v[68:71],v[124:127],a[44:47]\n" BL"v0,s[24:27],s41 offen offset:3072 sc1 lds\n" CV"v117,v86,v87\n" MF"a[48:51],v[72:75],v[120:123],a[48:51]\n" SA"m0,s43,4096\n" EX"v88,v88\n"
 MF"a[52:55],v[72:75],v[124:127],a[52:55]\n" DR"v[28:31],v2 offset:11264\n" DR"v[32:35],v2 offset:12288\n" WL"(4)\n" MF"a[56:59],v[76:79],v[120:123],a[56:59]\n" EX"v89,v89\n"
 MF"a[60:63],v[76:79],v[124:127],a[60:63]\n" CV"v114,v88,v89\n" MF"a[64:67],v[8:11],v[120:123],a[64:67]\n" EX"v90,v90\n" MF"a[68:71],v[8:11],v[124:127],a[68:71]\n" DR"v[36:39],v2 offset:13312\n"
 DR"v[40:43],v2 offset:14336\n" MF"v[96:99],v[16:19],v[128:131],0\n" EX"v91,v91\n" MF"v[100:103],v[16:19],v[144:147],0\n" BL"v1,s[24:27],s41 offen offset:0 sc1 lds\n" CV"v115,v90,v91\n" WL"(4)\n"
 MF"v[96:99],v[20:23],v[132:135],v[96:99]\n" EX"v92,v92\n" MF"v[100:103],v[20:23],v[148:151],v[100:103]\n" DR"v[44:47],v2 offset:15360\n" DR"v[48:51],v3 offset:8192\n"
 MF"v[96:99],v[24:27],v[136:139],v[96:99]\n" EX"v93,v93\n" MF"v[100:103],v[24:27],v[152:155],v[100:103]\n" CV"v118,v92,v93\n" WL"(4)\n" MF"v[96:99],v[28:31],v[140:143],v[96:99]\n" EX"v94,v94\n"
 MF"v[100:103],v[28:31],v[156:159],v[100:103]\n" DR"v[52:55],v3 offset:9216\n" DR"v[56:59],v3 offset:10240\n" MF"v[104:107],v[32:35],v[128:131],0\n" EX"v95,v95\n"
 MF"v[108:111],v[32:35],v[144:147],0\n" BL"v1,s[24:27],s41 offen offset:1024 sc1 lds\n" CV"v119,v94,v95\n" WL"(4)\n" MF"v[104:107],v[36:39],v[132:135],v[104:107]\n"
 MF"v[108:111],v[36:39],v[148:151],v[108:111]\n" DR"v[60:63],v3 offset:11264\n" DR"v[64:67],v3 offset:12288\n" MF"v[104:107],v[40:43],v[136:139],v[104:107]\n" EX"v96,v96\n"
 MF"v[108:111],v[40:43],v[152:155],v[108:111]\n" WL"(4)\n" MF"v[104:107],v[44:47],v[140:143],v[104:107]\n" EX"v97,v97\n" MF"v[108:111],v[44:47],v[156:159],v[108:111]\n" DR"v[68:71],v3 offset:13312\n"
 DR"v[72:75],v3 offset:14336\n" MF"a[0:3],v[48:51],v[112:115],a[0:3]\n" EX"v98,v98\n" MF"a[4:7],v[48:51],v[116:119],a[4:7]\n" BL"v1,s[24:27],s41 offen offset:2048 sc1 lds\n" CV"v120,v96,v97\n"
 WL"(4)\n" MF"a[8:11],v[52:55],v[112:115],a[8:11]\n" EX"v99,v99\n" MF"a[12:15],v[52:55],v[116:119],a[12:15]\n" DR"v[76:79],v3 offset:15360\n" DR"v[16:19],v2 offset:16384\n"
 MF"a[16:19],v[56:59],v[112:115],a[16:19]\n" EX"v100,v100\n" MF"a[20:23],v[56:59],v[116:119],a[20:23]\n" BL"v1,s[24:27],s41 offen offset:3072 sc1 lds\n" CV"v121,v98,v99\n" WL"(4)\n"
 MF"a[24:27],v[60:63],v[112:115],a[24:27]\n""s_mov_b32 m0,s42\n" EX"v101,v101\n" MF"a[28:31],v[60:63],v[116:119],a[28:31]\n" CV"v124,v100,v101\n" MF"a[32:35],v[64:67],v[112:115],a[32:35]\n"
 EX"v102,v102\n" MF"a[36:39],v[64:67],v[116:119],a[36:39]\n" DR"v[20:23],v2 offset:17408\n" DR"v[24:27],v2 offset:18432\n" WL"(4)\n" MF"a[40:43],v[68:71],v[112:115],a[40:43]\n" EX"v103,v103\n"
 MF"a[44:47],v[68:71],v[116:119],a[44:47]\n" BL"v0,s[20:23],s40 offen offset:0 sc1 lds\n" CV"v125,v102,v103\n" MF"a[48:51],v[72:75],v[112:115],a[48:51]\n" EX"v104,v104\n"
 MF"a[52:55],v[72:75],v[116:119],a[52:55]\n" DR"v[28:31],v2 offset:19456\n" DR"v[32:35],v2 offset:20480\n" WL"(4)\n" MF"a[56:59],v[76:79],v[112:115],a[56:59]\n" EX"v105,v105\n"
 MF"a[60:63],v[76:79],v[116:119],a[60:63]\n" CV"v122,v104,v105\n" MF"a[64:67],v[8:11],v[112:115],a[64:67]\n" EX"v106,v106\n" MF"a[68:71],v[8:11],v[116:119],a[68:71]\n" DR"v[36:39],v2 offset:21504\n"
 DR"v[40:43],v2 offset:22528\n" MF"v[80:83],v[16:19],v[128:131],0\n" EX"v107,v107\n" MF"v[84:87],v[16:19],v[144:147],0\n" BL"v0,s[20:23],s40 offen offset:1024 sc1 lds\n" CV"v123,v106,v107\n" WL"(4)\n"
 MF"v[80:83],v[20:23],v[132:135],v[80:83]\n" EX"v108,v108\n" MF"v[84:87],v[20:23],v[148:151],v[84:87]\n" DR"v[44:47],v2 offset:23552\n" DR"v[48:51],v3 offset:16384\n"
 MF"v[80:83],v[24:27],v[136:139],v[80:83]\n" EX"v109,v109\n" MF"v[84:87],v[24:27],v[152:155],v[84:87]\n" CV"v126,v108,v109\n" WL"(4)\n" MF"v[80:83],v[28:31],v[140:143],v[80:83]\n" EX"v110,v110\n"
 MF"v[84:87],v[28:31],v[156:159],v[84:87]\n" DR"v[52:55],v3 offset:17408\n" DR"v[56:59],v3 offset:18432\n" MF"v[88:91],v[32:35],v[128:131],0\n" EX"v111,v111\n" MF"v[92:95],v[32:35],v[144:147],0\n"
 CV"v127,v110,v111\n" WL"(4)\n" MF"v[88:91],v[36:39],v[132:135],v[88:91]\n" MF"v[92:95],v[36:39],v[148:151],v[92:95]\n" DR"v[60:63],v3 offset:19456\n" DR"v[64:67],v3 offset:20480\n"
 MF"v[88:91],v[40:43],v[136:139],v[88:91]\n" EX"v80,v80\n" MF"v[92:95],v[40:43],v[152:155],v[92:95]\n" BL"v0,s[20:23],s40 offen offset:2048 sc1 lds\n" WL"(4)\n"
 MF"v[88:91],v[44:47],v[140:143],v[88:91]\n" EX"v81,v81\n" MF"v[92:95],v[44:47],v[156:159],v[92:95]\n" DR"v[68:71],v3 offset:21504\n" DR"v[72:75],v3 offset:22528\n"
 MF"a[0:3],v[48:51],v[120:123],a[0:3]\n" EX"v82,v82\n" MF"a[4:7],v[48:51],v[124:127],a[4:7]\n" CV"v112,v80,v81\n" WL"(4)\n" MF"a[8:11],v[52:55],v[120:123],a[8:11]\n" EX"v83,v83\n"
 MF"a[12:15],v[52:55],v[124:127],a[12:15]\n" DR"v[76:79],v3 offset:23552\n" DR"v[16:19],v2 offset:24576\n" MF"a[16:19],v[56:59],v[120:123],a[16:19]\n" EX"v84,v84\n"
 MF"a[20:23],v[56:59],v[124:127],a[20:23]\n" BL"v0,s[20:23],s40 offen offset:3072 sc1 lds\n" CV"v113,v82,v83\n" WL"(4)\n" MF"a[24:27],v[60:63],v[120:123],a[24:27]\n" SA"m0,s42,4096\n" EX"v85,v85\n"
 MF"a[28:31],v[60:63],v[124:127],a[28:31]\n" SA"s50,s44,32768\n" CV"v116,v84,v85\n" MF"a[32:35],v[64:67],v[120:123],a[32:35]\n""s_cmp_eq_u32 s50,98304\n" EX"v86,v86\n"
 MF"a[36:39],v[64:67],v[124:127],a[36:39]\n" DR"v[20:23],v2 offset:25600\n" DR"v[24:27],v2 offset:26624\n" WL"(4)\n" MF"a[40:43],v[68:71],v[120:123],a[40:43]\n""s_cselect_b32 s50,0,s50\n"
 EX"v87,v87\n" MF"a[44:47],v[68:71],v[124:127],a[44:47]\n""s_xor_b32 s43,s43,0x38000\n" CV"v117,v86,v87\n" MF"a[48:51],v[72:75],v[120:123],a[48:51]\n" SA"s41,s41,32768\n" EX"v88,v88\n"
 MF"a[52:55],v[72:75],v[124:127],a[52:55]\n" DR"v[28:31],v2 offset:27648\n" DR"v[32:35],v2 offset:28672\n" WL"(4)\n" MF"a[56:59],v[76:79],v[120:123],a[56:59]\n""s_cmp_eq_u32 s41,s49\n" EX"v89,v89\n"
 MF"a[60:63],v[76:79],v[124:127],a[60:63]\n" BL"v1,s[20:23],s40 offen offset:0 sc1 lds\n" CV"v114,v88,v89\n" MF"a[64:67],v[8:11],v[120:123],a[64:67]\n""s_cselect_b32 s41,s58,s41\n" EX"v90,v90\n"
 MF"a[68:71],v[8:11],v[124:127],a[68:71]\n" DR"v[36:39],v2 offset:29696\n" DR"v[40:43],v2 offset:30720\n" MF"v[96:99],v[16:19],v[128:131],0\n""s_cselect_b32 s49,-1,s49\n" EX"v91,v91\n"
 MF"v[100:103],v[16:19],v[144:147],0\n" CV"v115,v90,v91\n" WL"(4)\n" MF"v[96:99],v[20:23],v[132:135],v[96:99]\n" EX"v92,v92\n" MF"v[100:103],v[20:23],v[148:151],v[100:103]\n"
 DR"v[44:47],v2 offset:31744\n" DR"v[48:51],v3 offset:24576\n" MF"v[96:99],v[24:27],v[136:139],v[96:99]\n" EX"v93,v93\n" MF"v[100:103],v[24:27],v[152:155],v[100:103]\n" CV"v118,v92,v93\n" WL"(4)\n"
 MF"v[96:99],v[28:31],v[140:143],v[96:99]\n" EX"v94,v94\n" MF"v[100:103],v[28:31],v[156:159],v[100:103]\n" DR"v[52:55],v3 offset:25600\n" DR"v[56:59],v3 offset:26624\n"
 MF"v[104:107],v[32:35],v[128:131],0\n" EX"v95,v95\n" MF"v[108:111],v[32:35],v[144:147],0\n" BL"v1,s[20:23],s40 offen offset:1024 sc1 lds\n" CV"v119,v94,v95\n" WL"(4)\n"
 MF"v[104:107],v[36:39],v[132:135],v[104:107]\n" MF"v[108:111],v[36:39],v[148:151],v[108:111]\n" DR"v[60:63],v3 offset:27648\n" DR"v[64:67],v3 offset:28672\n"
 MF"v[104:107],v[40:43],v[136:139],v[104:107]\n" EX"v96,v96\n" MF"v[108:111],v[40:43],v[152:155],v[108:111]\n" WL"(4)\n" MF"v[104:107],v[44:47],v[140:143],v[104:107]\n" EX"v97,v97\n"
 MF"v[108:111],v[44:47],v[156:159],v[108:111]\n" DR"v[68:71],v3 offset:29696\n" DR"v[72:75],v3 offset:30720\n" MF"a[0:3],v[48:51],v[112:115],a[0:3]\n" EX"v98,v98\n"
 MF"a[4:7],v[48:51],v[116:119],a[4:7]\n" DR"v[76:79],v3 offset:31744\n" CV"v120,v96,v97\n" WL"(5)\n" MF"a[8:11],v[52:55],v[112:115],a[8:11]\n" EX"v99,v99\n" MF"a[12:15],v[52:55],v[116:119],a[12:15]\n"
 CV"v121,v98,v99\n""v_add_u32 v2,s50,v0\n" MF"a[16:19],v[56:59],v[112:115],a[16:19]\n" SA"s42,s44,s46\n" EX"v100,v100\n" MF"a[20:23],v[56:59],v[116:119],a[20:23]\n""s_mov_b32 s44,s50\n"
"v_xor_b32 v3,0x38000,v3\n" WL"(3)\n" MF"a[24:27],v[60:63],v[112:115],a[24:27]\n" WV"(6) lgkmcnt(0)\n""s_barrier\n" EX"v101,v101\n" MF"a[28:31],v[60:63],v[116:119],a[28:31]\n"
 DR"v[16:19],v2 offset:0\n" DR"v[20:23],v2 offset:1024\n" CV"v124,v100,v101\n" MF"a[32:35],v[64:67],v[112:115],a[32:35]\n" BL"v1,s[20:23],s40 offen offset:2048 sc1 lds\n" EX"v102,v102\n"
 MF"a[36:39],v[64:67],v[116:119],a[36:39]\n" DR"v[24:27],v2 offset:2048\n" DR"v[28:31],v2 offset:3072\n" BL"v1,s[20:23],s40 offen offset:3072 sc1 lds\n" MF"a[40:43],v[68:71],v[112:115],a[40:43]\n"
 SA"s40,s40,32768\n" EX"v103,v103\n" MF"a[44:47],v[68:71],v[116:119],a[44:47]\n" DR"v[32:35],v2 offset:4096\n" CV"v125,v102,v103\n" MF"a[48:51],v[72:75],v[112:115],a[48:51]\n""s_cmp_eq_u32 s40,s48\n"
 EX"v104,v104\n" MF"a[52:55],v[72:75],v[116:119],a[52:55]\n""s_cselect_b32 s40,s46,s40\n""s_cselect_b32 s48,-1,s48\n" MF"a[56:59],v[76:79],v[112:115],a[56:59]\n""s_sub_u32 s39,s39,1\n" EX"v105,v105\n"
 MF"a[60:63],v[76:79],v[116:119],a[60:63]\n""s_cmp_lg_u32 s39,0\n" CV"v122,v104,v105\n" MF"a[64:67],v[8:11],v[112:115],a[64:67]\n" EX"v106,v106\n" MF"a[68:71],v[8:11],v[116:119],a[68:71]\n"
"s_cbranch_scc1 Lloop_%=\n""Ltail_%=:\n""s_cmp_eq_u32 s45,0\n""s_cbranch_scc1 Lnp3_%=\n""v_mbcnt_lo_u32_b32 v247,-1,0\n""v_mbcnt_hi_u32_b32 v247,-1,v247\n""v_min_u32 v247,19,v247\n"
"s_lshr_b32 s62,s96,5\n""s_and_b32 s63,s96,7\n""s_lshl_b32 s63,s63,2\n""s_bfe_u32 s57,s96,0x20003\n" SA"s63,s63,s57\n""s_lshl_b32 s61,s63,2\n" SA"s61,s61,s36\n""s_lshl_b32 s57,s63,6\n"
 SA"s57,s57,16384\n" VV"v244,s57\n" SA"s57,s62,0\n""s_lshl_b32 s57,s57,5\n" SA"s57,s57,4096\n""s_sub_u32 s57,s57,64\n" VV"v245,s57\n""v_cmp_le_u32 vcc,8,v247\n""s_nop 1\n"
"v_cndmask_b32 v244,v244,v245,vcc\n" SA"s57,s62,8\n""s_lshl_b32 s57,s57,5\n" SA"s57,s57,4096\n""s_sub_u32 s57,s57,96\n" VV"v245,s57\n""v_cmp_le_u32 vcc,12,v247\n""s_nop 1\n"
"v_cndmask_b32 v244,v244,v245,vcc\n" SA"s57,s62,16\n""s_lshl_b32 s57,s57,5\n" SA"s57,s57,4096\n""s_sub_u32 s57,s57,128\n" VV"v245,s57\n""v_cmp_le_u32 vcc,16,v247\n""s_nop 1\n"
"v_cndmask_b32 v244,v244,v245,vcc\n""v_lshl_add_u32 v244,v247,3,v244\n""buffer_load_dwordx2 v[248:249],v244,s[92:95],0 offen sc1\n""Lnp3_%=:\n""s_cmp_eq_u32 s36,1\n""s_cbranch_scc1 Ltail1_%=\n"
"s_cmp_eq_u32 s36,2\n""s_cbranch_scc1 Ltail2_%=\n""s_cmp_eq_u32 s36,3\n""s_cbranch_scc1 Ltail3_%=\n""Ltail0_%=:\n" WL"(3)\n" MF"v[80:83],v[16:19],v[128:131],v[192:195]\n""s_mov_b32 m0,s43\n"
 EX"v107,v107\n" MF"v[84:87],v[16:19],v[144:147],0\n" DR"v[36:39],v2 offset:5120\n" DR"v[40:43],v2 offset:6144\n" MF"v[80:83],v[20:23],v[132:135],v[80:83]\n" EX"v108,v108\n"
 MF"v[84:87],v[20:23],v[148:151],v[84:87]\n" DR"v[44:47],v2 offset:7168\n" DR"v[48:51],v3 offset:0\n" BL"v0,s[24:27],s41 offen offset:0 sc1 lds\n" WL"(5)\n" MF"v[80:83],v[24:27],v[136:139],v[80:83]\n"
 EX"v109,v109\n" MF"v[84:87],v[24:27],v[152:155],v[84:87]\n" DR"v[52:55],v3 offset:1024\n" DR"v[56:59],v3 offset:2048\n" MF"v[80:83],v[28:31],v[140:143],v[80:83]\n" EX"v110,v110\n"
 MF"v[84:87],v[28:31],v[156:159],v[84:87]\n" DR"v[60:63],v3 offset:3072\n" DR"v[64:67],v3 offset:4096\n" WL"(7)\n" MF"v[92:95],v[32:35],v[144:147],0\n" EX"v111,v111\n"
 MF"v[92:95],v[36:39],v[148:151],v[92:95]\n" CV"v123,v106,v107\n" CV"v126,v108,v109\n" CV"v127,v110,v111\n" WL"(5)\n" MF"v[92:95],v[40:43],v[152:155],v[92:95]\n" VV"v114,0\n" VV"v115,0\n"
 MF"v[92:95],v[44:47],v[156:159],v[92:95]\n" EX"v80,v80\n" WL"(3)\n" MF"a[0:3],v[48:51],v[120:123],a[0:3]\n" DR"v[68:71],v3 offset:5120\n" DR"v[72:75],v3 offset:6144\n"
 MF"a[4:7],v[48:51],v[124:127],a[4:7]\n" EX"v81,v81\n" MF"a[8:11],v[52:55],v[120:123],a[8:11]\n" BL"v0,s[24:27],s41 offen offset:1024 sc1 lds\n" CV"v112,v80,v81\n"
 MF"a[12:15],v[52:55],v[124:127],a[12:15]\n" EX"v82,v82\n" WL"(3)\n" MF"a[16:19],v[56:59],v[120:123],a[16:19]\n" DR"v[76:79],v3 offset:7168\n" DR"v[16:19],v2 offset:8192\n"
 MF"a[20:23],v[56:59],v[124:127],a[20:23]\n" EX"v83,v83\n" MF"a[24:27],v[60:63],v[120:123],a[24:27]\n" BL"v0,s[24:27],s41 offen offset:2048 sc1 lds\n" CV"v113,v82,v83\n"
 MF"a[28:31],v[60:63],v[124:127],a[28:31]\n" EX"v84,v84\n" WL"(3)\n" MF"a[32:35],v[64:67],v[120:123],a[32:35]\n" DR"v[20:23],v2 offset:9216\n" DR"v[24:27],v2 offset:10240\n"
 MF"a[36:39],v[64:67],v[124:127],a[36:39]\n" EX"v85,v85\n" MF"a[40:43],v[68:71],v[120:123],a[40:43]\n" BL"v0,s[24:27],s41 offen offset:3072 sc1 lds\n" CV"v116,v84,v85\n"
 MF"a[44:47],v[68:71],v[124:127],a[44:47]\n" DR"v[28:31],v2 offset:11264\n" DR"v[32:35],v2 offset:12288\n" WL"(5)\n" MF"a[48:51],v[72:75],v[120:123],a[48:51]\n" SA"m0,s43,4096\n" EX"v86,v86\n"
 MF"a[52:55],v[72:75],v[124:127],a[52:55]\n" DR"v[36:39],v2 offset:13312\n" DR"v[40:43],v2 offset:14336\n" BL"v1,s[24:27],s41 offen offset:0 sc1 lds\n" MF"a[56:59],v[76:79],v[120:123],a[56:59]\n"
 EX"v87,v87\n" MF"a[60:63],v[76:79],v[124:127],a[60:63]\n" DR"v[44:47],v2 offset:15360\n" DR"v[48:51],v3 offset:8192\n" MF"a[64:67],v[8:11],v[120:123],a[64:67]\n" EX"v92,v92\n"
 MF"a[68:71],v[8:11],v[124:127],a[68:71]\n" DR"v[52:55],v3 offset:9216\n" DR"v[56:59],v3 offset:10240\n" WL"(9)\n" MF"v[100:103],v[16:19],v[144:147],0\n" EX"v93,v93\n"
 MF"v[100:103],v[20:23],v[148:151],v[100:103]\n" CV"v117,v86,v87\n" CV"v118,v92,v93\n" WL"(7)\n" MF"v[100:103],v[24:27],v[152:155],v[100:103]\n" EX"v94,v94\n"
 MF"v[100:103],v[28:31],v[156:159],v[100:103]\n" BL"v1,s[24:27],s41 offen offset:1024 sc1 lds\n" WL"(5)\n" MF"v[108:111],v[32:35],v[144:147],0\n" DR"v[60:63],v3 offset:11264\n"
 DR"v[64:67],v3 offset:12288\n" MF"v[108:111],v[36:39],v[148:151],v[108:111]\n" EX"v95,v95\n" WL"(5)\n" MF"v[108:111],v[40:43],v[152:155],v[108:111]\n" BL"v1,s[24:27],s41 offen offset:2048 sc1 lds\n"
 CV"v119,v94,v95\n" MF"v[108:111],v[44:47],v[156:159],v[108:111]\n" EX"v100,v100\n" WL"(3)\n" MF"a[0:3],v[48:51],v[112:115],a[0:3]\n" DR"v[68:71],v3 offset:13312\n" DR"v[72:75],v3 offset:14336\n"
 MF"a[4:7],v[48:51],v[116:119],a[4:7]\n" MF"a[8:11],v[52:55],v[112:115],a[8:11]\n" EX"v101,v101\n" MF"a[12:15],v[52:55],v[116:119],a[12:15]\n" BL"v1,s[24:27],s41 offen offset:3072 sc1 lds\n"
 CV"v124,v100,v101\n" WL"(3)\n" MF"a[16:19],v[56:59],v[112:115],a[16:19]\n" DR"v[76:79],v3 offset:15360\n" DR"v[16:19],v2 offset:16384\n" MF"a[20:23],v[56:59],v[116:119],a[20:23]\n"
"s_mov_b32 m0,s42\n" EX"v102,v102\n" MF"a[24:27],v[60:63],v[112:115],a[24:27]\n" MF"a[28:31],v[60:63],v[116:119],a[28:31]\n" BL"v0,s[20:23],s40 offen offset:0 sc1 lds\n" WL"(4)\n"
 MF"a[32:35],v[64:67],v[112:115],a[32:35]\n" DR"v[20:23],v2 offset:17408\n" DR"v[24:27],v2 offset:18432\n" MF"a[36:39],v[64:67],v[116:119],a[36:39]\n" EX"v103,v103\n" WL"(4)\n"
 MF"a[40:43],v[68:71],v[112:115],a[40:43]\n" CV"v125,v102,v103\n" MF"a[44:47],v[68:71],v[116:119],a[44:47]\n" DR"v[28:31],v2 offset:19456\n" DR"v[32:35],v2 offset:20480\n"
 MF"a[48:51],v[72:75],v[112:115],a[48:51]\n" EX"v108,v108\n" MF"a[52:55],v[72:75],v[116:119],a[52:55]\n" DR"v[36:39],v2 offset:21504\n" DR"v[40:43],v2 offset:22528\n" WL"(6)\n"
 MF"a[56:59],v[76:79],v[112:115],a[56:59]\n" BL"v0,s[20:23],s40 offen offset:1024 sc1 lds\n" MF"a[60:63],v[76:79],v[116:119],a[60:63]\n" DR"v[44:47],v2 offset:23552\n" DR"v[48:51],v3 offset:16384\n"
 MF"a[64:67],v[8:11],v[112:115],a[64:67]\n" EX"v109,v109\n" MF"a[68:71],v[8:11],v[116:119],a[68:71]\n" DR"v[52:55],v3 offset:17408\n" DR"v[56:59],v3 offset:18432\n"
 MF"v[84:87],v[16:19],v[144:147],0\n" DR"v[60:63],v3 offset:19456\n" DR"v[64:67],v3 offset:20480\n" WL"(10)\n" MF"v[84:87],v[20:23],v[148:151],v[84:87]\n" EX"v110,v110\n"
 MF"v[84:87],v[24:27],v[152:155],v[84:87]\n" BL"v0,s[20:23],s40 offen offset:2048 sc1 lds\n" CV"v126,v108,v109\n" WL"(8)\n" MF"v[84:87],v[28:31],v[156:159],v[84:87]\n" DR"v[68:71],v3 offset:21504\n"
 DR"v[72:75],v3 offset:22528\n" MF"v[92:95],v[32:35],v[144:147],0\n" DR"v[76:79],v3 offset:23552\n" DR"v[16:19],v2 offset:24576\n" WL"(10)\n" MF"v[92:95],v[36:39],v[148:151],v[92:95]\n"
 EX"v111,v111\n" MF"v[92:95],v[40:43],v[152:155],v[92:95]\n" CV"v127,v110,v111\n" WL"(8)\n" MF"v[92:95],v[44:47],v[156:159],v[92:95]\n" EX"v84,v84\n" MF"a[4:7],v[48:51],v[124:127],a[4:7]\n"
 DR"v[20:23],v2 offset:25600\n" DR"v[24:27],v2 offset:26624\n" WL"(8)\n" MF"a[12:15],v[52:55],v[124:127],a[12:15]\n" BL"v0,s[20:23],s40 offen offset:3072 sc1 lds\n" EX"v85,v85\n"
 MF"a[20:23],v[56:59],v[124:127],a[20:23]\n" DR"v[28:31],v2 offset:27648\n" DR"v[32:35],v2 offset:28672\n" WL"(8)\n" MF"a[28:31],v[60:63],v[124:127],a[28:31]\n" SA"m0,s42,4096\n" EX"v86,v86\n"
 MF"a[36:39],v[64:67],v[124:127],a[36:39]\n" DR"v[36:39],v2 offset:29696\n" DR"v[40:43],v2 offset:30720\n" WL"(8)\n" MF"a[44:47],v[68:71],v[124:127],a[44:47]\n" SA"s50,s44,32768\n" EX"v87,v87\n"
 MF"a[52:55],v[72:75],v[124:127],a[52:55]\n" DR"v[44:47],v2 offset:31744\n" DR"v[48:51],v3 offset:24576\n" WL"(8)\n" MF"a[60:63],v[76:79],v[124:127],a[60:63]\n""s_cmp_eq_u32 s50,98304\n" EX"v92,v92\n"
 MF"a[68:71],v[8:11],v[124:127],a[68:71]\n" DR"v[52:55],v3 offset:25600\n" DR"v[56:59],v3 offset:26624\n" BL"v1,s[20:23],s40 offen offset:0 sc1 lds\n" MF"v[100:103],v[16:19],v[144:147],0\n"
"s_cselect_b32 s50,0,s50\n" EX"v93,v93\n" WL"(8)\n" MF"v[100:103],v[20:23],v[148:151],v[100:103]\n" DR"v[60:63],v3 offset:27648\n" DR"v[64:67],v3 offset:28672\n"
 MF"v[100:103],v[24:27],v[152:155],v[100:103]\n""s_xor_b32 s43,s43,0x38000\n" EX"v94,v94\n" WL"(8)\n" MF"v[100:103],v[28:31],v[156:159],v[100:103]\n" DR"v[68:71],v3 offset:29696\n"
 DR"v[72:75],v3 offset:30720\n" MF"v[108:111],v[32:35],v[144:147],v[192:195]\n" DR"v[76:79],v3 offset:31744\n" CV"v116,v84,v85\n" WL"(9)\n" MF"v[108:111],v[36:39],v[148:151],v[108:111]\n"
 SA"s41,s41,32768\n" EX"v95,v95\n" MF"v[108:111],v[40:43],v[152:155],v[108:111]\n" CV"v117,v86,v87\n" CV"v118,v92,v93\n" CV"v119,v94,v95\n" WL"(7)\n" MF"v[108:111],v[44:47],v[156:159],v[108:111]\n"
"s_cmp_eq_u32 s41,s49\n" EX"v100,v100\n" MF"a[4:7],v[48:51],v[116:119],a[4:7]\n""s_cselect_b32 s41,s58,s41\n" EX"v101,v101\n" WL"(5)\n" MF"a[12:15],v[52:55],v[116:119],a[12:15]\n"
 BL"v1,s[20:23],s40 offen offset:1024 sc1 lds\n" VV"v153,v185\n" CV"v124,v100,v101\n" MF"a[20:23],v[56:59],v[116:119],a[20:23]\n""s_cselect_b32 s49,-1,s49\n" VV"v154,v186\n" EX"v102,v102\n" WL"(3)\n"
 MF"a[28:31],v[60:63],v[116:119],a[28:31]\n" VV"v155,v187\n""v_add_u32 v2,s50,v0\n""v_xor_b32 v3,0x38000,v3\n" MF"a[36:39],v[64:67],v[116:119],a[36:39]\n" SA"s42,s44,s46\n" VV"v156,v188\n"
 EX"v103,v103\n" WL"(1)\n" MF"a[44:47],v[68:71],v[116:119],a[44:47]\n""s_mov_b32 s44,s50\n" VV"v128,v160\n" VV"v157,v189\n" CV"v125,v102,v103\n" MF"a[52:55],v[72:75],v[116:119],a[52:55]\n"
 VV"v158,v190\n" EX"v108,v108\n" WL"(0)\n" MF"a[60:63],v[76:79],v[116:119],a[60:63]\n" VV"v129,v161\n" VV"v130,v162\n" VV"v159,v191\n" MF"a[68:71],v[8:11],v[116:119],a[68:71]\n" EX"v109,v109\n"
 WV"(6) lgkmcnt(0)\n""s_barrier\n" DR"v[36:39],v3 offset:0\n" DR"v[40:43],v3 offset:1024\n" DR"v[44:47],v3 offset:2048\n" DR"v[48:51],v3 offset:3072\n" DR"v[52:55],v3 offset:4096\n"
 DR"v[56:59],v3 offset:5120\n" BL"v1,s[20:23],s40 offen offset:2048 sc1 lds\n" EX"v110,v110\n" DR"v[60:63],v3 offset:6144\n" DR"v[64:67],v3 offset:7168\n" DR"v[16:19],v2 offset:0\n"
 DR"v[20:23],v2 offset:1024\n" DR"v[24:27],v2 offset:2048\n" DR"v[28:31],v2 offset:3072\n" EX"v111,v111\n" DR"v[32:35],v2 offset:4096\n" BL"v1,s[20:23],s40 offen offset:3072 sc1 lds\n"
 CV"v126,v108,v109\n" CV"v127,v110,v111\n" SA"s40,s40,32768\n""s_cmp_eq_u32 s40,s48\n""s_cselect_b32 s40,s46,s40\n""s_cselect_b32 s48,-1,s48\n" VV"v131,v163\n" VV"v132,v164\n" VV"v133,v165\n"
 VV"v134,v166\n" WL"(11)\n" MF"a[4:7],v[36:39],v[124:127],a[4:7]\n" VV"v135,v167\n" VV"v136,v168\n" MF"a[12:15],v[40:43],v[124:127],a[12:15]\n" VV"v137,v169\n" VV"v138,v170\n" WL"(9)\n"
 MF"a[20:23],v[44:47],v[124:127],a[20:23]\n" VV"v139,v171\n" VV"v140,v172\n" MF"a[28:31],v[48:51],v[124:127],a[28:31]\n" VV"v141,v173\n" VV"v142,v174\n" WL"(7)\n"
 MF"a[36:39],v[52:55],v[124:127],a[36:39]\n" VV"v143,v175\n" VV"v144,v176\n" MF"a[44:47],v[56:59],v[124:127],a[44:47]\n" VV"v145,v177\n" VV"v146,v178\n" WL"(5)\n"
 MF"a[52:55],v[60:63],v[124:127],a[52:55]\n" VV"v147,v179\n" VV"v148,v180\n" MF"a[60:63],v[64:67],v[124:127],a[60:63]\n" VV"v149,v181\n" VV"v150,v182\n" MF"a[68:71],v[8:11],v[124:127],a[68:71]\n"
 VV"v151,v183\n" VV"v152,v184\n""s_branch Ltend_%=\n""Ltail1_%=:\n" WL"(3)\n" MF"v[80:83],v[16:19],v[128:131],0\n""s_mov_b32 m0,s43\n" EX"v107,v107\n" MF"v[84:87],v[16:19],v[144:147],0\n"
 DR"v[36:39],v2 offset:5120\n" DR"v[40:43],v2 offset:6144\n" MF"v[80:83],v[20:23],v[132:135],v[80:83]\n" EX"v108,v108\n" MF"v[84:87],v[20:23],v[148:151],v[84:87]\n" DR"v[44:47],v2 offset:7168\n"
 DR"v[48:51],v3 offset:0\n" WL"(5)\n" MF"v[80:83],v[24:27],v[136:139],v[80:83]\n" EX"v109,v109\n" MF"v[84:87],v[24:27],v[152:155],v[84:87]\n" CV"v123,v106,v107\n" CV"v126,v108,v109\n"
 MF"v[80:83],v[28:31],v[140:143],v[80:83]\n" EX"v110,v110\n" MF"v[84:87],v[28:31],v[156:159],v[84:87]\n" DR"v[52:55],v3 offset:1024\n" DR"v[56:59],v3 offset:2048\n" WL"(6)\n"
 MF"v[88:91],v[32:35],v[128:131],v[192:195]\n" EX"v111,v111\n" MF"v[92:95],v[32:35],v[144:147],0\n" BL"v0,s[24:27],s41 offen offset:0 sc1 lds\n" CV"v127,v110,v111\n" WL"(4)\n"
 MF"v[88:91],v[36:39],v[132:135],v[88:91]\n" BL"v0,s[24:27],s41 offen offset:1024 sc1 lds\n" MF"v[92:95],v[36:39],v[148:151],v[92:95]\n" EX"v80,v80\n" MF"v[88:91],v[40:43],v[136:139],v[88:91]\n"
 EX"v81,v81\n" MF"v[92:95],v[40:43],v[152:155],v[92:95]\n" DR"v[60:63],v3 offset:3072\n" DR"v[64:67],v3 offset:4096\n" BL"v0,s[24:27],s41 offen offset:2048 sc1 lds\n" WL"(4)\n"
 MF"v[88:91],v[44:47],v[140:143],v[88:91]\n" EX"v82,v82\n" MF"v[92:95],v[44:47],v[156:159],v[92:95]\n" DR"v[68:71],v3 offset:5120\n" DR"v[72:75],v3 offset:6144\n"
 MF"a[0:3],v[48:51],v[120:123],a[0:3]\n" EX"v83,v83\n" MF"a[4:7],v[48:51],v[124:127],a[4:7]\n" CV"v112,v80,v81\n" CV"v113,v82,v83\n" WL"(4)\n" MF"a[8:11],v[52:55],v[120:123],a[8:11]\n" EX"v84,v84\n"
 MF"a[12:15],v[52:55],v[124:127],a[12:15]\n" DR"v[76:79],v3 offset:7168\n" DR"v[16:19],v2 offset:8192\n" MF"a[16:19],v[56:59],v[120:123],a[16:19]\n" EX"v85,v85\n"
 MF"a[20:23],v[56:59],v[124:127],a[20:23]\n" BL"v0,s[24:27],s41 offen offset:3072 sc1 lds\n" CV"v116,v84,v85\n" WL"(4)\n" MF"a[24:27],v[60:63],v[120:123],a[24:27]\n" SA"m0,s43,4096\n" EX"v86,v86\n"
 MF"a[28:31],v[60:63],v[124:127],a[28:31]\n" MF"a[32:35],v[64:67],v[120:123],a[32:35]\n" BL"v1,s[24:27],s41 offen offset:0 sc1 lds\n" EX"v87,v87\n" MF"a[36:39],v[64:67],v[124:127],a[36:39]\n"
 EX"v88,v88\n" WL"(2)\n" MF"a[40:43],v[68:71],v[120:123],a[40:43]\n" DR"v[20:23],v2 offset:9216\n" DR"v[24:27],v2 offset:10240\n" MF"a[44:47],v[68:71],v[124:127],a[44:47]\n" EX"v89,v89\n"
 MF"a[48:51],v[72:75],v[120:123],a[48:51]\n" DR"v[28:31],v2 offset:11264\n" DR"v[32:35],v2 offset:12288\n" MF"a[52:55],v[72:75],v[124:127],a[52:55]\n" BL"v1,s[24:27],s41 offen offset:1024 sc1 lds\n"
 EX"v90,v90\n" WL"(4)\n" MF"a[56:59],v[76:79],v[120:123],a[56:59]\n" DR"v[36:39],v2 offset:13312\n" DR"v[40:43],v2 offset:14336\n" MF"a[60:63],v[76:79],v[124:127],a[60:63]\n" EX"v91,v91\n"
 MF"a[64:67],v[8:11],v[120:123],a[64:67]\n" DR"v[44:47],v2 offset:15360\n" DR"v[48:51],v3 offset:8192\n" MF"a[68:71],v[8:11],v[124:127],a[68:71]\n" EX"v92,v92\n" MF"v[100:103],v[16:19],v[144:147],0\n"
 DR"v[52:55],v3 offset:9216\n" DR"v[56:59],v3 offset:10240\n" WL"(8)\n" MF"v[100:103],v[20:23],v[148:151],v[100:103]\n" BL"v1,s[24:27],s41 offen offset:2048 sc1 lds\n" EX"v93,v93\n"
 MF"v[100:103],v[24:27],v[152:155],v[100:103]\n" CV"v117,v86,v87\n" CV"v114,v88,v89\n" WL"(6)\n" MF"v[100:103],v[28:31],v[156:159],v[100:103]\n" EX"v94,v94\n" MF"v[108:111],v[32:35],v[144:147],0\n"
 DR"v[60:63],v3 offset:11264\n" DR"v[64:67],v3 offset:12288\n" WL"(6)\n" MF"v[108:111],v[36:39],v[148:151],v[108:111]\n" EX"v95,v95\n" MF"v[108:111],v[40:43],v[152:155],v[108:111]\n"
 CV"v115,v90,v91\n" CV"v118,v92,v93\n" CV"v119,v94,v95\n" WL"(4)\n" MF"v[108:111],v[44:47],v[156:159],v[108:111]\n" EX"v100,v100\n" MF"a[0:3],v[48:51],v[112:115],a[0:3]\n"
 DR"v[68:71],v3 offset:13312\n" DR"v[72:75],v3 offset:14336\n" MF"a[4:7],v[48:51],v[116:119],a[4:7]\n" BL"v1,s[24:27],s41 offen offset:3072 sc1 lds\n" WL"(4)\n"
 MF"a[8:11],v[52:55],v[112:115],a[8:11]\n""s_mov_b32 m0,s42\n" EX"v101,v101\n" MF"a[12:15],v[52:55],v[116:119],a[12:15]\n" CV"v124,v100,v101\n" MF"a[16:19],v[56:59],v[112:115],a[16:19]\n"
 DR"v[76:79],v3 offset:15360\n" DR"v[16:19],v2 offset:16384\n" MF"a[20:23],v[56:59],v[116:119],a[20:23]\n" EX"v102,v102\n" WL"(4)\n" MF"a[24:27],v[60:63],v[112:115],a[24:27]\n"
 BL"v0,s[20:23],s40 offen offset:0 sc1 lds\n" MF"a[28:31],v[60:63],v[116:119],a[28:31]\n" MF"a[32:35],v[64:67],v[112:115],a[32:35]\n" DR"v[20:23],v2 offset:17408\n" DR"v[24:27],v2 offset:18432\n"
 MF"a[36:39],v[64:67],v[116:119],a[36:39]\n" EX"v103,v103\n" WL"(4)\n" MF"a[40:43],v[68:71],v[112:115],a[40:43]\n" CV"v125,v102,v103\n" MF"a[44:47],v[68:71],v[116:119],a[44:47]\n"
 DR"v[28:31],v2 offset:19456\n" DR"v[32:35],v2 offset:20480\n" MF"a[48:51],v[72:75],v[112:115],a[48:51]\n" EX"v108,v108\n" MF"a[52:55],v[72:75],v[116:119],a[52:55]\n" DR"v[36:39],v2 offset:21504\n"
 DR"v[40:43],v2 offset:22528\n" WL"(6)\n" MF"a[56:59],v[76:79],v[112:115],a[56:59]\n" BL"v0,s[20:23],s40 offen offset:1024 sc1 lds\n" MF"a[60:63],v[76:79],v[116:119],a[60:63]\n"
 DR"v[44:47],v2 offset:23552\n" DR"v[48:51],v3 offset:16384\n" MF"a[64:67],v[8:11],v[112:115],a[64:67]\n" EX"v109,v109\n" MF"a[68:71],v[8:11],v[116:119],a[68:71]\n" DR"v[52:55],v3 offset:17408\n"
 DR"v[56:59],v3 offset:18432\n" MF"v[84:87],v[16:19],v[144:147],0\n" DR"v[60:63],v3 offset:19456\n" DR"v[64:67],v3 offset:20480\n" WL"(10)\n" MF"v[84:87],v[20:23],v[148:151],v[84:87]\n"
 EX"v110,v110\n" MF"v[84:87],v[24:27],v[152:155],v[84:87]\n" BL"v0,s[20:23],s40 offen offset:2048 sc1 lds\n" CV"v126,v108,v109\n" WL"(8)\n" MF"v[84:87],v[28:31],v[156:159],v[84:87]\n"
 DR"v[68:71],v3 offset:21504\n" DR"v[72:75],v3 offset:22528\n" MF"v[92:95],v[32:35],v[144:147],0\n" DR"v[76:79],v3 offset:23552\n" DR"v[16:19],v2 offset:24576\n" WL"(10)\n"
 MF"v[92:95],v[36:39],v[148:151],v[92:95]\n" EX"v111,v111\n" MF"v[92:95],v[40:43],v[152:155],v[92:95]\n" BL"v0,s[20:23],s40 offen offset:3072 sc1 lds\n" CV"v127,v110,v111\n" WL"(8)\n"
 MF"v[92:95],v[44:47],v[156:159],v[92:95]\n" SA"m0,s42,4096\n" EX"v84,v84\n" MF"a[4:7],v[48:51],v[124:127],a[4:7]\n" SA"s50,s44,32768\n" EX"v85,v85\n" WL"(6)\n"
 MF"a[12:15],v[52:55],v[124:127],a[12:15]\n" DR"v[20:23],v2 offset:25600\n" DR"v[24:27],v2 offset:26624\n" CV"v116,v84,v85\n" MF"a[20:23],v[56:59],v[124:127],a[20:23]\n""s_cmp_eq_u32 s50,98304\n"
 EX"v86,v86\n" WL"(6)\n" MF"a[28:31],v[60:63],v[124:127],a[28:31]\n""s_cselect_b32 s50,0,s50\n" EX"v87,v87\n" MF"a[36:39],v[64:67],v[124:127],a[36:39]\n" DR"v[28:31],v2 offset:27648\n"
 DR"v[32:35],v3 offset:24576\n" CV"v117,v86,v87\n" WL"(6)\n" MF"a[44:47],v[68:71],v[124:127],a[44:47]\n" BL"v1,s[20:23],s40 offen offset:0 sc1 lds\n" EX"v92,v92\n"
 MF"a[52:55],v[72:75],v[124:127],a[52:55]\n""s_xor_b32 s43,s43,0x38000\n" EX"v93,v93\n" WL"(4)\n" MF"a[60:63],v[76:79],v[124:127],a[60:63]\n" DR"v[36:39],v3 offset:25600\n"
 DR"v[40:43],v3 offset:26624\n" CV"v118,v92,v93\n" MF"a[68:71],v[8:11],v[124:127],a[68:71]\n" SA"s41,s41,32768\n" EX"v94,v94\n" MF"v[100:103],v[16:19],v[144:147],v[192:195]\n" VV"v126,0\n"
 VV"v127,0\n" WL"(4)\n" MF"v[100:103],v[20:23],v[148:151],v[100:103]\n""s_cmp_eq_u32 s41,s49\n" EX"v95,v95\n" MF"v[100:103],v[24:27],v[152:155],v[100:103]\n" DR"v[44:47],v3 offset:27648\n"
 DR"v[48:51],v3 offset:28672\n" CV"v119,v94,v95\n" WL"(4)\n" MF"v[100:103],v[28:31],v[156:159],v[100:103]\n" DR"v[52:55],v3 offset:29696\n" DR"v[56:59],v3 offset:30720\n"
 MF"a[4:7],v[32:35],v[116:119],a[4:7]\n" DR"v[60:63],v3 offset:31744\n" BL"v1,s[20:23],s40 offen offset:1024 sc1 lds\n" WL"(5)\n" MF"a[12:15],v[36:39],v[116:119],a[12:15]\n""v_add_u32 v2,s50,v0\n"
"v_xor_b32 v3,0x38000,v3\n" MF"a[20:23],v[40:43],v[116:119],a[20:23]\n""s_cselect_b32 s41,s58,s41\n""s_cselect_b32 s49,-1,s49\n" VV"v128,v160\n" VV"v129,v161\n" WL"(4)\n"
 MF"a[28:31],v[44:47],v[116:119],a[28:31]\n" SA"s42,s44,s46\n" EX"v100,v100\n" WL"(3)\n" MF"a[36:39],v[48:51],v[116:119],a[36:39]\n""s_mov_b32 s44,s50\n" VV"v130,v162\n" VV"v131,v163\n" WL"(1)\n"
 MF"a[44:47],v[52:55],v[116:119],a[44:47]\n" VV"v132,v164\n" VV"v133,v165\n" MF"a[52:55],v[56:59],v[116:119],a[52:55]\n" EX"v101,v101\n" WL"(0)\n" MF"a[60:63],v[60:63],v[116:119],a[60:63]\n"
 VV"v134,v166\n" CV"v124,v100,v101\n" MF"a[68:71],v[8:11],v[116:119],a[68:71]\n" VV"v135,v167\n" VV"v136,v168\n" WV"(6) lgkmcnt(0)\n""s_barrier\n" DR"v[64:67],v3 offset:0\n"
 DR"v[68:71],v3 offset:1024\n" EX"v102,v102\n" DR"v[72:75],v3 offset:2048\n" DR"v[76:79],v3 offset:3072\n" DR"v[36:39],v3 offset:4096\n" DR"v[40:43],v3 offset:5120\n"
 BL"v1,s[20:23],s40 offen offset:2048 sc1 lds\n" DR"v[44:47],v3 offset:6144\n" DR"v[48:51],v3 offset:7168\n" DR"v[16:19],v2 offset:0\n" DR"v[20:23],v2 offset:1024\n" DR"v[24:27],v2 offset:2048\n"
 DR"v[28:31],v2 offset:3072\n" EX"v103,v103\n" DR"v[32:35],v2 offset:4096\n" BL"v1,s[20:23],s40 offen offset:3072 sc1 lds\n" VV"v137,v169\n" CV"v125,v102,v103\n" SA"s40,s40,32768\n"
"s_cmp_eq_u32 s40,s48\n""s_cselect_b32 s40,s46,s40\n""s_cselect_b32 s48,-1,s48\n" VV"v138,v170\n" VV"v139,v171\n" VV"v140,v172\n" VV"v141,v173\n" WL"(11)\n" MF"a[4:7],v[64:67],v[124:127],a[4:7]\n"
 VV"v142,v174\n" VV"v143,v175\n" MF"a[12:15],v[68:71],v[124:127],a[12:15]\n" VV"v144,v176\n" VV"v145,v177\n" WL"(9)\n" MF"a[20:23],v[72:75],v[124:127],a[20:23]\n" VV"v146,v178\n" VV"v147,v179\n"
 MF"a[28:31],v[76:79],v[124:127],a[28:31]\n" VV"v148,v180\n" VV"v149,v181\n" WL"(7)\n" MF"a[36:39],v[36:39],v[124:127],a[36:39]\n" VV"v150,v182\n" VV"v151,v183\n"
 MF"a[44:47],v[40:43],v[124:127],a[44:47]\n" VV"v152,v184\n" VV"v153,v185\n" WL"(5)\n" MF"a[52:55],v[44:47],v[124:127],a[52:55]\n" VV"v154,v186\n" VV"v155,v187\n"
 MF"a[60:63],v[48:51],v[124:127],a[60:63]\n" VV"v156,v188\n" VV"v157,v189\n" MF"a[68:71],v[8:11],v[124:127],a[68:71]\n" VV"v158,v190\n" VV"v159,v191\n""s_branch Ltend_%=\n""Ltail2_%=:\n" WL"(3)\n"
 MF"v[80:83],v[16:19],v[128:131],0\n""s_mov_b32 m0,s43\n" EX"v107,v107\n" MF"v[84:87],v[16:19],v[144:147],0\n" DR"v[36:39],v2 offset:5120\n" DR"v[40:43],v2 offset:6144\n"
 MF"v[80:83],v[20:23],v[132:135],v[80:83]\n" EX"v108,v108\n" MF"v[84:87],v[20:23],v[148:151],v[84:87]\n" DR"v[44:47],v2 offset:7168\n" DR"v[48:51],v3 offset:0\n" WL"(5)\n"
 MF"v[80:83],v[24:27],v[136:139],v[80:83]\n" EX"v109,v109\n" MF"v[84:87],v[24:27],v[152:155],v[84:87]\n" CV"v123,v106,v107\n" CV"v126,v108,v109\n" MF"v[80:83],v[28:31],v[140:143],v[80:83]\n"
 EX"v110,v110\n" MF"v[84:87],v[28:31],v[156:159],v[84:87]\n" DR"v[52:55],v3 offset:1024\n" DR"v[56:59],v3 offset:2048\n" WL"(6)\n" MF"v[88:91],v[32:35],v[128:131],0\n" EX"v111,v111\n"
 MF"v[92:95],v[32:35],v[144:147],0\n" BL"v0,s[24:27],s41 offen offset:0 sc1 lds\n" CV"v127,v110,v111\n" WL"(4)\n" MF"v[88:91],v[36:39],v[132:135],v[88:91]\n"
 BL"v0,s[24:27],s41 offen offset:1024 sc1 lds\n" MF"v[92:95],v[36:39],v[148:151],v[92:95]\n" EX"v80,v80\n" MF"v[88:91],v[40:43],v[136:139],v[88:91]\n" DR"v[60:63],v3 offset:3072\n"
 DR"v[64:67],v3 offset:4096\n" MF"v[92:95],v[40:43],v[152:155],v[92:95]\n" EX"v81,v81\n" WL"(4)\n" MF"v[88:91],v[44:47],v[140:143],v[88:91]\n" BL"v0,s[24:27],s41 offen offset:2048 sc1 lds\n"
 CV"v112,v80,v81\n" MF"v[92:95],v[44:47],v[156:159],v[92:95]\n" EX"v82,v82\n" MF"a[0:3],v[48:51],v[120:123],a[0:3]\n" DR"v[68:71],v3 offset:5120\n" DR"v[72:75],v3 offset:6144\n"
 MF"a[4:7],v[48:51],v[124:127],a[4:7]\n" EX"v83,v83\n" WL"(4)\n" MF"a[8:11],v[52:55],v[120:123],a[8:11]\n" CV"v113,v82,v83\n" MF"a[12:15],v[52:55],v[124:127],a[12:15]\n" EX"v84,v84\n"
 MF"a[16:19],v[56:59],v[120:123],a[16:19]\n" DR"v[76:79],v3 offset:7168\n" DR"v[16:19],v2 offset:8192\n" MF"a[20:23],v[56:59],v[124:127],a[20:23]\n" EX"v85,v85\n" WL"(4)\n"
 MF"a[24:27],v[60:63],v[120:123],a[24:27]\n" BL"v0,s[24:27],s41 offen offset:3072 sc1 lds\n" CV"v116,v84,v85\n" MF"a[28:31],v[60:63],v[124:127],a[28:31]\n" SA"m0,s43,4096\n" EX"v86,v86\n"
 MF"a[32:35],v[64:67],v[120:123],a[32:35]\n" MF"a[36:39],v[64:67],v[124:127],a[36:39]\n" EX"v87,v87\n" WL"(2)\n" MF"a[40:43],v[68:71],v[120:123],a[40:43]\n" DR"v[20:23],v2 offset:9216\n"
 DR"v[24:27],v2 offset:10240\n" MF"a[44:47],v[68:71],v[124:127],a[44:47]\n" BL"v1,s[24:27],s41 offen offset:0 sc1 lds\n" CV"v117,v86,v87\n" MF"a[48:51],v[72:75],v[120:123],a[48:51]\n" EX"v88,v88\n"
 MF"a[52:55],v[72:75],v[124:127],a[52:55]\n" DR"v[28:31],v2 offset:11264\n" DR"v[32:35],v2 offset:12288\n" WL"(4)\n" MF"a[56:59],v[76:79],v[120:123],a[56:59]\n" EX"v89,v89\n"
 MF"a[60:63],v[76:79],v[124:127],a[60:63]\n" BL"v1,s[24:27],s41 offen offset:1024 sc1 lds\n" CV"v114,v88,v89\n" MF"a[64:67],v[8:11],v[120:123],a[64:67]\n" EX"v90,v90\n"
 MF"a[68:71],v[8:11],v[124:127],a[68:71]\n" DR"v[36:39],v2 offset:13312\n" DR"v[40:43],v2 offset:14336\n" MF"v[96:99],v[16:19],v[128:131],v[192:195]\n" EX"v91,v91\n"
 MF"v[100:103],v[16:19],v[144:147],0\n" BL"v1,s[24:27],s41 offen offset:2048 sc1 lds\n" VV"v122,0\n" VV"v123,0\n" WL"(4)\n" MF"v[96:99],v[20:23],v[132:135],v[96:99]\n" EX"v92,v92\n"
 MF"v[100:103],v[20:23],v[148:151],v[100:103]\n" DR"v[44:47],v2 offset:15360\n" DR"v[48:51],v3 offset:8192\n" MF"v[96:99],v[24:27],v[136:139],v[96:99]\n" EX"v93,v93\n"
 MF"v[100:103],v[24:27],v[152:155],v[100:103]\n" DR"v[52:55],v3 offset:9216\n" DR"v[56:59],v3 offset:10240\n" WL"(6)\n" MF"v[96:99],v[28:31],v[140:143],v[96:99]\n" EX"v94,v94\n"
 MF"v[100:103],v[28:31],v[156:159],v[100:103]\n" CV"v115,v90,v91\n" CV"v118,v92,v93\n" MF"v[108:111],v[32:35],v[144:147],0\n" DR"v[60:63],v3 offset:11264\n" DR"v[64:67],v3 offset:12288\n" WL"(6)\n"
 MF"v[108:111],v[36:39],v[148:151],v[108:111]\n" EX"v95,v95\n" MF"v[108:111],v[40:43],v[152:155],v[108:111]\n" BL"v1,s[24:27],s41 offen offset:3072 sc1 lds\n" CV"v119,v94,v95\n" WL"(4)\n"
 MF"v[108:111],v[44:47],v[156:159],v[108:111]\n""s_mov_b32 m0,s42\n" EX"v96,v96\n" MF"a[0:3],v[48:51],v[112:115],a[0:3]\n" DR"v[68:71],v3 offset:13312\n" DR"v[72:75],v3 offset:14336\n"
 MF"a[4:7],v[48:51],v[116:119],a[4:7]\n" EX"v97,v97\n" WL"(4)\n" MF"a[8:11],v[52:55],v[112:115],a[8:11]\n" BL"v0,s[20:23],s40 offen offset:0 sc1 lds\n" CV"v120,v96,v97\n"
 MF"a[12:15],v[52:55],v[116:119],a[12:15]\n" EX"v98,v98\n" MF"a[16:19],v[56:59],v[112:115],a[16:19]\n" DR"v[76:79],v3 offset:15360\n" DR"v[16:19],v2 offset:16384\n"
 MF"a[20:23],v[56:59],v[116:119],a[20:23]\n" EX"v99,v99\n" WL"(4)\n" MF"a[24:27],v[60:63],v[112:115],a[24:27]\n" CV"v121,v98,v99\n" MF"a[28:31],v[60:63],v[116:119],a[28:31]\n" EX"v100,v100\n"
 MF"a[32:35],v[64:67],v[112:115],a[32:35]\n" DR"v[20:23],v2 offset:17408\n" DR"v[24:27],v2 offset:18432\n" MF"a[36:39],v[64:67],v[116:119],a[36:39]\n" EX"v101,v101\n" WL"(4)\n"
 MF"a[40:43],v[68:71],v[112:115],a[40:43]\n" BL"v0,s[20:23],s40 offen offset:1024 sc1 lds\n" CV"v124,v100,v101\n" MF"a[44:47],v[68:71],v[116:119],a[44:47]\n" DR"v[28:31],v2 offset:19456\n"
 DR"v[32:35],v2 offset:20480\n" MF"a[48:51],v[72:75],v[112:115],a[48:51]\n" EX"v102,v102\n" MF"a[52:55],v[72:75],v[116:119],a[52:55]\n" DR"v[36:39],v2 offset:21504\n" DR"v[40:43],v2 offset:22528\n"
 WL"(6)\n" MF"a[56:59],v[76:79],v[112:115],a[56:59]\n" BL"v0,s[20:23],s40 offen offset:2048 sc1 lds\n" EX"v103,v103\n" MF"a[60:63],v[76:79],v[116:119],a[60:63]\n" DR"v[44:47],v2 offset:23552\n"
 DR"v[48:51],v3 offset:16384\n" MF"a[64:67],v[8:11],v[112:115],a[64:67]\n" EX"v108,v108\n" MF"a[68:71],v[8:11],v[116:119],a[68:71]\n" DR"v[52:55],v3 offset:17408\n" DR"v[56:59],v3 offset:18432\n"
 MF"v[84:87],v[16:19],v[144:147],0\n" EX"v109,v109\n" WL"(8)\n" MF"v[84:87],v[20:23],v[148:151],v[84:87]\n" CV"v125,v102,v103\n" CV"v126,v108,v109\n" MF"v[84:87],v[24:27],v[152:155],v[84:87]\n"
 EX"v110,v110\n" WL"(6)\n" MF"v[84:87],v[28:31],v[156:159],v[84:87]\n" BL"v0,s[20:23],s40 offen offset:3072 sc1 lds\n" MF"v[92:95],v[32:35],v[144:147],v[192:195]\n" DR"v[60:63],v3 offset:19456\n"
 DR"v[64:67],v3 offset:20480\n" WL"(6)\n" MF"v[92:95],v[36:39],v[148:151],v[92:95]\n" SA"m0,s42,4096\n" EX"v111,v111\n" MF"v[92:95],v[40:43],v[152:155],v[92:95]\n" SA"s50,s44,32768\n"
 CV"v127,v110,v111\n" WL"(4)\n" MF"v[92:95],v[44:47],v[156:159],v[92:95]\n""s_cmp_eq_u32 s50,98304\n" EX"v84,v84\n" MF"a[0:3],v[48:51],v[120:123],a[0:3]\n" DR"v[68:71],v3 offset:21504\n"
 DR"v[72:75],v3 offset:22528\n" MF"a[4:7],v[48:51],v[124:127],a[4:7]\n""s_cselect_b32 s50,0,s50\n" VV"v142,v174\n" EX"v85,v85\n" WL"(4)\n" MF"a[8:11],v[52:55],v[120:123],a[8:11]\n"
 BL"v1,s[20:23],s40 offen offset:0 sc1 lds\n" VV"v143,v175\n" CV"v116,v84,v85\n" MF"a[12:15],v[52:55],v[124:127],a[12:15]\n""s_xor_b32 s43,s43,0x38000\n" VV"v144,v176\n" EX"v86,v86\n"
 MF"a[16:19],v[56:59],v[120:123],a[16:19]\n" DR"v[76:79],v3 offset:23552\n" DR"v[36:39],v3 offset:24576\n" VV"v145,v177\n" MF"a[20:23],v[56:59],v[124:127],a[20:23]\n" SA"s41,s41,32768\n"
 VV"v146,v178\n" EX"v87,v87\n" WL"(4)\n" MF"a[24:27],v[60:63],v[120:123],a[24:27]\n""s_cmp_eq_u32 s41,s49\n" VV"v128,v160\n" VV"v147,v179\n" CV"v117,v86,v87\n"
 MF"a[28:31],v[60:63],v[124:127],a[28:31]\n""s_cselect_b32 s41,s58,s41\n" BL"v1,s[20:23],s40 offen offset:1024 sc1 lds\n" VV"v129,v161\n" VV"v148,v180\n" MF"a[32:35],v[64:67],v[120:123],a[32:35]\n"
"s_cselect_b32 s49,-1,s49\n" VV"v149,v181\n" EX"v92,v92\n" MF"a[36:39],v[64:67],v[124:127],a[36:39]\n" DR"v[40:43],v3 offset:25600\n" DR"v[44:47],v3 offset:26624\n" VV"v150,v182\n" WL"(4)\n"
 MF"a[40:43],v[68:71],v[120:123],a[40:43]\n" VV"v151,v183\n" EX"v93,v93\n" MF"a[44:47],v[68:71],v[124:127],a[44:47]\n" DR"v[48:51],v3 offset:27648\n" DR"v[52:55],v3 offset:28672\n" VV"v152,v184\n"
 MF"a[48:51],v[72:75],v[120:123],a[48:51]\n" VV"v153,v185\n" EX"v94,v94\n" MF"a[52:55],v[72:75],v[124:127],a[52:55]\n" DR"v[56:59],v3 offset:29696\n" DR"v[60:63],v3 offset:30720\n" VV"v154,v186\n"
 WL"(6)\n" MF"a[56:59],v[76:79],v[120:123],a[56:59]\n" DR"v[64:67],v3 offset:31744\n" VV"v155,v187\n" CV"v118,v92,v93\n" MF"a[60:63],v[76:79],v[124:127],a[60:63]\n" VV"v156,v188\n" EX"v95,v95\n"
 MF"a[64:67],v[8:11],v[120:123],a[64:67]\n" VV"v157,v189\n" CV"v119,v94,v95\n""v_add_u32 v2,s50,v0\n" MF"a[68:71],v[8:11],v[124:127],a[68:71]\n" SA"s42,s44,s46\n" VV"v130,v162\n" VV"v158,v190\n"
"v_xor_b32 v3,0x38000,v3\n" MF"a[4:7],v[36:39],v[116:119],a[4:7]\n" WV"(6) lgkmcnt(0)\n""s_barrier\n" DR"v[16:19],v2 offset:0\n" DR"v[20:23],v2 offset:1024\n" VV"v159,v191\n"
 MF"a[12:15],v[40:43],v[116:119],a[12:15]\n" DR"v[24:27],v2 offset:2048\n" DR"v[28:31],v2 offset:3072\n" MF"a[20:23],v[44:47],v[116:119],a[20:23]\n" DR"v[32:35],v2 offset:4096\n"
 BL"v1,s[20:23],s40 offen offset:2048 sc1 lds\n" MF"a[28:31],v[48:51],v[116:119],a[28:31]\n""s_mov_b32 s44,s50\n" BL"v1,s[20:23],s40 offen offset:3072 sc1 lds\n" VV"v131,v163\n"
 MF"a[36:39],v[52:55],v[116:119],a[36:39]\n" SA"s40,s40,32768\n""s_cmp_eq_u32 s40,s48\n" VV"v132,v164\n" VV"v133,v165\n" MF"a[44:47],v[56:59],v[116:119],a[44:47]\n""s_cselect_b32 s40,s46,s40\n"
"s_cselect_b32 s48,-1,s48\n" VV"v134,v166\n" VV"v135,v167\n" MF"a[52:55],v[60:63],v[116:119],a[52:55]\n" VV"v136,v168\n" VV"v137,v169\n" MF"a[60:63],v[64:67],v[116:119],a[60:63]\n" VV"v138,v170\n"
 VV"v139,v171\n" MF"a[68:71],v[8:11],v[116:119],a[68:71]\n" VV"v140,v172\n" VV"v141,v173\n""s_branch Ltend_%=\n""Ltail3_%=:\n" WL"(3)\n" MF"v[80:83],v[16:19],v[128:131],0\n""s_mov_b32 m0,s43\n"
 EX"v107,v107\n" MF"v[84:87],v[16:19],v[144:147],0\n" DR"v[36:39],v2 offset:5120\n" DR"v[40:43],v2 offset:6144\n" MF"v[80:83],v[20:23],v[132:135],v[80:83]\n" EX"v108,v108\n"
 MF"v[84:87],v[20:23],v[148:151],v[84:87]\n" DR"v[44:47],v2 offset:7168\n" DR"v[48:51],v3 offset:0\n" WL"(5)\n" MF"v[80:83],v[24:27],v[136:139],v[80:83]\n" EX"v109,v109\n"
 MF"v[84:87],v[24:27],v[152:155],v[84:87]\n" CV"v123,v106,v107\n" CV"v126,v108,v109\n" MF"v[80:83],v[28:31],v[140:143],v[80:83]\n" EX"v110,v110\n" MF"v[84:87],v[28:31],v[156:159],v[84:87]\n"
 DR"v[52:55],v3 offset:1024\n" DR"v[56:59],v3 offset:2048\n" WL"(6)\n" MF"v[88:91],v[32:35],v[128:131],0\n" EX"v111,v111\n" MF"v[92:95],v[32:35],v[144:147],0\n"
 BL"v0,s[24:27],s41 offen offset:0 sc1 lds\n" CV"v127,v110,v111\n" WL"(4)\n" MF"v[88:91],v[36:39],v[132:135],v[88:91]\n" BL"v0,s[24:27],s41 offen offset:1024 sc1 lds\n"
 MF"v[92:95],v[36:39],v[148:151],v[92:95]\n" EX"v80,v80\n" MF"v[88:91],v[40:43],v[136:139],v[88:91]\n" DR"v[60:63],v3 offset:3072\n" DR"v[64:67],v3 offset:4096\n"
 MF"v[92:95],v[40:43],v[152:155],v[92:95]\n" EX"v81,v81\n" WL"(4)\n" MF"v[88:91],v[44:47],v[140:143],v[88:91]\n" BL"v0,s[24:27],s41 offen offset:2048 sc1 lds\n" CV"v112,v80,v81\n"
 MF"v[92:95],v[44:47],v[156:159],v[92:95]\n" EX"v82,v82\n" MF"a[0:3],v[48:51],v[120:123],a[0:3]\n" DR"v[68:71],v3 offset:5120\n" DR"v[72:75],v3 offset:6144\n" MF"a[4:7],v[48:51],v[124:127],a[4:7]\n"
 WL"(4)\n" MF"a[8:11],v[52:55],v[120:123],a[8:11]\n" EX"v83,v83\n" MF"a[12:15],v[52:55],v[124:127],a[12:15]\n" DR"v[76:79],v3 offset:7168\n" DR"v[16:19],v2 offset:8192\n"
 MF"a[16:19],v[56:59],v[120:123],a[16:19]\n" EX"v84,v84\n" MF"a[20:23],v[56:59],v[124:127],a[20:23]\n" BL"v0,s[24:27],s41 offen offset:3072 sc1 lds\n" CV"v113,v82,v83\n" WL"(4)\n"
 MF"a[24:27],v[60:63],v[120:123],a[24:27]\n" SA"m0,s43,4096\n" MF"a[28:31],v[60:63],v[124:127],a[28:31]\n" EX"v85,v85\n" MF"a[32:35],v[64:67],v[120:123],a[32:35]\n"
 BL"v1,s[24:27],s41 offen offset:0 sc1 lds\n" CV"v116,v84,v85\n" MF"a[36:39],v[64:67],v[124:127],a[36:39]\n" EX"v86,v86\n" WL"(2)\n" MF"a[40:43],v[68:71],v[120:123],a[40:43]\n"
 DR"v[20:23],v2 offset:9216\n" DR"v[24:27],v2 offset:10240\n" MF"a[44:47],v[68:71],v[124:127],a[44:47]\n" EX"v87,v87\n" MF"a[48:51],v[72:75],v[120:123],a[48:51]\n" CV"v117,v86,v87\n"
 MF"a[52:55],v[72:75],v[124:127],a[52:55]\n" DR"v[28:31],v2 offset:11264\n" DR"v[32:35],v2 offset:12288\n" WL"(4)\n" MF"a[56:59],v[76:79],v[120:123],a[56:59]\n" EX"v88,v88\n"
 MF"a[60:63],v[76:79],v[124:127],a[60:63]\n" BL"v1,s[24:27],s41 offen offset:1024 sc1 lds\n" MF"a[64:67],v[8:11],v[120:123],a[64:67]\n" EX"v89,v89\n" MF"a[68:71],v[8:11],v[124:127],a[68:71]\n"
 CV"v114,v88,v89\n" MF"v[96:99],v[16:19],v[128:131],0\n" DR"v[36:39],v2 offset:13312\n" DR"v[40:43],v2 offset:14336\n" MF"v[100:103],v[16:19],v[144:147],0\n" EX"v90,v90\n" WL"(4)\n"
 MF"v[96:99],v[20:23],v[132:135],v[96:99]\n" BL"v1,s[24:27],s41 offen offset:2048 sc1 lds\n" MF"v[100:103],v[20:23],v[148:151],v[100:103]\n" EX"v91,v91\n" MF"v[96:99],v[24:27],v[136:139],v[96:99]\n"
 DR"v[44:47],v2 offset:15360\n" DR"v[48:51],v3 offset:8192\n" MF"v[100:103],v[24:27],v[152:155],v[100:103]\n" EX"v92,v92\n" WL"(4)\n" MF"v[96:99],v[28:31],v[140:143],v[96:99]\n"
 BL"v1,s[24:27],s41 offen offset:3072 sc1 lds\n" CV"v115,v90,v91\n" MF"v[100:103],v[28:31],v[156:159],v[100:103]\n" DR"v[52:55],v3 offset:9216\n" DR"v[56:59],v3 offset:10240\n"
 MF"v[104:107],v[32:35],v[128:131],v[192:195]\n""s_mov_b32 m0,s42\n" EX"v93,v93\n" MF"v[108:111],v[32:35],v[144:147],0\n" DR"v[60:63],v3 offset:11264\n" DR"v[64:67],v3 offset:12288\n"
 CV"v118,v92,v93\n" WL"(6)\n" MF"v[104:107],v[36:39],v[132:135],v[104:107]\n" BL"v0,s[20:23],s40 offen offset:0 sc1 lds\n" EX"v94,v94\n" MF"v[108:111],v[36:39],v[148:151],v[108:111]\n" EX"v96,v96\n"
 MF"v[104:107],v[40:43],v[136:139],v[104:107]\n" EX"v97,v97\n" MF"v[108:111],v[40:43],v[152:155],v[108:111]\n" EX"v95,v95\n" WL"(4)\n" MF"v[104:107],v[44:47],v[140:143],v[104:107]\n"
 CV"v119,v94,v95\n" EX"v98,v98\n" MF"v[108:111],v[44:47],v[156:159],v[108:111]\n" EX"v99,v99\n" MF"a[0:3],v[48:51],v[112:115],a[0:3]\n" DR"v[68:71],v3 offset:13312\n" DR"v[72:75],v3 offset:14336\n"
 MF"a[4:7],v[48:51],v[116:119],a[4:7]\n" BL"v0,s[20:23],s40 offen offset:1024 sc1 lds\n" EX"v100,v100\n" WL"(4)\n" MF"a[8:11],v[52:55],v[112:115],a[8:11]\n" CV"v120,v96,v97\n" CV"v121,v98,v99\n"
 MF"a[12:15],v[52:55],v[116:119],a[12:15]\n" EX"v101,v101\n" MF"a[16:19],v[56:59],v[112:115],a[16:19]\n" EX"v102,v102\n" MF"a[20:23],v[56:59],v[116:119],a[20:23]\n" DR"v[76:79],v3 offset:15360\n"
 DR"v[16:19],v2 offset:16384\n" WL"(4)\n" MF"a[24:27],v[60:63],v[112:115],a[24:27]\n" EX"v103,v103\n" MF"a[28:31],v[60:63],v[116:119],a[28:31]\n" EX"v104,v104\n"
 MF"a[32:35],v[64:67],v[112:115],a[32:35]\n" DR"v[20:23],v2 offset:17408\n" DR"v[24:27],v2 offset:18432\n" BL"v0,s[20:23],s40 offen offset:2048 sc1 lds\n" MF"a[36:39],v[64:67],v[116:119],a[36:39]\n"
 EX"v105,v105\n" WL"(4)\n" MF"a[40:43],v[68:71],v[112:115],a[40:43]\n" DR"v[28:31],v2 offset:19456\n" DR"v[32:35],v3 offset:16384\n" CV"v122,v104,v105\n" MF"a[44:47],v[68:71],v[116:119],a[44:47]\n"
 EX"v106,v106\n" MF"a[48:51],v[72:75],v[112:115],a[48:51]\n" EX"v107,v107\n" MF"a[52:55],v[72:75],v[116:119],a[52:55]\n" DR"v[36:39],v3 offset:17408\n" DR"v[40:43],v3 offset:18432\n"
 CV"v123,v106,v107\n" WL"(6)\n" MF"a[56:59],v[76:79],v[112:115],a[56:59]\n" EX"v108,v108\n" MF"a[60:63],v[76:79],v[116:119],a[60:63]\n" BL"v0,s[20:23],s40 offen offset:3072 sc1 lds\n" EX"v109,v109\n"
 MF"a[64:67],v[8:11],v[112:115],a[64:67]\n" DR"v[44:47],v3 offset:19456\n" DR"v[48:51],v3 offset:20480\n" CV"v126,v108,v109\n" MF"a[68:71],v[8:11],v[116:119],a[68:71]\n" SA"m0,s42,4096\n"
 EX"v110,v110\n" MF"v[84:87],v[16:19],v[144:147],v[192:195]\n" VV"v118,0\n" VV"v119,0\n" WL"(6)\n" MF"v[84:87],v[20:23],v[148:151],v[84:87]\n" SA"s50,s44,32768\n" EX"v111,v111\n"
 MF"v[84:87],v[24:27],v[152:155],v[84:87]\n" CV"v124,v100,v101\n" CV"v125,v102,v103\n" CV"v127,v110,v111\n" WL"(4)\n" MF"v[84:87],v[28:31],v[156:159],v[84:87]\n""s_cmp_eq_u32 s50,98304\n"
"s_cselect_b32 s50,0,s50\n" MF"a[0:3],v[32:35],v[120:123],a[0:3]\n" DR"v[52:55],v3 offset:21504\n" DR"v[56:59],v3 offset:22528\n" MF"a[4:7],v[32:35],v[124:127],a[4:7]\n""s_xor_b32 s43,s43,0x38000\n"
 BL"v1,s[20:23],s40 offen offset:0 sc1 lds\n" VV"v128,v160\n" VV"v150,v182\n" WL"(4)\n" MF"a[8:11],v[36:39],v[120:123],a[8:11]\n" SA"s41,s41,32768\n""s_cmp_eq_u32 s41,s49\n" VV"v129,v161\n"
 VV"v130,v162\n" VV"v151,v183\n" MF"a[12:15],v[36:39],v[124:127],a[12:15]\n""s_cselect_b32 s41,s58,s41\n" VV"v152,v184\n" EX"v84,v84\n" MF"a[16:19],v[40:43],v[120:123],a[16:19]\n"
 DR"v[60:63],v3 offset:23552\n" DR"v[64:67],v3 offset:24576\n" VV"v153,v185\n" MF"a[20:23],v[40:43],v[124:127],a[20:23]\n""s_cselect_b32 s49,-1,s49\n" VV"v131,v163\n" VV"v132,v164\n" VV"v154,v186\n"
 WL"(4)\n" MF"a[24:27],v[44:47],v[120:123],a[24:27]\n" VV"v133,v165\n" VV"v134,v166\n" VV"v155,v187\n" MF"a[28:31],v[44:47],v[124:127],a[28:31]\n" VV"v156,v188\n" EX"v85,v85\n"
 MF"a[32:35],v[48:51],v[120:123],a[32:35]\n" BL"v1,s[20:23],s40 offen offset:1024 sc1 lds\n" VV"v157,v189\n" CV"v116,v84,v85\n" MF"a[36:39],v[48:51],v[124:127],a[36:39]\n"
 DR"v[68:71],v3 offset:25600\n" DR"v[72:75],v3 offset:26624\n" VV"v158,v190\n" WL"(4)\n" MF"a[40:43],v[52:55],v[120:123],a[40:43]\n" DR"v[76:79],v3 offset:27648\n" DR"v[36:39],v3 offset:28672\n"
 VV"v159,v191\n" MF"a[44:47],v[52:55],v[124:127],a[44:47]\n" EX"v86,v86\n" MF"a[48:51],v[56:59],v[120:123],a[48:51]\n" VV"v135,v167\n""v_add_u32 v2,s50,v0\n"
 MF"a[52:55],v[56:59],v[124:127],a[52:55]\n" DR"v[40:43],v3 offset:29696\n" DR"v[44:47],v3 offset:30720\n" WL"(6)\n" MF"a[56:59],v[60:63],v[120:123],a[56:59]\n" DR"v[48:51],v3 offset:31744\n"
"v_xor_b32 v3,0x38000,v3\n" MF"a[60:63],v[60:63],v[124:127],a[60:63]\n" SA"s42,s44,s46\n" EX"v87,v87\n" MF"a[64:67],v[8:11],v[120:123],a[64:67]\n""s_mov_b32 s44,s50\n" VV"v136,v168\n"
 CV"v117,v86,v87\n" MF"a[68:71],v[8:11],v[124:127],a[68:71]\n" VV"v137,v169\n" VV"v138,v170\n" MF"a[4:7],v[64:67],v[116:119],a[4:7]\n" WV"(6) lgkmcnt(0)\n""s_barrier\n" DR"v[16:19],v2 offset:0\n"
 DR"v[20:23],v2 offset:1024\n" MF"a[12:15],v[68:71],v[116:119],a[12:15]\n" DR"v[24:27],v2 offset:2048\n" DR"v[28:31],v2 offset:3072\n" MF"a[20:23],v[72:75],v[116:119],a[20:23]\n"
 DR"v[32:35],v2 offset:4096\n" BL"v1,s[20:23],s40 offen offset:2048 sc1 lds\n" MF"a[28:31],v[76:79],v[116:119],a[28:31]\n" BL"v1,s[20:23],s40 offen offset:3072 sc1 lds\n" VV"v139,v171\n"
 MF"a[36:39],v[36:39],v[116:119],a[36:39]\n" SA"s40,s40,32768\n""s_cmp_eq_u32 s40,s48\n" VV"v140,v172\n" VV"v141,v173\n" MF"a[44:47],v[40:43],v[116:119],a[44:47]\n""s_cselect_b32 s40,s46,s40\n"
"s_cselect_b32 s48,-1,s48\n" VV"v142,v174\n" VV"v143,v175\n" MF"a[52:55],v[44:47],v[116:119],a[52:55]\n" VV"v144,v176\n" VV"v145,v177\n" MF"a[60:63],v[48:51],v[116:119],a[60:63]\n" VV"v146,v178\n"
 VV"v147,v179\n" MF"a[68:71],v[8:11],v[116:119],a[68:71]\n" VV"v148,v180\n" VV"v149,v181\n""Ltend_%=:\n""s_cmp_eq_u32 s45,1\n""s_cbranch_scc1 Lfin_%=\n""s_lshr_b32 s62,s96,5\n""s_and_b32 s63,s96,7\n"
"s_lshl_b32 s63,s63,2\n""s_bfe_u32 s57,s96,0x20003\n" SA"s63,s63,s57\n""s_lshl_b32 s61,s63,2\n" SA"s61,s61,s36\n""s_lshl_b32 s57,s63,3\n" SA"s57,s57,s62\n""s_lshl_b32 s57,s57,3\n" SA"s57,s57,16384\n"
 VV"v244,s98\n" VV"v245,s99\n" VV"v246,0\n""buffer_store_dwordx2 v[244:245],v246,s[92:95],s57 offen sc1\n""s_getpc_b64 s[72:73]\n"".Lmk_pc1:\n""s_lshr_b32 s57,s96,3\n""s_mul_i32 s57,s57,2048\n"
"v_mbcnt_lo_u32_b32 v244,-1,0\n""v_mbcnt_hi_u32_b32 v244,-1,v244\n""v_min_u32 v244,15,v244\n""v_lshlrev_b32 v244,7,v244\n""v_add_u32 v244,s57,v244\n""s_mov_b32 s57,.Lmk_final-.Lmk_pc1-8\n"
"v_min_u32 v244,s57,v244\n""v_and_b32 v244,-4,v244\n""global_load_dword v245,v244,s[72:73]\n""s_cmp_lt_u32 s36,2\n""s_cselect_b32 s57,10,18\n""s_cmp_eq_u32 s36,3\n""s_cselect_b32 s57,16,s57\n"
"v_readlane_b32 s72,v254,s57\n" SA"s57,s57,1\n""v_readlane_b32 s73,v254,s57\n""s_mov_b32 s75,0x20000\n""s_mov_b32 s74,1409024\n""s_cmp_lt_u32 s36,2\n""s_cselect_b32 s74,0x400000,s74\n"
"s_lshl_b32 s57,s96,14\n""s_lshl_b32 s59,s36,13\n" SA"s57,s57,s59\n""s_mul_i32 s59,s96,5504\n""s_cmp_lt_u32 s36,2\n""s_cselect_b32 s57,s57,s59\n""v_mbcnt_lo_u32_b32 v244,-1,0\n"
"v_mbcnt_hi_u32_b32 v244,-1,v244\n""v_mul_u32_u24 v244,128,v244\n""buffer_load_dword v245,v244,s[72:75],s57 offen\n""s_lshl_b32 s50,s47,3\n" SA"s59,s50,s36\n" SA"s60,s50,7\n""s_sub_u32 s60,s60,s36\n"
"s_lshl_b32 s59,s59,17\n""s_lshl_b32 s60,s60,17\n""s_lshl_b32 s50,s37,12\n" SA"s59,s59,s50\n" SA"s60,s60,s50\n""s_mov_b32 s61,0\n""s_mov_b32 s45,1\n""s_sub_u32 s39,14,s47\n" WL"(3)\n"
 MF"v[80:83],v[16:19],v[128:131],0\n" DR"v[36:39],v2 offset:5120\n" DR"v[40:43],v2 offset:6144\n" AR"v200,a64\n" MF"v[84:87],v[16:19],v[144:147],0\n""s_mov_b32 m0,s43\n" AR"v201,a68\n"
"v_rcp_f32 v204,v200\n""v_rcp_f32 v206,v201\n" MF"v[80:83],v[20:23],v[132:135],v[80:83]\n""v_min_f32 v202,v200,v201\n""v_max_f32 v203,v200,v201\n""v_cmp_le_f32 vcc,0x26800000,v202\n"
"s_mov_b32 s55,0x58800000\n" MF"v[84:87],v[20:23],v[148:151],v[84:87]\n" BL"v0,s[24:27],s41 offen offset:0 sc1 lds\n""v_cmp_ge_f32 s[50:51],s55,v203\n""v_cmp_o_f32 s[62:63],v200,v201\n""s_nop 0\n"
"s_and_b64 vcc,vcc,s[50:51]\n""s_and_b64 vcc,vcc,s[62:63]\n""s_cmp_eq_u64 vcc,-1\n""s_cselect_b32 s50,0,1\n""s_lshl_b32 s50,s50,s61\n""s_or_b32 s54,s54,s50\n" WL"(3)\n"
 MF"v[80:83],v[24:27],v[136:139],v[80:83]\n" DR"v[44:47],v2 offset:7168\n" DR"v[48:51],v2 offset:8192\n" AR"v208,a0\n" MF"v[84:87],v[24:27],v[152:155],v[84:87]\n" AR"v209,a1\n" AR"v210,a2\n"
 AR"v211,a3\n" MF"v[80:83],v[28:31],v[140:143],v[80:83]\n" AR"v212,a8\n" AR"v213,a9\n" AR"v214,a10\n" MF"v[84:87],v[28:31],v[156:159],v[84:87]\n" AR"v215,a11\n" VM"v208,v208,v204\n"
 VM"v209,v209,v204\n" WL"(3)\n" MF"v[88:91],v[32:35],v[128:131],0\n" VM"v210,v210,v204\n" VM"v211,v211,v204\n" VM"v212,v212,v204\n" MF"v[92:95],v[32:35],v[144:147],0\n" VM"v213,v213,v204\n"
 VM"v214,v214,v204\n" VM"v215,v215,v204\n" MF"v[88:91],v[36:39],v[132:135],v[88:91]\n" BL"v0,s[24:27],s41 offen offset:1024 sc1 lds\n" CV"v216,v208,v209\n" CV"v217,v210,v211\n"
 MF"v[92:95],v[36:39],v[148:151],v[92:95]\n" CV"v218,v212,v213\n" CV"v219,v214,v215\n" BS"v[216:219],v0,s[64:67],s59 offen offset:0 sc1\n" WL"(1)\n" MF"v[88:91],v[40:43],v[136:139],v[88:91]\n"
 EX"v80,v80\n" AR"v220,a16\n" MF"v[92:95],v[40:43],v[152:155],v[92:95]\n" AR"v221,a17\n" AR"v222,a18\n" AR"v223,a19\n" MF"v[88:91],v[44:47],v[140:143],v[88:91]\n" EX"v81,v81\n" AR"v224,a24\n"
 MF"v[92:95],v[44:47],v[156:159],v[92:95]\n" CV"v112,v80,v81\n" AR"v225,a25\n" AR"v226,a26\n" EX"v82,v82\n" AR"v227,a27\n" VM"v220,v220,v204\n" VM"v221,v221,v204\n" VM"v222,v222,v204\n"
 VM"v223,v223,v204\n" VM"v224,v224,v204\n" VM"v225,v225,v204\n" VM"v226,v226,v204\n" BL"v0,s[24:27],s41 offen offset:2048 sc1 lds\n" EX"v83,v83\n" VM"v227,v227,v204\n" CV"v228,v220,v221\n"
 CV"v113,v82,v83\n" CV"v229,v222,v223\n" CV"v230,v224,v225\n" CV"v231,v226,v227\n" BS"v[228:231],v0,s[64:67],s59 offen offset:1024 sc1\n" EX"v84,v84\n" AR"v232,a32\n" AR"v233,a33\n" AR"v234,a34\n"
 AR"v235,a35\n" AR"v236,a40\n" AR"v237,a41\n" AR"v238,a42\n" AR"v239,a43\n" EX"v85,v85\n" VM"v232,v232,v204\n" VM"v233,v233,v204\n" VM"v234,v234,v204\n" CV"v116,v84,v85\n" VM"v235,v235,v204\n"
 VM"v236,v236,v204\n" VM"v237,v237,v204\n" VM"v238,v238,v204\n" EX"v86,v86\n" VM"v239,v239,v204\n" CV"v240,v232,v233\n" CV"v241,v234,v235\n" BL"v0,s[24:27],s41 offen offset:3072 sc1 lds\n"
 CV"v242,v236,v237\n" CV"v243,v238,v239\n" BS"v[240:243],v0,s[64:67],s59 offen offset:2048 sc1\n" AR"v208,a48\n" DR"v[52:55],v2 offset:9216\n" DR"v[56:59],v2 offset:10240\n" SA"m0,s43,4096\n"
 EX"v87,v87\n" AR"v209,a49\n" CV"v117,v86,v87\n" AR"v210,a50\n" AR"v211,a51\n" AR"v212,a56\n" AR"v213,a57\n" EX"v88,v88\n" AR"v214,a58\n" AR"v215,a59\n" VM"v208,v208,v204\n" VM"v209,v209,v204\n"
 VM"v210,v210,v204\n" VM"v211,v211,v204\n" VM"v212,v212,v204\n" VM"v213,v213,v204\n" DR"v[60:63],v2 offset:11264\n" DR"v[64:67],v2 offset:12288\n" EX"v89,v89\n" VM"v214,v214,v204\n" CV"v114,v88,v89\n"
 VM"v215,v215,v204\n" CV"v216,v208,v209\n" CV"v217,v210,v211\n" CV"v218,v212,v213\n" EX"v90,v90\n" CV"v219,v214,v215\n" BS"v[216:219],v0,s[64:67],s59 offen offset:3072 sc1\n" AR"v220,a4\n"
 DR"v[68:71],v2 offset:13312\n" DR"v[72:75],v2 offset:14336\n" BL"v1,s[24:27],s41 offen offset:0 sc1 lds\n" AR"v221,a5\n" AR"v222,a6\n" WL"(5)\n" MF"v[96:99],v[48:51],v[128:131],0\n" EX"v91,v91\n"
 AR"v223,a7\n" MF"v[100:103],v[48:51],v[144:147],0\n" CV"v115,v90,v91\n" AR"v224,a12\n" AR"v225,a13\n" MF"v[96:99],v[52:55],v[132:135],v[96:99]\n" EX"v92,v92\n" AR"v226,a14\n"
 MF"v[100:103],v[52:55],v[148:151],v[100:103]\n" DR"v[76:79],v2 offset:15360\n" DR"v[16:19],v3 offset:8192\n" AR"v227,a15\n" WL"(5)\n" MF"v[96:99],v[56:59],v[136:139],v[96:99]\n" EX"v93,v93\n"
 VM"v220,v220,v206\n" MF"v[100:103],v[56:59],v[152:155],v[100:103]\n" CV"v118,v92,v93\n" VM"v221,v221,v206\n" VM"v222,v222,v206\n" MF"v[96:99],v[60:63],v[140:143],v[96:99]\n" EX"v94,v94\n"
 VM"v223,v223,v206\n" MF"v[100:103],v[60:63],v[156:159],v[100:103]\n" DR"v[20:23],v3 offset:9216\n" DR"v[24:27],v3 offset:10240\n" VM"v224,v224,v206\n" WL"(5)\n" MF"v[104:107],v[64:67],v[128:131],0\n"
 EX"v95,v95\n" VM"v225,v225,v206\n" MF"v[108:111],v[64:67],v[144:147],0\n" BL"v1,s[24:27],s41 offen offset:1024 sc1 lds\n" CV"v119,v94,v95\n" VM"v226,v226,v206\n"
 MF"v[104:107],v[68:71],v[132:135],v[104:107]\n" VM"v227,v227,v206\n" CV"v228,v220,v221\n" CV"v229,v222,v223\n" MF"v[108:111],v[68:71],v[148:151],v[108:111]\n" DR"v[28:31],v3 offset:11264\n"
 DR"v[32:35],v3 offset:12288\n" CV"v230,v224,v225\n" WL"(5)\n" MF"v[104:107],v[72:75],v[136:139],v[104:107]\n" EX"v96,v96\n" CV"v231,v226,v227\n" MF"v[108:111],v[72:75],v[152:155],v[108:111]\n"
 BS"v[228:231],v0,s[64:67],s60 offen offset:0 sc1\n" AR"v232,a20\n" AR"v233,a21\n" MF"v[104:107],v[76:79],v[140:143],v[104:107]\n" EX"v97,v97\n" AR"v234,a22\n"
 MF"v[108:111],v[76:79],v[156:159],v[108:111]\n" DR"v[36:39],v3 offset:13312\n" DR"v[40:43],v3 offset:14336\n" AR"v235,a23\n" AR"v236,a28\n" AR"v237,a29\n" AR"v238,a30\n" AR"v239,a31\n"
 VM"v232,v232,v206\n" VM"v233,v233,v206\n" VM"v234,v234,v206\n" VM"v235,v235,v206\n" VM"v236,v236,v206\n" VM"v237,v237,v206\n" VM"v238,v238,v206\n" VM"v239,v239,v206\n" CV"v240,v232,v233\n"
 CV"v241,v234,v235\n" CV"v242,v236,v237\n" CV"v243,v238,v239\n" BS"v[240:243],v0,s[64:67],s60 offen offset:1024 sc1\n" AR"v208,a36\n" AR"v209,a37\n" AR"v210,a38\n" AR"v211,a39\n" AR"v212,a44\n"
 AR"v213,a45\n" AR"v214,a46\n" AR"v215,a47\n" VM"v208,v208,v206\n" VM"v209,v209,v206\n" VM"v210,v210,v206\n" VM"v211,v211,v206\n" VM"v212,v212,v206\n" VM"v213,v213,v206\n" VM"v214,v214,v206\n"
 VM"v215,v215,v206\n" CV"v216,v208,v209\n" CV"v217,v210,v211\n" CV"v218,v212,v213\n" CV"v219,v214,v215\n" BS"v[216:219],v0,s[64:67],s60 offen offset:2048 sc1\n" AR"v220,a52\n" AR"v221,a53\n"
 AR"v222,a54\n" AR"v223,a55\n" AR"v224,a60\n" AR"v225,a61\n" AR"v226,a62\n" AR"v227,a63\n" VM"v220,v220,v206\n" VM"v221,v221,v206\n" VM"v222,v222,v206\n" VM"v223,v223,v206\n" VM"v224,v224,v206\n"
 VM"v225,v225,v206\n" VM"v226,v226,v206\n" VM"v227,v227,v206\n" CV"v228,v220,v221\n" CV"v229,v222,v223\n" CV"v230,v224,v225\n" CV"v231,v226,v227\n"
 BS"v[228:231],v0,s[64:67],s60 offen offset:3072 sc1\n" WL"(5)\n" MF"a[0:3],v[16:19],v[112:115],0\n" EX"v98,v98\n" MF"a[4:7],v[16:19],v[116:119],0\n" BL"v1,s[24:27],s41 offen offset:2048 sc1 lds\n"
 CV"v120,v96,v97\n" MF"a[8:11],v[20:23],v[112:115],0\n" EX"v99,v99\n" MF"a[12:15],v[20:23],v[116:119],0\n" DR"v[44:47],v3 offset:15360\n" DR"v[48:51],v2 offset:16384\n" WL"(5)\n"
 MF"a[16:19],v[24:27],v[112:115],0\n" EX"v100,v100\n" MF"a[20:23],v[24:27],v[116:119],0\n" BL"v1,s[24:27],s41 offen offset:3072 sc1 lds\n" CV"v121,v98,v99\n" MF"a[24:27],v[28:31],v[112:115],0\n"
"s_mov_b32 m0,s42\n" EX"v101,v101\n" MF"a[28:31],v[28:31],v[116:119],0\n" CV"v124,v100,v101\n" WL"(3)\n" MF"a[32:35],v[32:35],v[112:115],0\n" EX"v102,v102\n" MF"a[36:39],v[32:35],v[116:119],0\n"
 DR"v[52:55],v2 offset:17408\n" DR"v[56:59],v2 offset:18432\n" MF"a[40:43],v[36:39],v[112:115],0\n" EX"v103,v103\n" MF"a[44:47],v[36:39],v[116:119],0\n" BL"v0,s[20:23],s40 offen offset:0 sc1 lds\n"
 CV"v125,v102,v103\n" WL"(3)\n" MF"a[48:51],v[40:43],v[112:115],0\n" EX"v104,v104\n" MF"a[52:55],v[40:43],v[116:119],0\n" DR"v[60:63],v2 offset:19456\n" DR"v[64:67],v2 offset:20480\n"
 MF"a[56:59],v[44:47],v[112:115],0\n" EX"v105,v105\n" MF"a[60:63],v[44:47],v[116:119],0\n" CV"v122,v104,v105\n" MF"a[64:67],v[8:11],v[112:115],0\n" EX"v106,v106\n" MF"a[68:71],v[8:11],v[116:119],0\n"
 DR"v[68:71],v2 offset:21504\n" DR"v[72:75],v2 offset:22528\n" WL"(5)\n" MF"v[80:83],v[48:51],v[128:131],0\n" EX"v107,v107\n" MF"v[84:87],v[48:51],v[144:147],0\n"
 BL"v0,s[20:23],s40 offen offset:1024 sc1 lds\n" CV"v123,v106,v107\n" MF"v[80:83],v[52:55],v[132:135],v[80:83]\n" EX"v108,v108\n" MF"v[84:87],v[52:55],v[148:151],v[84:87]\n"
 DR"v[76:79],v2 offset:23552\n" DR"v[16:19],v3 offset:16384\n" WL"(5)\n" MF"v[80:83],v[56:59],v[136:139],v[80:83]\n" EX"v109,v109\n" MF"v[84:87],v[56:59],v[152:155],v[84:87]\n" CV"v126,v108,v109\n"
 MF"v[80:83],v[60:63],v[140:143],v[80:83]\n" EX"v110,v110\n" MF"v[84:87],v[60:63],v[156:159],v[84:87]\n" DR"v[20:23],v3 offset:17408\n" DR"v[24:27],v3 offset:18432\n" WL"(5)\n"
 MF"v[88:91],v[64:67],v[128:131],0\n" EX"v111,v111\n" MF"v[92:95],v[64:67],v[144:147],0\n" CV"v127,v110,v111\n" MF"v[88:91],v[68:71],v[132:135],v[88:91]\n" MF"v[92:95],v[68:71],v[148:151],v[92:95]\n"
 DR"v[28:31],v3 offset:19456\n" DR"v[32:35],v3 offset:20480\n" WL"(5)\n" MF"v[88:91],v[72:75],v[136:139],v[88:91]\n" EX"v80,v80\n" MF"v[92:95],v[72:75],v[152:155],v[92:95]\n"
 BL"v0,s[20:23],s40 offen offset:2048 sc1 lds\n" MF"v[88:91],v[76:79],v[140:143],v[88:91]\n" EX"v81,v81\n" MF"v[92:95],v[76:79],v[156:159],v[92:95]\n" DR"v[36:39],v3 offset:21504\n"
 DR"v[40:43],v3 offset:22528\n" WL"(5)\n" MF"a[0:3],v[16:19],v[120:123],a[0:3]\n" EX"v82,v82\n" MF"a[4:7],v[16:19],v[124:127],a[4:7]\n" CV"v112,v80,v81\n" MF"a[8:11],v[20:23],v[120:123],a[8:11]\n"
 EX"v83,v83\n" MF"a[12:15],v[20:23],v[124:127],a[12:15]\n" DR"v[44:47],v3 offset:23552\n" DR"v[48:51],v2 offset:24576\n" WL"(5)\n" MF"a[16:19],v[24:27],v[120:123],a[16:19]\n" EX"v84,v84\n"
 MF"a[20:23],v[24:27],v[124:127],a[20:23]\n" BL"v0,s[20:23],s40 offen offset:3072 sc1 lds\n" CV"v113,v82,v83\n" MF"a[24:27],v[28:31],v[120:123],a[24:27]\n" SA"m0,s42,4096\n" EX"v85,v85\n"
 MF"a[28:31],v[28:31],v[124:127],a[28:31]\n" SA"s50,s44,32768\n" CV"v116,v84,v85\n" WL"(3)\n" MF"a[32:35],v[32:35],v[120:123],a[32:35]\n""s_cmp_eq_u32 s50,98304\n" EX"v86,v86\n"
 MF"a[36:39],v[32:35],v[124:127],a[36:39]\n" DR"v[52:55],v2 offset:25600\n" DR"v[56:59],v2 offset:26624\n" MF"a[40:43],v[36:39],v[120:123],a[40:43]\n""s_cselect_b32 s50,0,s50\n" EX"v87,v87\n"
 MF"a[44:47],v[36:39],v[124:127],a[44:47]\n""s_xor_b32 s43,s43,0x38000\n" CV"v117,v86,v87\n" WL"(3)\n" MF"a[48:51],v[40:43],v[120:123],a[48:51]\n" SA"s41,s41,32768\n" EX"v88,v88\n"
 MF"a[52:55],v[40:43],v[124:127],a[52:55]\n" DR"v[60:63],v2 offset:27648\n" DR"v[64:67],v2 offset:28672\n" MF"a[56:59],v[44:47],v[120:123],a[56:59]\n""s_cmp_eq_u32 s41,s49\n" EX"v89,v89\n"
 MF"a[60:63],v[44:47],v[124:127],a[60:63]\n" BL"v1,s[20:23],s40 offen offset:0 sc1 lds\n" CV"v114,v88,v89\n" MF"a[64:67],v[8:11],v[120:123],a[64:67]\n""s_cselect_b32 s41,s58,s41\n" EX"v90,v90\n"
 MF"a[68:71],v[8:11],v[124:127],a[68:71]\n" DR"v[68:71],v2 offset:29696\n" DR"v[72:75],v2 offset:30720\n" WL"(5)\n" MF"v[96:99],v[48:51],v[128:131],0\n""s_cselect_b32 s49,-1,s49\n" EX"v91,v91\n"
 MF"v[100:103],v[48:51],v[144:147],0\n" CV"v115,v90,v91\n" MF"v[96:99],v[52:55],v[132:135],v[96:99]\n" EX"v92,v92\n" MF"v[100:103],v[52:55],v[148:151],v[100:103]\n" DR"v[76:79],v2 offset:31744\n"
 DR"v[16:19],v3 offset:24576\n" WL"(5)\n" MF"v[96:99],v[56:59],v[136:139],v[96:99]\n" EX"v93,v93\n" MF"v[100:103],v[56:59],v[152:155],v[100:103]\n" CV"v118,v92,v93\n"
 MF"v[96:99],v[60:63],v[140:143],v[96:99]\n" EX"v94,v94\n" MF"v[100:103],v[60:63],v[156:159],v[100:103]\n" DR"v[20:23],v3 offset:25600\n" DR"v[24:27],v3 offset:26624\n" WL"(5)\n"
 MF"v[104:107],v[64:67],v[128:131],0\n" EX"v95,v95\n" MF"v[108:111],v[64:67],v[144:147],0\n" BL"v1,s[20:23],s40 offen offset:1024 sc1 lds\n" CV"v119,v94,v95\n"
 MF"v[104:107],v[68:71],v[132:135],v[104:107]\n" MF"v[108:111],v[68:71],v[148:151],v[108:111]\n" DR"v[36:39],v3 offset:27648\n" DR"v[40:43],v3 offset:28672\n" WL"(5)\n"
 MF"v[104:107],v[72:75],v[136:139],v[104:107]\n" EX"v96,v96\n" MF"v[108:111],v[72:75],v[152:155],v[108:111]\n" MF"v[104:107],v[76:79],v[140:143],v[104:107]\n" EX"v97,v97\n"
 MF"v[108:111],v[76:79],v[156:159],v[108:111]\n" DR"v[44:47],v3 offset:29696\n" DR"v[48:51],v3 offset:30720\n" WL"(5)\n" MF"a[0:3],v[16:19],v[112:115],a[0:3]\n" EX"v98,v98\n"
 MF"a[4:7],v[16:19],v[116:119],a[4:7]\n" DR"v[52:55],v3 offset:31744\n" CV"v120,v96,v97\n" MF"a[8:11],v[20:23],v[112:115],a[8:11]\n" EX"v99,v99\n" MF"a[12:15],v[20:23],v[116:119],a[12:15]\n"
 CV"v121,v98,v99\n""v_add_u32 v2,s50,v0\n" WL"(4)\n" MF"a[16:19],v[24:27],v[112:115],a[16:19]\n" SA"s42,s44,s46\n" EX"v100,v100\n" MF"a[20:23],v[24:27],v[116:119],a[20:23]\n""s_mov_b32 s44,s50\n"
"v_xor_b32 v3,0x38000,v3\n" MF"a[24:27],v[36:39],v[112:115],a[24:27]\n" WV"(6) lgkmcnt(0)\n""s_barrier\n" EX"v101,v101\n" MF"a[28:31],v[36:39],v[116:119],a[28:31]\n" DR"v[16:19],v2 offset:0\n"
 DR"v[20:23],v2 offset:1024\n" CV"v124,v100,v101\n" MF"a[32:35],v[40:43],v[112:115],a[32:35]\n" BL"v1,s[20:23],s40 offen offset:2048 sc1 lds\n" EX"v102,v102\n"
 MF"a[36:39],v[40:43],v[116:119],a[36:39]\n" DR"v[24:27],v2 offset:2048\n" DR"v[28:31],v2 offset:3072\n" BL"v1,s[20:23],s40 offen offset:3072 sc1 lds\n" MF"a[40:43],v[44:47],v[112:115],a[40:43]\n"
 SA"s40,s40,32768\n" EX"v103,v103\n" MF"a[44:47],v[44:47],v[116:119],a[44:47]\n" DR"v[32:35],v2 offset:4096\n" CV"v125,v102,v103\n" MF"a[48:51],v[48:51],v[112:115],a[48:51]\n""s_cmp_eq_u32 s40,s48\n"
 EX"v104,v104\n" MF"a[52:55],v[48:51],v[116:119],a[52:55]\n""s_cselect_b32 s40,s46,s40\n""s_cselect_b32 s48,-1,s48\n" MF"a[56:59],v[52:55],v[112:115],a[56:59]\n" EX"v105,v105\n"
 MF"a[60:63],v[52:55],v[116:119],a[60:63]\n" CV"v122,v104,v105\n" MF"a[64:67],v[8:11],v[112:115],a[64:67]\n" EX"v106,v106\n" MF"a[68:71],v[8:11],v[116:119],a[68:71]\n""s_branch Lloop_%=\n"
"Ldummy_%=:\n""v_accvgpr_write_b32 a0,0\n""v_accvgpr_write_b32 a1,0\n""v_accvgpr_write_b32 a2,0\n""v_accvgpr_write_b32 a3,0\n""v_accvgpr_write_b32 a4,0\n""v_accvgpr_write_b32 a5,0\n"
"v_accvgpr_write_b32 a6,0\n""v_accvgpr_write_b32 a7,0\n""v_accvgpr_write_b32 a8,0\n""v_accvgpr_write_b32 a9,0\n""v_accvgpr_write_b32 a10,0\n""v_accvgpr_write_b32 a11,0\n""v_accvgpr_write_b32 a12,0\n"
"v_accvgpr_write_b32 a13,0\n""v_accvgpr_write_b32 a14,0\n""v_accvgpr_write_b32 a15,0\n""v_accvgpr_write_b32 a16,0\n""v_accvgpr_write_b32 a17,0\n""v_accvgpr_write_b32 a18,0\n"
"v_accvgpr_write_b32 a19,0\n""v_accvgpr_write_b32 a20,0\n""v_accvgpr_write_b32 a21,0\n""v_accvgpr_write_b32 a22,0\n""v_accvgpr_write_b32 a23,0\n""v_accvgpr_write_b32 a24,0\n"
"v_accvgpr_write_b32 a25,0\n""v_accvgpr_write_b32 a26,0\n""v_accvgpr_write_b32 a27,0\n""v_accvgpr_write_b32 a28,0\n""v_accvgpr_write_b32 a29,0\n""v_accvgpr_write_b32 a30,0\n"
"v_accvgpr_write_b32 a31,0\n""v_accvgpr_write_b32 a32,0\n""v_accvgpr_write_b32 a33,0\n""v_accvgpr_write_b32 a34,0\n""v_accvgpr_write_b32 a35,0\n""v_accvgpr_write_b32 a36,0\n"
"v_accvgpr_write_b32 a37,0\n""v_accvgpr_write_b32 a38,0\n""v_accvgpr_write_b32 a39,0\n""v_accvgpr_write_b32 a40,0\n""v_accvgpr_write_b32 a41,0\n""v_accvgpr_write_b32 a42,0\n"
"v_accvgpr_write_b32 a43,0\n""v_accvgpr_write_b32 a44,0\n""v_accvgpr_write_b32 a45,0\n""v_accvgpr_write_b32 a46,0\n""v_accvgpr_write_b32 a47,0\n""v_accvgpr_write_b32 a48,0\n"
"v_accvgpr_write_b32 a49,0\n""v_accvgpr_write_b32 a50,0\n""v_accvgpr_write_b32 a51,0\n""v_accvgpr_write_b32 a52,0\n""v_accvgpr_write_b32 a53,0\n""v_accvgpr_write_b32 a54,0\n"
"v_accvgpr_write_b32 a55,0\n""v_accvgpr_write_b32 a56,0\n""v_accvgpr_write_b32 a57,0\n""v_accvgpr_write_b32 a58,0\n""v_accvgpr_write_b32 a59,0\n""v_accvgpr_write_b32 a60,0\n"
"v_accvgpr_write_b32 a61,0\n""v_accvgpr_write_b32 a62,0\n""v_accvgpr_write_b32 a63,0\n""v_accvgpr_write_b32 a64,0\n""v_accvgpr_write_b32 a65,0\n""v_accvgpr_write_b32 a66,0\n"
"v_accvgpr_write_b32 a67,0\n""v_accvgpr_write_b32 a68,0\n""v_accvgpr_write_b32 a69,0\n""v_accvgpr_write_b32 a70,0\n""v_accvgpr_write_b32 a71,0\n" VV"v96,0\n" VV"v97,0\n" VV"v98,0\n" VV"v99,0\n"
 VV"v100,0\n" VV"v101,0\n" VV"v102,0\n" VV"v103,0\n" VV"v104,0\n" VV"v105,0\n" VV"v106,0\n" VV"v107,0xff800000\n" VV"v108,0xff800000\n" VV"v109,0xff800000\n" VV"v110,0xff800000\n"
 VV"v111,0xff800000\n" VV"v120,0\n" VV"v121,0\n" VV"v122,0\n" VV"v123,0\n" VV"v124,0\n" VV"v125,0\n" VV"v126,0\n" VV"v127,0\n""s_nop 1\n""s_branch Ltail_%=\n""Lfin_%=:\n" WL"(0)\n"
"s_sub_u32 s55,15,s47\n""s_lshl_b32 s50,s55,3\n" SA"s59,s50,s36\n" SA"s60,s50,7\n""s_sub_u32 s60,s60,s36\n""s_lshl_b32 s59,s59,17\n""s_lshl_b32 s60,s60,17\n""s_lshl_b32 s50,s37,12\n" SA"s59,s59,s50\n"
 SA"s60,s60,s50\n""s_mov_b32 s48,s59\n""s_mov_b32 s49,s60\n""s_mov_b32 s61,1\n""v_mbcnt_lo_u32_b32 v12,-1,0\n""v_mbcnt_hi_u32_b32 v12,-1,v12\n""v_lshlrev_b32 v0,4,v12\n""v_lshrrev_b32 v13,3,v12\n"
"v_mul_u32_u24 v13,2752,v13\n""v_lshrrev_b32 v14,4,v12\n""v_lshlrev_b32 v15,1,v14\n""v_and_b32 v2,7,v12\n""v_xor_b32 v15,v15,v2\n""v_lshl_add_u32 v2,v15,4,v13\n" VV"v3,v0\n""s_lshl_b32 s63,s36,12\n"
"s_lshr_b32 s38,s96,5\n""s_and_b32 s39,s96,7\n""s_lshl_b32 s39,s39,2\n""s_bfe_u32 s57,s96,0x20003\n" SA"s39,s39,s57\n""s_lshl_b32 s42,s39,2\n" SA"s42,s42,s36\n""v_readlane_b32 s72,v254,16\n"
"v_readlane_b32 s73,v254,17\n""s_mov_b32 s74,1409024\n""s_mov_b32 s75,0x20000\n" SA"s76,s52,7340032\n""s_addc_u32 s77,s53,0\n""s_mov_b32 s78,2097152\n""s_mov_b32 s79,0x20000\n"
"s_mul_i32 s40,s36,88064\n""s_lshl_b32 s57,s38,7\n" SA"s40,s40,s57\n" SA"s46,s40,1024\n" SA"s47,s40,2048\n""s_cmp_gt_u32 s38,5\n""s_cselect_b32 s47,0x70000000,s47\n""s_lshl_b32 s41,s42,14\n"
"s_nop 7\n" AR"v200,a64\n" AR"v201,a68\n""v_rcp_f32 v204,v200\n""v_rcp_f32 v206,v201\n""v_min_f32 v202,v200,v201\n""v_max_f32 v203,v200,v201\n""v_cmp_le_f32 vcc,0x26800000,v202\n"
"s_mov_b32 s55,0x58800000\n""v_cmp_ge_f32 s[50:51],s55,v203\n""v_cmp_o_f32 s[70:71],v200,v201\n""s_nop 0\n""s_and_b64 vcc,vcc,s[50:51]\n""s_and_b64 vcc,vcc,s[70:71]\n""s_cmp_eq_u64 vcc,-1\n"
"s_cselect_b32 s50,0,1\n""s_lshl_b32 s50,s50,s61\n""s_or_b32 s54,s54,s50\n" AR"v208,a0\n" AR"v209,a1\n" AR"v210,a2\n" AR"v211,a3\n" AR"v212,a8\n" AR"v213,a9\n" AR"v214,a10\n" AR"v215,a11\n"
 VM"v208,v208,v204\n" VM"v209,v209,v204\n" VM"v210,v210,v204\n" VM"v211,v211,v204\n" VM"v212,v212,v204\n" VM"v213,v213,v204\n" VM"v214,v214,v204\n" VM"v215,v215,v204\n" CV"v216,v208,v209\n"
 CV"v217,v210,v211\n" CV"v218,v212,v213\n" CV"v219,v214,v215\n" BS"v[216:219],v0,s[64:67],s48 offen offset:0 sc1\n" AR"v220,a16\n" AR"v221,a17\n" AR"v222,a18\n" AR"v223,a19\n" AR"v224,a24\n"
 AR"v225,a25\n" AR"v226,a26\n" AR"v227,a27\n" VM"v220,v220,v204\n" VM"v221,v221,v204\n" VM"v222,v222,v204\n" VM"v223,v223,v204\n" VM"v224,v224,v204\n" VM"v225,v225,v204\n" VM"v226,v226,v204\n"
 VM"v227,v227,v204\n" CV"v228,v220,v221\n" CV"v229,v222,v223\n" CV"v230,v224,v225\n" CV"v231,v226,v227\n" BS"v[228:231],v0,s[64:67],s48 offen offset:1024 sc1\n" AR"v232,a32\n" AR"v233,a33\n"
 AR"v234,a34\n" AR"v235,a35\n" AR"v236,a40\n" AR"v237,a41\n" AR"v238,a42\n" AR"v239,a43\n" VM"v232,v232,v204\n" VM"v233,v233,v204\n" VM"v234,v234,v204\n" VM"v235,v235,v204\n" VM"v236,v236,v204\n"
 VM"v237,v237,v204\n" VM"v238,v238,v204\n" VM"v239,v239,v204\n" CV"v240,v232,v233\n" CV"v241,v234,v235\n" CV"v242,v236,v237\n" CV"v243,v238,v239\n"
 BS"v[240:243],v0,s[64:67],s48 offen offset:2048 sc1\n" AR"v208,a48\n" AR"v209,a49\n" AR"v210,a50\n" AR"v211,a51\n" AR"v212,a56\n" AR"v213,a57\n" AR"v214,a58\n" AR"v215,a59\n" VM"v208,v208,v204\n"
 VM"v209,v209,v204\n" VM"v210,v210,v204\n" VM"v211,v211,v204\n" SA"m0,s63,0\n" SA"s58,s40,0\n" BL"v2,s[72:75],s58 offen lds\n" VM"v212,v212,v204\n" VM"v213,v213,v204\n" VM"v214,v214,v204\n"
 VM"v215,v215,v204\n" CV"v216,v208,v209\n" CV"v217,v210,v211\n" CV"v218,v212,v213\n" CV"v219,v214,v215\n" BS"v[216:219],v0,s[64:67],s48 offen offset:3072 sc1\n" AR"v220,a4\n" AR"v221,a5\n"
 SA"m0,s63,1024\n" SA"s58,s40,22016\n" BL"v2,s[72:75],s58 offen lds\n" AR"v222,a6\n" AR"v223,a7\n" AR"v224,a12\n" AR"v225,a13\n" AR"v226,a14\n" AR"v227,a15\n" VM"v220,v220,v206\n" VM"v221,v221,v206\n"
 VM"v222,v222,v206\n" VM"v223,v223,v206\n" VM"v224,v224,v206\n" SA"m0,s63,2048\n" SA"s58,s40,44032\n" BL"v2,s[72:75],s58 offen lds\n" VM"v225,v225,v206\n" VM"v226,v226,v206\n" VM"v227,v227,v206\n"
 CV"v228,v220,v221\n" CV"v229,v222,v223\n" CV"v230,v224,v225\n" CV"v231,v226,v227\n" BS"v[228:231],v0,s[64:67],s49 offen offset:0 sc1\n" AR"v232,a20\n" AR"v233,a21\n" AR"v234,a22\n" SA"m0,s63,3072\n"
 SA"s58,s40,66048\n" BL"v2,s[72:75],s58 offen lds\n" AR"v235,a23\n" AR"v236,a28\n" AR"v237,a29\n" AR"v238,a30\n" AR"v239,a31\n" VM"v232,v232,v206\n" VM"v233,v233,v206\n" VM"v234,v234,v206\n"
 VM"v235,v235,v206\n" VM"v236,v236,v206\n" VM"v237,v237,v206\n" SA"m0,s63,16384\n" SA"s58,s40,352256\n" BL"v2,s[72:75],s58 offen lds\n" VM"v238,v238,v206\n" VM"v239,v239,v206\n" CV"v240,v232,v233\n"
 CV"v241,v234,v235\n" CV"v242,v236,v237\n" CV"v243,v238,v239\n" BS"v[240:243],v0,s[64:67],s49 offen offset:1024 sc1\n" AR"v208,a36\n" AR"v209,a37\n" AR"v210,a38\n" AR"v211,a39\n" SA"m0,s63,17408\n"
 SA"s58,s40,374272\n" BL"v2,s[72:75],s58 offen lds\n" AR"v212,a44\n" AR"v213,a45\n" AR"v214,a46\n" AR"v215,a47\n" VM"v208,v208,v206\n" VM"v209,v209,v206\n" VM"v210,v210,v206\n" VM"v211,v211,v206\n"
 VM"v212,v212,v206\n" VM"v213,v213,v206\n" VM"v214,v214,v206\n" SA"m0,s63,18432\n" SA"s58,s40,396288\n" BL"v2,s[72:75],s58 offen lds\n" VM"v215,v215,v206\n" CV"v216,v208,v209\n" CV"v217,v210,v211\n"
 CV"v218,v212,v213\n" CV"v219,v214,v215\n" BS"v[216:219],v0,s[64:67],s49 offen offset:2048 sc1\n" AR"v220,a52\n" AR"v221,a53\n" AR"v222,a54\n" AR"v223,a55\n" AR"v224,a60\n" SA"m0,s63,19456\n"
 SA"s58,s40,418304\n" BL"v2,s[72:75],s58 offen lds\n" AR"v225,a61\n" AR"v226,a62\n" AR"v227,a63\n" VM"v220,v220,v206\n" VM"v221,v221,v206\n" VM"v222,v222,v206\n" VM"v223,v223,v206\n"
 VM"v224,v224,v206\n" VM"v225,v225,v206\n" VM"v226,v226,v206\n" VM"v227,v227,v206\n" SA"m0,s63,32768\n" SA"s58,s40,704512\n" BL"v2,s[72:75],s58 offen lds\n" CV"v228,v220,v221\n" CV"v229,v222,v223\n"
 CV"v230,v224,v225\n" CV"v231,v226,v227\n" BS"v[228:231],v0,s[64:67],s49 offen offset:3072 sc1\n" SA"m0,s63,33792\n" SA"s58,s40,726528\n" BL"v2,s[72:75],s58 offen lds\n" SA"m0,s63,34816\n"
 SA"s58,s40,748544\n" BL"v2,s[72:75],s58 offen lds\n" SA"m0,s63,35840\n" SA"s58,s40,770560\n" BL"v2,s[72:75],s58 offen lds\n" SA"m0,s63,49152\n" SA"s58,s40,1056768\n" BL"v2,s[72:75],s58 offen lds\n"
 SA"m0,s63,50176\n" SA"s58,s40,1078784\n" BL"v2,s[72:75],s58 offen lds\n" SA"m0,s63,51200\n" SA"s58,s40,1100800\n" BL"v2,s[72:75],s58 offen lds\n" SA"m0,s63,52224\n" SA"s58,s40,1122816\n"
 BL"v2,s[72:75],s58 offen lds\n" SA"m0,s63,65536\n" SA"s58,s46,0\n" BL"v2,s[72:75],s58 offen lds\n" SA"m0,s63,66560\n" SA"s58,s46,22016\n" BL"v2,s[72:75],s58 offen lds\n" SA"m0,s63,67584\n"
 SA"s58,s46,44032\n" BL"v2,s[72:75],s58 offen lds\n" SA"m0,s63,68608\n" SA"s58,s46,66048\n" BL"v2,s[72:75],s58 offen lds\n" SA"m0,s63,81920\n" SA"s58,s46,352256\n" BL"v2,s[72:75],s58 offen lds\n"
 SA"m0,s63,82944\n" SA"s58,s46,374272\n" BL"v2,s[72:75],s58 offen lds\n" SA"m0,s63,83968\n" SA"s58,s46,396288\n" BL"v2,s[72:75],s58 offen lds\n" SA"m0,s63,84992\n" SA"s58,s46,418304\n"
 BL"v2,s[72:75],s58 offen lds\n" WV"(48)\n""v_cmp_ne_u64 vcc,s[98:99],v[248:249]\n""s_nop 1\n""s_cmp_eq_u64 vcc,0\n""s_cbranch_scc1 Lokp3_%=\n""s_mov_b32 s57,32768\n""Lspp3_%=:\n""s_sleep 2\n"
"buffer_load_dwordx2 v[248:249],v244,s[92:95],0 offen sc1\n" WV"(0)\n""v_cmp_ne_u64 vcc,s[98:99],v[248:249]\n""s_nop 1\n""s_cmp_eq_u64 vcc,0\n""s_cbranch_scc1 Lokp3_%=\n""s_sub_u32 s57,s57,1\n"
"s_cmp_lg_u32 s57,0\n""s_cbranch_scc1 Lspp3_%=\n""Lokp3_%=:\n" SA"s59,s41,0\n" BL"v[16:19],v3,s[76:79],s59 offen offset:0 sc1\n" BL"v[20:23],v3,s[76:79],s59 offen offset:1024 sc1\n"
 BL"v[24:27],v3,s[76:79],s59 offen offset:2048 sc1\n" BL"v[28:31],v3,s[76:79],s59 offen offset:3072 sc1\n" SA"s59,s41,4096\n" BL"v[32:35],v3,s[76:79],s59 offen offset:0 sc1\n"
 BL"v[36:39],v3,s[76:79],s59 offen offset:1024 sc1\n" BL"v[40:43],v3,s[76:79],s59 offen offset:2048 sc1\n" BL"v[44:47],v3,s[76:79],s59 offen offset:3072 sc1\n" SA"s59,s41,8192\n"
 BL"v[48:51],v3,s[76:79],s59 offen offset:0 sc1\n" BL"v[52:55],v3,s[76:79],s59 offen offset:1024 sc1\n" BL"v[56:59],v3,s[76:79],s59 offen offset:2048 sc1\n"
 BL"v[60:63],v3,s[76:79],s59 offen offset:3072 sc1\n" SA"s59,s41,12288\n" BL"v[64:67],v3,s[76:79],s59 offen offset:0 sc1\n" BL"v[68:71],v3,s[76:79],s59 offen offset:1024 sc1\n"
 BL"v[72:75],v3,s[76:79],s59 offen offset:2048 sc1\n" BL"v[76:79],v3,s[76:79],s59 offen offset:3072 sc1\n""s_lshl_b32 s50,s36,2\n" SA"s50,s50,163776\n" VV"v12,s50\n" VV"v13,s54\n"
"ds_write_b32 v12,v13\n" WL"(0)\n""s_barrier\n" VV"v12,163776\n" DR"v[96:99],v12\n" WL"(0)\n""v_or_b32 v96,v96,v97\n""v_or_b32 v98,v98,v99\n""v_or_b32 v96,v96,v98\n""s_nop 0\n"
"v_readfirstlane_b32 s54,v96\n""s_nop 0\n""s_cmp_lg_u32 s54,0\n""s_cbranch_scc1 Lexit_%=\n""Lg3_%=:\n" SA"m0,s63,98304\n" SA"s58,s46,704512\n" BL"v2,s[72:75],s58 offen lds\n" SA"m0,s63,99328\n"
 SA"s58,s46,726528\n" BL"v2,s[72:75],s58 offen lds\n" SA"m0,s63,100352\n" SA"s58,s46,748544\n" BL"v2,s[72:75],s58 offen lds\n" SA"m0,s63,101376\n" SA"s58,s46,770560\n" BL"v2,s[72:75],s58 offen lds\n"
"v_mbcnt_lo_u32_b32 v12,-1,0\n""v_mbcnt_hi_u32_b32 v12,-1,v12\n""v_lshrrev_b32 v14,4,v12\n""v_bfe_u32 v15,v12,2,2\n""v_lshl_add_u32 v15,v14,2,v15\n""v_lshlrev_b32 v13,7,v15\n""v_and_b32 v14,3,v12\n"
"v_lshl_add_u32 v13,v14,3,v13\n""v_bfe_u32 v15,v15,1,2\n""v_xor_b32 v14,0,v15\n""v_lshl_add_u32 v4,v14,5,v13\n""v_add_u32 v8,0x10000,v4\n""v_xor_b32 v14,1,v15\n""v_lshl_add_u32 v5,v14,5,v13\n"
"v_add_u32 v9,0x10000,v5\n""v_xor_b32 v14,2,v15\n""v_lshl_add_u32 v6,v14,5,v13\n""v_add_u32 v10,0x10000,v6\n""v_xor_b32 v14,3,v15\n""v_lshl_add_u32 v7,v14,5,v13\n""v_add_u32 v11,0x10000,v7\n"
 WV"(16)\n""s_barrier\n""s_and_b32 s57,s96,31\n""s_lshr_b32 s58,s96,5\n""s_lshl_b32 s58,s58,5\n" SA"s57,s57,s58\n""s_lshl_b32 s57,s57,3\n" SA"s57,s57,24576\n" VV"v250,s98\n" VV"v251,s99\n"
 VV"v252,0\n""buffer_store_dwordx2 v[250:251],v252,s[92:95],s57 offen sc1\n" TR"a[128:129],v4 offset:0\n" TR"a[130:131],v4 offset:2048\n" TR"a[132:133],v5 offset:0\n" TR"a[134:135],v5 offset:2048\n"
 TR"a[136:137],v6 offset:0\n" TR"a[138:139],v6 offset:2048\n" TR"a[140:141],v7 offset:0\n" TR"a[142:143],v7 offset:2048\n" TR"a[144:145],v4 offset:4096\n" TR"a[146:147],v4 offset:6144\n"
 TR"a[148:149],v5 offset:4096\n" TR"a[150:151],v5 offset:6144\n" TR"a[152:153],v6 offset:4096\n" TR"a[154:155],v6 offset:6144\n" TR"a[156:157],v7 offset:4096\n" WL"(8)\n"
 TR"a[158:159],v7 offset:6144\n" TR"a[160:161],v4 offset:8192\n" TR"a[162:163],v4 offset:10240\n" TR"a[164:165],v5 offset:8192\n" TR"a[166:167],v5 offset:10240\n" TR"a[168:169],v6 offset:8192\n"
 TR"a[170:171],v6 offset:10240\n" WL"(8)\n" TR"a[172:173],v7 offset:8192\n" TR"a[174:175],v7 offset:10240\n" TR"a[176:177],v4 offset:12288\n" TR"a[178:179],v4 offset:14336\n"
 TR"a[180:181],v5 offset:12288\n" TR"a[182:183],v5 offset:14336\n" TR"a[184:185],v6 offset:12288\n" WL"(8)\n" TR"a[186:187],v6 offset:14336\n" TR"a[188:189],v7 offset:12288\n"
 TR"a[190:191],v7 offset:14336\n" WV"(13)\n""s_barrier\n" SA"s80,s52,23068672\n""s_addc_u32 s81,s53,0\n""s_mov_b32 s82,131072\n""s_mov_b32 s83,0x20000\n" SA"s84,s52,24117248\n""s_addc_u32 s85,s53,0\n"
"s_mov_b32 s86,5504\n""s_mov_b32 s87,0x20000\n""v_bfe_u32 v13,v0,4,4\n""v_lshlrev_b32 v13,6,v13\n""v_lshrrev_b32 v15,8,v0\n""v_lshl_add_u32 v13,v15,4,v13\n""s_lshl_b32 s58,s42,10\n"
 BL"v[240:243],v13,s[80:83],s58 offen sc1\n""v_lshlrev_b32 v15,4,v15\n""s_lshl_b32 s58,s38,8\n" BL"v[160:163],v15,s[84:87],s58 offen offset:0 sc1\n"
 BL"v[164:167],v15,s[84:87],s58 offen offset:64 sc1\n" BL"v[168:171],v15,s[84:87],s58 offen offset:128 sc1\n" BL"v[172:175],v15,s[84:87],s58 offen offset:192 sc1\n" SA"s58,s58,2048\n"
 BL"v[176:179],v15,s[84:87],s58 offen offset:0 sc1\n" BL"v[180:183],v15,s[84:87],s58 offen offset:64 sc1\n" BL"v[184:187],v15,s[84:87],s58 offen offset:128 sc1\n"
 BL"v[188:191],v15,s[84:87],s58 offen offset:192 sc1\n" SA"s58,s58,2048\n" BL"v[144:147],v15,s[84:87],s58 offen offset:0 sc1\n" BL"v[148:151],v15,s[84:87],s58 offen offset:64 sc1\n"
 BL"v[152:155],v15,s[84:87],s58 offen offset:128 sc1\n" BL"v[156:159],v15,s[84:87],s58 offen offset:192 sc1\n" MF"v[192:195],a[128:131],v[16:19],0\n" TR"a[192:193],v4 offset:16384\n"
 TR"a[194:195],v4 offset:18432\n" MF"v[196:199],a[132:135],v[16:19],0\n" TR"a[196:197],v5 offset:16384\n" TR"a[198:199],v5 offset:18432\n" SA"m0,s63,114688\n" SA"s58,s46,1056768\n"
 BL"v2,s[72:75],s58 offen lds\n" MF"v[200:203],a[136:139],v[16:19],0\n" WL"(8)\n" TR"a[200:201],v6 offset:16384\n" TR"a[202:203],v6 offset:18432\n" MF"v[204:207],a[140:143],v[16:19],0\n"
 TR"a[204:205],v7 offset:16384\n" TR"a[206:207],v7 offset:18432\n" SA"m0,s63,115712\n" SA"s58,s46,1078784\n" BL"v2,s[72:75],s58 offen lds\n" MF"v[192:195],a[144:147],v[20:23],v[192:195]\n"
 TR"a[208:209],v4 offset:20480\n" TR"a[210:211],v4 offset:22528\n" MF"v[196:199],a[148:151],v[20:23],v[196:199]\n" TR"a[212:213],v5 offset:20480\n" WL"(8)\n" TR"a[214:215],v5 offset:22528\n"
 SA"m0,s63,116736\n" SA"s58,s46,1100800\n" BL"v2,s[72:75],s58 offen lds\n" MF"v[200:203],a[152:155],v[20:23],v[200:203]\n" TR"a[216:217],v6 offset:20480\n" TR"a[218:219],v6 offset:22528\n"
 MF"v[204:207],a[156:159],v[20:23],v[204:207]\n" TR"a[220:221],v7 offset:20480\n" TR"a[222:223],v7 offset:22528\n" SA"m0,s63,117760\n" SA"s58,s46,1122816\n" BL"v2,s[72:75],s58 offen lds\n"
 MF"v[192:195],a[160:163],v[24:27],v[192:195]\n" TR"a[224:225],v4 offset:24576\n" TR"a[226:227],v4 offset:26624\n" MF"v[196:199],a[164:167],v[24:27],v[196:199]\n" WL"(8)\n"
 TR"a[228:229],v5 offset:24576\n" TR"a[230:231],v5 offset:26624\n" MF"v[200:203],a[168:171],v[24:27],v[200:203]\n" TR"a[232:233],v6 offset:24576\n" TR"a[234:235],v6 offset:26624\n"
 MF"v[204:207],a[172:175],v[24:27],v[204:207]\n" TR"a[236:237],v7 offset:24576\n" TR"a[238:239],v7 offset:26624\n" MF"v[192:195],a[176:179],v[28:31],v[192:195]\n" TR"a[240:241],v4 offset:28672\n"
 WL"(8)\n" TR"a[242:243],v4 offset:30720\n" MF"v[196:199],a[180:183],v[28:31],v[196:199]\n" TR"a[244:245],v5 offset:28672\n" TR"a[246:247],v5 offset:30720\n"
 MF"v[200:203],a[184:187],v[28:31],v[200:203]\n" TR"a[248:249],v6 offset:28672\n" TR"a[250:251],v6 offset:30720\n" MF"v[204:207],a[188:191],v[28:31],v[204:207]\n" TR"a[252:253],v7 offset:28672\n"
 TR"a[254:255],v7 offset:30720\n" WV"(26)\n""s_barrier\n" MF"v[192:195],a[192:195],v[32:35],v[192:195]\n" WL"(8)\n" TR"a[128:129],v4 offset:32768\n" TR"a[130:131],v4 offset:34816\n"
 MF"v[196:199],a[196:199],v[32:35],v[196:199]\n" TR"a[132:133],v5 offset:32768\n" TR"a[134:135],v5 offset:34816\n" SA"m0,s63,0\n" SA"s58,s47,0\n" BL"v2,s[72:75],s58 offen lds\n"
 MF"v[200:203],a[200:203],v[32:35],v[200:203]\n" TR"a[136:137],v6 offset:32768\n" TR"a[138:139],v6 offset:34816\n" MF"v[204:207],a[204:207],v[32:35],v[204:207]\n" TR"a[140:141],v7 offset:32768\n"
 WL"(8)\n" TR"a[142:143],v7 offset:34816\n" SA"m0,s63,1024\n" SA"s58,s47,22016\n" BL"v2,s[72:75],s58 offen lds\n" MF"v[192:195],a[208:211],v[36:39],v[192:195]\n" TR"a[144:145],v4 offset:36864\n"
 TR"a[146:147],v4 offset:38912\n" MF"v[196:199],a[212:215],v[36:39],v[196:199]\n" TR"a[148:149],v5 offset:36864\n" TR"a[150:151],v5 offset:38912\n" SA"m0,s63,2048\n" SA"s58,s47,44032\n"
 BL"v2,s[72:75],s58 offen lds\n" MF"v[200:203],a[216:219],v[36:39],v[200:203]\n" TR"a[152:153],v6 offset:36864\n" TR"a[154:155],v6 offset:38912\n" MF"v[204:207],a[220:223],v[36:39],v[204:207]\n"
 WL"(8)\n" TR"a[156:157],v7 offset:36864\n" TR"a[158:159],v7 offset:38912\n" SA"m0,s63,3072\n" SA"s58,s47,66048\n" BL"v2,s[72:75],s58 offen lds\n" MF"v[192:195],a[224:227],v[40:43],v[192:195]\n"
 TR"a[160:161],v4 offset:40960\n" TR"a[162:163],v4 offset:43008\n" MF"v[196:199],a[228:231],v[40:43],v[196:199]\n" TR"a[164:165],v5 offset:40960\n" TR"a[166:167],v5 offset:43008\n"
 MF"v[200:203],a[232:235],v[40:43],v[200:203]\n" TR"a[168:169],v6 offset:40960\n" WL"(8)\n" TR"a[170:171],v6 offset:43008\n" MF"v[204:207],a[236:239],v[40:43],v[204:207]\n"
 TR"a[172:173],v7 offset:40960\n" TR"a[174:175],v7 offset:43008\n" MF"v[192:195],a[240:243],v[44:47],v[192:195]\n" TR"a[176:177],v4 offset:45056\n" TR"a[178:179],v4 offset:47104\n"
 MF"v[196:199],a[244:247],v[44:47],v[196:199]\n" TR"a[180:181],v5 offset:45056\n" TR"a[182:183],v5 offset:47104\n" MF"v[200:203],a[248:251],v[44:47],v[200:203]\n" WL"(8)\n"
 TR"a[184:185],v6 offset:45056\n" TR"a[186:187],v6 offset:47104\n" MF"v[204:207],a[252:255],v[44:47],v[204:207]\n" TR"a[188:189],v7 offset:45056\n" TR"a[190:191],v7 offset:47104\n" WV"(26)\n"
"s_barrier\n" MF"v[192:195],a[128:131],v[48:51],v[192:195]\n" TR"a[192:193],v4 offset:49152\n" TR"a[194:195],v4 offset:51200\n" MF"v[196:199],a[132:135],v[48:51],v[196:199]\n"
 TR"a[196:197],v5 offset:49152\n" WL"(8)\n" TR"a[198:199],v5 offset:51200\n" SA"m0,s63,16384\n" SA"s58,s47,352256\n" BL"v2,s[72:75],s58 offen lds\n" MF"v[200:203],a[136:139],v[48:51],v[200:203]\n"
 TR"a[200:201],v6 offset:49152\n" TR"a[202:203],v6 offset:51200\n" MF"v[204:207],a[140:143],v[48:51],v[204:207]\n" TR"a[204:205],v7 offset:49152\n" TR"a[206:207],v7 offset:51200\n" SA"m0,s63,17408\n"
 SA"s58,s47,374272\n" BL"v2,s[72:75],s58 offen lds\n" MF"v[192:195],a[144:147],v[52:55],v[192:195]\n" TR"a[208:209],v4 offset:53248\n" TR"a[210:211],v4 offset:55296\n"
 MF"v[196:199],a[148:151],v[52:55],v[196:199]\n" WL"(8)\n" TR"a[212:213],v5 offset:53248\n" TR"a[214:215],v5 offset:55296\n" SA"m0,s63,18432\n" SA"s58,s47,396288\n" BL"v2,s[72:75],s58 offen lds\n"
 MF"v[200:203],a[152:155],v[52:55],v[200:203]\n" TR"a[216:217],v6 offset:53248\n" TR"a[218:219],v6 offset:55296\n" MF"v[204:207],a[156:159],v[52:55],v[204:207]\n" TR"a[220:221],v7 offset:53248\n"
 TR"a[222:223],v7 offset:55296\n" SA"m0,s63,19456\n" SA"s58,s47,418304\n" BL"v2,s[72:75],s58 offen lds\n" MF"v[192:195],a[160:163],v[56:59],v[192:195]\n" TR"a[224:225],v4 offset:57344\n" WL"(8)\n"
 TR"a[226:227],v4 offset:59392\n" MF"v[196:199],a[164:167],v[56:59],v[196:199]\n" TR"a[228:229],v5 offset:57344\n" TR"a[230:231],v5 offset:59392\n" MF"v[200:203],a[168:171],v[56:59],v[200:203]\n"
 TR"a[232:233],v6 offset:57344\n" TR"a[234:235],v6 offset:59392\n" MF"v[204:207],a[172:175],v[56:59],v[204:207]\n" TR"a[236:237],v7 offset:57344\n" TR"a[238:239],v7 offset:59392\n"
 MF"v[192:195],a[176:179],v[60:63],v[192:195]\n" WL"(8)\n" TR"a[240:241],v4 offset:61440\n" TR"a[242:243],v4 offset:63488\n" MF"v[196:199],a[180:183],v[60:63],v[196:199]\n"
 TR"a[244:245],v5 offset:61440\n" TR"a[246:247],v5 offset:63488\n" MF"v[200:203],a[184:187],v[60:63],v[200:203]\n" TR"a[248:249],v6 offset:61440\n" TR"a[250:251],v6 offset:63488\n"
 MF"v[204:207],a[188:191],v[60:63],v[204:207]\n" TR"a[252:253],v7 offset:61440\n" WL"(8)\n" TR"a[254:255],v7 offset:63488\n" WV"(50)\n""s_barrier\n" MF"v[192:195],a[192:195],v[64:67],v[192:195]\n"
 TR"a[128:129],v8 offset:0\n" TR"a[130:131],v8 offset:2048\n" MF"v[196:199],a[196:199],v[64:67],v[196:199]\n" TR"a[132:133],v9 offset:0\n" TR"a[134:135],v9 offset:2048\n" SA"m0,s63,32768\n"
 SA"s58,s47,704512\n" BL"v2,s[72:75],s58 offen lds\n" MF"v[200:203],a[200:203],v[64:67],v[200:203]\n" TR"a[136:137],v10 offset:0\n" TR"a[138:139],v10 offset:2048\n"
 MF"v[204:207],a[204:207],v[64:67],v[204:207]\n" WL"(8)\n" TR"a[140:141],v11 offset:0\n" TR"a[142:143],v11 offset:2048\n" SA"m0,s63,33792\n" SA"s58,s47,726528\n" BL"v2,s[72:75],s58 offen lds\n"
 MF"v[192:195],a[208:211],v[68:71],v[192:195]\n" TR"a[144:145],v8 offset:4096\n" TR"a[146:147],v8 offset:6144\n" MF"v[196:199],a[212:215],v[68:71],v[196:199]\n" TR"a[148:149],v9 offset:4096\n"
 TR"a[150:151],v9 offset:6144\n" SA"m0,s63,34816\n" SA"s58,s47,748544\n" BL"v2,s[72:75],s58 offen lds\n" MF"v[200:203],a[216:219],v[68:71],v[200:203]\n" TR"a[152:153],v10 offset:4096\n" WL"(8)\n"
 TR"a[154:155],v10 offset:6144\n" MF"v[204:207],a[220:223],v[68:71],v[204:207]\n" TR"a[156:157],v11 offset:4096\n" TR"a[158:159],v11 offset:6144\n" SA"m0,s63,35840\n" SA"s58,s47,770560\n"
 BL"v2,s[72:75],s58 offen lds\n" MF"v[192:195],a[224:227],v[72:75],v[192:195]\n" TR"a[160:161],v8 offset:8192\n" TR"a[162:163],v8 offset:10240\n" MF"v[196:199],a[228:231],v[72:75],v[196:199]\n"
 TR"a[164:165],v9 offset:8192\n" TR"a[166:167],v9 offset:10240\n" MF"v[200:203],a[232:235],v[72:75],v[200:203]\n" WL"(8)\n" TR"a[168:169],v10 offset:8192\n" TR"a[170:171],v10 offset:10240\n"
 MF"v[204:207],a[236:239],v[72:75],v[204:207]\n" TR"a[172:173],v11 offset:8192\n" TR"a[174:175],v11 offset:10240\n" MF"v[192:195],a[240:243],v[76:79],v[192:195]\n" TR"a[176:177],v8 offset:12288\n"
 TR"a[178:179],v8 offset:14336\n" MF"v[196:199],a[244:247],v[76:79],v[196:199]\n" TR"a[180:181],v9 offset:12288\n" WL"(8)\n" TR"a[182:183],v9 offset:14336\n"
 MF"v[200:203],a[248:251],v[76:79],v[200:203]\n" TR"a[184:185],v10 offset:12288\n" TR"a[186:187],v10 offset:14336\n" MF"v[204:207],a[252:255],v[76:79],v[204:207]\n" TR"a[188:189],v11 offset:12288\n"
 TR"a[190:191],v11 offset:14336\n" WV"(50)\n""s_barrier\n" MF"v[208:211],a[128:131],v[16:19],0\n" TR"a[192:193],v8 offset:16384\n" TR"a[194:195],v8 offset:18432\n"
 MF"v[212:215],a[132:135],v[16:19],0\n" WL"(8)\n" TR"a[196:197],v9 offset:16384\n" TR"a[198:199],v9 offset:18432\n" SA"m0,s63,49152\n" SA"s58,s47,1056768\n" BL"v2,s[72:75],s58 offen lds\n"
 MF"v[216:219],a[136:139],v[16:19],0\n" TR"a[200:201],v10 offset:16384\n" TR"a[202:203],v10 offset:18432\n" MF"v[220:223],a[140:143],v[16:19],0\n" TR"a[204:205],v11 offset:16384\n"
 TR"a[206:207],v11 offset:18432\n" SA"m0,s63,50176\n" SA"s58,s47,1078784\n" BL"v2,s[72:75],s58 offen lds\n" MF"v[208:211],a[144:147],v[20:23],v[208:211]\n" TR"a[208:209],v8 offset:20480\n" WL"(8)\n"
 TR"a[210:211],v8 offset:22528\n" MF"v[212:215],a[148:151],v[20:23],v[212:215]\n" TR"a[212:213],v9 offset:20480\n" TR"a[214:215],v9 offset:22528\n" SA"m0,s63,51200\n" SA"s58,s47,1100800\n"
 BL"v2,s[72:75],s58 offen lds\n" MF"v[216:219],a[152:155],v[20:23],v[216:219]\n" TR"a[216:217],v10 offset:20480\n" TR"a[218:219],v10 offset:22528\n" MF"v[220:223],a[156:159],v[20:23],v[220:223]\n"
 TR"a[220:221],v11 offset:20480\n" TR"a[222:223],v11 offset:22528\n" SA"m0,s63,52224\n" SA"s58,s47,1122816\n" BL"v2,s[72:75],s58 offen lds\n" MF"v[208:211],a[160:163],v[24:27],v[208:211]\n" WL"(8)\n"
 TR"a[224:225],v8 offset:24576\n" TR"a[226:227],v8 offset:26624\n" MF"v[212:215],a[164:167],v[24:27],v[212:215]\n" TR"a[228:229],v9 offset:24576\n" TR"a[230:231],v9 offset:26624\n"
 MF"v[216:219],a[168:171],v[24:27],v[216:219]\n" TR"a[232:233],v10 offset:24576\n" TR"a[234:235],v10 offset:26624\n" MF"v[220:223],a[172:175],v[24:27],v[220:223]\n" TR"a[236:237],v11 offset:24576\n"
 WL"(8)\n" TR"a[238:239],v11 offset:26624\n" MF"v[208:211],a[176:179],v[28:31],v[208:211]\n" TR"a[240:241],v8 offset:28672\n" TR"a[242:243],v8 offset:30720\n"
 MF"v[212:215],a[180:183],v[28:31],v[212:215]\n" TR"a[244:245],v9 offset:28672\n" TR"a[246:247],v9 offset:30720\n" MF"v[216:219],a[184:187],v[28:31],v[216:219]\n" TR"a[248:249],v10 offset:28672\n"
 TR"a[250:251],v10 offset:30720\n" MF"v[220:223],a[188:191],v[28:31],v[220:223]\n" WL"(8)\n" TR"a[252:253],v11 offset:28672\n" TR"a[254:255],v11 offset:30720\n" WV"(34)\n""s_barrier\n"
"s_mul_i32 s97,s42,44032\n""s_lshl_b32 s57,s38,11\n" SA"s97,s97,s57\n""s_mov_b32 s65,s38\n" MF"v[208:211],a[192:195],v[32:35],v[208:211]\n" TR"a[128:129],v8 offset:32768\n"
 TR"a[130:131],v8 offset:34816\n" MF"v[212:215],a[196:199],v[32:35],v[212:215]\n" TR"a[132:133],v9 offset:32768\n" TR"a[134:135],v9 offset:34816\n" MF"v[216:219],a[200:203],v[32:35],v[216:219]\n"
 TR"a[136:137],v10 offset:32768\n" WL"(8)\n" TR"a[138:139],v10 offset:34816\n" MF"v[220:223],a[204:207],v[32:35],v[220:223]\n" TR"a[140:141],v11 offset:32768\n" TR"a[142:143],v11 offset:34816\n"
 MF"v[208:211],a[208:211],v[36:39],v[208:211]\n" TR"a[144:145],v8 offset:36864\n" TR"a[146:147],v8 offset:38912\n" MF"v[212:215],a[212:215],v[36:39],v[212:215]\n" TR"a[148:149],v9 offset:36864\n"
 TR"a[150:151],v9 offset:38912\n" MF"v[216:219],a[216:219],v[36:39],v[216:219]\n" WL"(8)\n" TR"a[152:153],v10 offset:36864\n" TR"a[154:155],v10 offset:38912\n"
 MF"v[220:223],a[220:223],v[36:39],v[220:223]\n" TR"a[156:157],v11 offset:36864\n" TR"a[158:159],v11 offset:38912\n" MF"v[208:211],a[224:227],v[40:43],v[208:211]\n" TR"a[160:161],v8 offset:40960\n"
 TR"a[162:163],v8 offset:43008\n" MF"v[212:215],a[228:231],v[40:43],v[212:215]\n" TR"a[164:165],v9 offset:40960\n" WL"(8)\n" TR"a[166:167],v9 offset:43008\n"
 MF"v[216:219],a[232:235],v[40:43],v[216:219]\n" TR"a[168:169],v10 offset:40960\n" TR"a[170:171],v10 offset:43008\n" MF"v[220:223],a[236:239],v[40:43],v[220:223]\n" TR"a[172:173],v11 offset:40960\n"
 TR"a[174:175],v11 offset:43008\n" MF"v[208:211],a[240:243],v[44:47],v[208:211]\n" TR"a[176:177],v8 offset:45056\n" TR"a[178:179],v8 offset:47104\n" MF"v[212:215],a[244:247],v[44:47],v[212:215]\n"
 WL"(8)\n" TR"a[180:181],v9 offset:45056\n" TR"a[182:183],v9 offset:47104\n" MF"v[216:219],a[248:251],v[44:47],v[216:219]\n" TR"a[184:185],v10 offset:45056\n" TR"a[186:187],v10 offset:47104\n"
 MF"v[220:223],a[252:255],v[44:47],v[220:223]\n" TR"a[188:189],v11 offset:45056\n" TR"a[190:191],v11 offset:47104\n" WV"(16)\n""s_barrier\n" MF"v[208:211],a[128:131],v[48:51],v[208:211]\n"
 TR"a[192:193],v8 offset:49152\n" WL"(8)\n" TR"a[194:195],v8 offset:51200\n" MF"v[212:215],a[132:135],v[48:51],v[212:215]\n" TR"a[196:197],v9 offset:49152\n" TR"a[198:199],v9 offset:51200\n"
"v_mbcnt_lo_u32_b32 v251,-1,0\n""v_mbcnt_hi_u32_b32 v251,-1,v251\n""v_min_u32 v251,31,v251\n""s_bfe_u32 s57,s96,0x10002\n""s_lshl_b32 s57,s57,3\n""s_bfe_u32 s58,s96,0x30003\n" SA"s57,s57,s58\n"
"s_sub_u32 s58,15,s57\n""s_min_u32 s57,s57,s58\n""s_lshl_b32 s57,s57,8\n" SA"s57,s57,24576\n" VV"v248,s57\n""v_lshl_add_u32 v248,v251,3,v248\n"
"buffer_load_dwordx2 v[252:253],v248,s[92:95],0 offen sc1\n" MF"v[216:219],a[136:139],v[48:51],v[216:219]\n" TR"a[200:201],v10 offset:49152\n" TR"a[202:203],v10 offset:51200\n"
 MF"v[220:223],a[140:143],v[48:51],v[220:223]\n" TR"a[204:205],v11 offset:49152\n" TR"a[206:207],v11 offset:51200\n" MF"v[208:211],a[144:147],v[52:55],v[208:211]\n" WL"(8)\n"
 TR"a[208:209],v8 offset:53248\n" TR"a[210:211],v8 offset:55296\n" MF"v[212:215],a[148:151],v[52:55],v[212:215]\n" TR"a[212:213],v9 offset:53248\n" TR"a[214:215],v9 offset:55296\n"
 MF"v[216:219],a[152:155],v[52:55],v[216:219]\n" TR"a[216:217],v10 offset:53248\n" TR"a[218:219],v10 offset:55296\n" MF"v[220:223],a[156:159],v[52:55],v[220:223]\n" TR"a[220:221],v11 offset:53248\n"
 WL"(8)\n" TR"a[222:223],v11 offset:55296\n" MF"v[208:211],a[160:163],v[56:59],v[208:211]\n" TR"a[224:225],v8 offset:57344\n" TR"a[226:227],v8 offset:59392\n"
 MF"v[212:215],a[164:167],v[56:59],v[212:215]\n" TR"a[228:229],v9 offset:57344\n" TR"a[230:231],v9 offset:59392\n" MF"v[216:219],a[168:171],v[56:59],v[216:219]\n" TR"a[232:233],v10 offset:57344\n"
 TR"a[234:235],v10 offset:59392\n" MF"v[220:223],a[172:175],v[56:59],v[220:223]\n" WL"(8)\n" TR"a[236:237],v11 offset:57344\n" TR"a[238:239],v11 offset:59392\n"
 MF"v[208:211],a[176:179],v[60:63],v[208:211]\n" TR"a[240:241],v8 offset:61440\n" TR"a[242:243],v8 offset:63488\n" MF"v[212:215],a[180:183],v[60:63],v[212:215]\n" TR"a[244:245],v9 offset:61440\n"
 TR"a[246:247],v9 offset:63488\n" MF"v[216:219],a[184:187],v[60:63],v[216:219]\n" TR"a[248:249],v10 offset:61440\n" WL"(8)\n" TR"a[250:251],v10 offset:63488\n"
 MF"v[220:223],a[188:191],v[60:63],v[220:223]\n" TR"a[252:253],v11 offset:61440\n" TR"a[254:255],v11 offset:63488\n" WV"(13)\n""s_barrier\n" MF"v[208:211],a[192:195],v[64:67],v[208:211]\n"
 TR"a[128:129],v4 offset:0\n" TR"a[130:131],v4 offset:2048\n" MF"v[212:215],a[196:199],v[64:67],v[212:215]\n" TR"a[132:133],v5 offset:0\n" TR"a[134:135],v5 offset:2048\n"
 MF"v[216:219],a[200:203],v[64:67],v[216:219]\n" WL"(8)\n" TR"a[136:137],v6 offset:0\n" TR"a[138:139],v6 offset:2048\n" MF"v[220:223],a[204:207],v[64:67],v[220:223]\n" TR"a[140:141],v7 offset:0\n"
 TR"a[142:143],v7 offset:2048\n" MF"v[208:211],a[208:211],v[68:71],v[208:211]\n" TR"a[144:145],v4 offset:4096\n" TR"a[146:147],v4 offset:6144\n" MF"v[212:215],a[212:215],v[68:71],v[212:215]\n"
 TR"a[148:149],v5 offset:4096\n" WL"(8)\n" TR"a[150:151],v5 offset:6144\n" MF"v[216:219],a[216:219],v[68:71],v[216:219]\n" TR"a[152:153],v6 offset:4096\n" TR"a[154:155],v6 offset:6144\n"
 MF"v[220:223],a[220:223],v[68:71],v[220:223]\n" TR"a[156:157],v7 offset:4096\n" TR"a[158:159],v7 offset:6144\n" MF"v[208:211],a[224:227],v[72:75],v[208:211]\n" TR"a[160:161],v4 offset:8192\n"
 TR"a[162:163],v4 offset:10240\n" MF"v[212:215],a[228:231],v[72:75],v[212:215]\n" WL"(8)\n" TR"a[164:165],v5 offset:8192\n" TR"a[166:167],v5 offset:10240\n"
 MF"v[216:219],a[232:235],v[72:75],v[216:219]\n" TR"a[168:169],v6 offset:8192\n" TR"a[170:171],v6 offset:10240\n" MF"v[220:223],a[236:239],v[72:75],v[220:223]\n" TR"a[172:173],v7 offset:8192\n"
 TR"a[174:175],v7 offset:10240\n" MF"v[208:211],a[240:243],v[76:79],v[208:211]\n" TR"a[176:177],v4 offset:12288\n" WL"(8)\n" TR"a[178:179],v4 offset:14336\n"
 MF"v[212:215],a[244:247],v[76:79],v[212:215]\n" TR"a[180:181],v5 offset:12288\n" TR"a[182:183],v5 offset:14336\n" MF"v[216:219],a[248:251],v[76:79],v[216:219]\n" TR"a[184:185],v6 offset:12288\n"
 TR"a[186:187],v6 offset:14336\n" MF"v[220:223],a[252:255],v[76:79],v[220:223]\n" TR"a[188:189],v7 offset:12288\n" TR"a[190:191],v7 offset:14336\n" WV"(9)\n""s_barrier\n"
 MF"v[224:227],a[128:131],v[16:19],0\n" WL"(8)\n" TR"a[192:193],v4 offset:16384\n" TR"a[194:195],v4 offset:18432\n" MF"v[228:231],a[132:135],v[16:19],0\n" TR"a[196:197],v5 offset:16384\n"
 TR"a[198:199],v5 offset:18432\n""v_mbcnt_lo_u32_b32 v140,-1,0\n""v_mbcnt_hi_u32_b32 v140,-1,v140\n""v_lshlrev_b32 v0,4,v140\n""v_lshrrev_b32 v141,4,v140\n""v_and_b32 v142,15,v140\n"
"v_add_u32 v143,0,v141\n""v_lshlrev_b32 v143,1,v143\n""v_xor_b32 v143,v143,v142\n""v_lshlrev_b32 v143,4,v143\n""v_lshl_add_u32 v2,v141,10,v143\n""v_add_u32 v143,4,v141\n""v_lshlrev_b32 v143,1,v143\n"
"v_xor_b32 v143,v143,v142\n""v_lshlrev_b32 v143,4,v143\n""v_lshl_add_u32 v3,v141,10,v143\n""v_add_u32 v136,6144,v2\n""v_add_u32 v137,9216,v3\n""v_add_u32 v3,3072,v3\n""s_and_b32 s42,s96,3\n"
"s_bfe_u32 s39,s96,0x10002\n""s_lshl_b32 s39,s39,3\n""s_bfe_u32 s57,s96,0x30003\n" SA"s39,s39,s57\n""s_lshr_b32 s38,s96,6\n""v_readlane_b32 s72,v254,10\n""v_readlane_b32 s73,v254,11\n"
"s_mov_b32 s74,4194304\n""s_mov_b32 s75,0x20000\n" SA"s76,s52,67108864\n""s_addc_u32 s77,s53,0\n""s_mov_b32 s78,16777216\n""s_mov_b32 s79,0x20000\n""s_lshl_b32 s40,s42,20\n""s_lshl_b32 s57,s36,14\n"
 SA"s40,s40,s57\n""s_lshl_b32 s58,s38,8\n" SA"s40,s40,s58\n""s_lshl_b32 s57,s39,3\n""s_lshl_b32 s58,s36,1\n" SA"s57,s57,s58\n""s_lshl_b32 s41,s57,17\n""s_lshl_b32 s58,s42,15\n" SA"s41,s41,s58\n"
 SA"s43,s41,131072\n""s_lshl_b32 s63,s36,12\n" SA"s63,s63,16384\n""s_lshl_b32 s61,s36,12\n" MF"v[232:235],a[136:139],v[16:19],0\n" TR"a[200:201],v6 offset:16384\n" TR"a[202:203],v6 offset:18432\n"
 MF"v[236:239],a[140:143],v[16:19],0\n" TR"a[204:205],v7 offset:16384\n" WL"(8)\n" TR"a[206:207],v7 offset:18432\n" MF"v[224:227],a[144:147],v[20:23],v[224:227]\n" TR"a[208:209],v4 offset:20480\n"
 TR"a[210:211],v4 offset:22528\n" MF"v[228:231],a[148:151],v[20:23],v[228:231]\n" TR"a[212:213],v5 offset:20480\n" TR"a[214:215],v5 offset:22528\n" MF"v[232:235],a[152:155],v[20:23],v[232:235]\n"
 TR"a[216:217],v6 offset:20480\n" TR"a[218:219],v6 offset:22528\n" MF"v[236:239],a[156:159],v[20:23],v[236:239]\n" WL"(8)\n" TR"a[220:221],v7 offset:20480\n" TR"a[222:223],v7 offset:22528\n"
 MF"v[224:227],a[160:163],v[24:27],v[224:227]\n" TR"a[224:225],v4 offset:24576\n" TR"a[226:227],v4 offset:26624\n" MF"v[228:231],a[164:167],v[24:27],v[228:231]\n" TR"a[228:229],v5 offset:24576\n"
 TR"a[230:231],v5 offset:26624\n" MF"v[232:235],a[168:171],v[24:27],v[232:235]\n" TR"a[232:233],v6 offset:24576\n" WL"(8)\n" TR"a[234:235],v6 offset:26624\n"
 MF"v[236:239],a[172:175],v[24:27],v[236:239]\n" TR"a[236:237],v7 offset:24576\n" TR"a[238:239],v7 offset:26624\n" MF"v[224:227],a[176:179],v[28:31],v[224:227]\n" TR"a[240:241],v4 offset:28672\n"
 TR"a[242:243],v4 offset:30720\n" MF"v[228:231],a[180:183],v[28:31],v[228:231]\n" TR"a[244:245],v5 offset:28672\n" TR"a[246:247],v5 offset:30720\n" MF"v[232:235],a[184:187],v[28:31],v[232:235]\n"
 WL"(8)\n" TR"a[248:249],v6 offset:28672\n" TR"a[250:251],v6 offset:30720\n" MF"v[236:239],a[188:191],v[28:31],v[236:239]\n" TR"a[252:253],v7 offset:28672\n" TR"a[254:255],v7 offset:30720\n" WV"(5)\n"
"s_barrier\n" WV"(0)\n""v_cmp_ne_u64 vcc,s[98:99],v[252:253]\n""s_nop 1\n""s_cmp_eq_u64 vcc,0\n""s_cbranch_scc1 Lokp4_%=\n""s_mov_b32 s57,32768\n""Lspp4_%=:\n""s_sleep 2\n"
"buffer_load_dwordx2 v[252:253],v248,s[92:95],0 offen sc1\n" WV"(0)\n""v_cmp_ne_u64 vcc,s[98:99],v[252:253]\n""s_nop 1\n""s_cmp_eq_u64 vcc,0\n""s_cbranch_scc1 Lokp4_%=\n""s_sub_u32 s57,s57,1\n"
"s_cmp_lg_u32 s57,0\n""s_cbranch_scc1 Lspp4_%=\n""Lokp4_%=:\n" MF"v[224:227],a[192:195],v[32:35],v[224:227]\n" TR"a[128:129],v4 offset:32768\n" TR"a[130:131],v4 offset:34816\n"
 MF"v[228:231],a[196:199],v[32:35],v[228:231]\n" TR"a[132:133],v5 offset:32768\n" WL"(8)\n" TR"a[134:135],v5 offset:34816\n" SA"m0,s63,65536\n" SA"s58,s40,0\n" BL"v2,s[72:75],s58 offen offset:0 lds\n"
 MF"v[232:235],a[200:203],v[32:35],v[232:235]\n" TR"a[136:137],v6 offset:32768\n" TR"a[138:139],v6 offset:34816\n" MF"v[236:239],a[204:207],v[32:35],v[236:239]\n" TR"a[140:141],v7 offset:32768\n"
 TR"a[142:143],v7 offset:34816\n" BL"v3,s[72:75],s58 offen offset:1024 lds\n" MF"v[224:227],a[208:211],v[36:39],v[224:227]\n" TR"a[144:145],v4 offset:36864\n" TR"a[146:147],v4 offset:38912\n"
 MF"v[228:231],a[212:215],v[36:39],v[228:231]\n" WL"(8)\n" TR"a[148:149],v5 offset:36864\n" TR"a[150:151],v5 offset:38912\n" BL"v136,s[72:75],s58 offen offset:2048 lds\n"
 MF"v[232:235],a[216:219],v[36:39],v[232:235]\n" TR"a[152:153],v6 offset:36864\n" TR"a[154:155],v6 offset:38912\n" MF"v[236:239],a[220:223],v[36:39],v[236:239]\n" TR"a[156:157],v7 offset:36864\n"
 TR"a[158:159],v7 offset:38912\n" BL"v137,s[72:75],s58 offen offset:3072 lds\n" MF"v[224:227],a[224:227],v[40:43],v[224:227]\n" TR"a[160:161],v4 offset:40960\n" WL"(8)\n"
 TR"a[162:163],v4 offset:43008\n" MF"v[228:231],a[228:231],v[40:43],v[228:231]\n" TR"a[164:165],v5 offset:40960\n" TR"a[166:167],v5 offset:43008\n" SA"m0,s61,65536\n" SA"s59,s41,0\n"
 BL"v0,s[76:79],s59 offen offset:0 sc1 lds\n" MF"v[232:235],a[232:235],v[40:43],v[232:235]\n" TR"a[168:169],v6 offset:40960\n" TR"a[170:171],v6 offset:43008\n"
 MF"v[236:239],a[236:239],v[40:43],v[236:239]\n" TR"a[172:173],v7 offset:40960\n" TR"a[174:175],v7 offset:43008\n" BL"v0,s[76:79],s59 offen offset:1024 sc1 lds\n"
 MF"v[224:227],a[240:243],v[44:47],v[224:227]\n" WL"(8)\n" TR"a[176:177],v4 offset:45056\n" TR"a[178:179],v4 offset:47104\n" MF"v[228:231],a[244:247],v[44:47],v[228:231]\n"
 TR"a[180:181],v5 offset:45056\n" TR"a[182:183],v5 offset:47104\n" SA"m0,s61,67584\n" SA"s59,s43,0\n" BL"v0,s[76:79],s59 offen offset:0 sc1 lds\n" MF"v[232:235],a[248:251],v[44:47],v[232:235]\n"
 TR"a[184:185],v6 offset:45056\n" TR"a[186:187],v6 offset:47104\n" MF"v[236:239],a[252:255],v[44:47],v[236:239]\n" TR"a[188:189],v7 offset:45056\n" WL"(8)\n" TR"a[190:191],v7 offset:47104\n"
 BL"v0,s[76:79],s59 offen offset:1024 sc1 lds\n" WV"(9)\n""s_barrier\n" MF"v[224:227],a[128:131],v[48:51],v[224:227]\n" TR"a[192:193],v4 offset:49152\n" TR"a[194:195],v4 offset:51200\n"
 MF"v[228:231],a[132:135],v[48:51],v[228:231]\n" TR"a[196:197],v5 offset:49152\n" TR"a[198:199],v5 offset:51200\n" SA"m0,s63,98304\n" SA"s58,s40,65536\n" BL"v2,s[72:75],s58 offen offset:0 lds\n"
 MF"v[232:235],a[136:139],v[48:51],v[232:235]\n" TR"a[200:201],v6 offset:49152\n" TR"a[202:203],v6 offset:51200\n" MF"v[236:239],a[140:143],v[48:51],v[236:239]\n" WL"(8)\n"
 TR"a[204:205],v7 offset:49152\n" TR"a[206:207],v7 offset:51200\n" BL"v3,s[72:75],s58 offen offset:1024 lds\n" MF"v[224:227],a[144:147],v[52:55],v[224:227]\n" TR"a[208:209],v4 offset:53248\n"
 TR"a[210:211],v4 offset:55296\n" MF"v[228:231],a[148:151],v[52:55],v[228:231]\n" TR"a[212:213],v5 offset:53248\n" TR"a[214:215],v5 offset:55296\n" BL"v136,s[72:75],s58 offen offset:2048 lds\n"
 MF"v[232:235],a[152:155],v[52:55],v[232:235]\n" TR"a[216:217],v6 offset:53248\n" WL"(8)\n" TR"a[218:219],v6 offset:55296\n" MF"v[236:239],a[156:159],v[52:55],v[236:239]\n"
 TR"a[220:221],v7 offset:53248\n" TR"a[222:223],v7 offset:55296\n" BL"v137,s[72:75],s58 offen offset:3072 lds\n" MF"v[224:227],a[160:163],v[56:59],v[224:227]\n" TR"a[224:225],v4 offset:57344\n"
 TR"a[226:227],v4 offset:59392\n" MF"v[228:231],a[164:167],v[56:59],v[228:231]\n" TR"a[228:229],v5 offset:57344\n" TR"a[230:231],v5 offset:59392\n" SA"m0,s61,98304\n" SA"s59,s41,2048\n"
 BL"v0,s[76:79],s59 offen offset:0 sc1 lds\n" MF"v[232:235],a[168:171],v[56:59],v[232:235]\n" WL"(8)\n" TR"a[232:233],v6 offset:57344\n" TR"a[234:235],v6 offset:59392\n"
 MF"v[236:239],a[172:175],v[56:59],v[236:239]\n" TR"a[236:237],v7 offset:57344\n" TR"a[238:239],v7 offset:59392\n" BL"v0,s[76:79],s59 offen offset:1024 sc1 lds\n"
 MF"v[224:227],a[176:179],v[60:63],v[224:227]\n" TR"a[240:241],v4 offset:61440\n" TR"a[242:243],v4 offset:63488\n" MF"v[228:231],a[180:183],v[60:63],v[228:231]\n" TR"a[244:245],v5 offset:61440\n"
 WL"(8)\n" TR"a[246:247],v5 offset:63488\n" SA"m0,s61,100352\n" SA"s59,s43,2048\n" BL"v0,s[76:79],s59 offen offset:0 sc1 lds\n" MF"v[232:235],a[184:187],v[60:63],v[232:235]\n"
 TR"a[248:249],v6 offset:61440\n" TR"a[250:251],v6 offset:63488\n" MF"v[236:239],a[188:191],v[60:63],v[236:239]\n" TR"a[252:253],v7 offset:61440\n" TR"a[254:255],v7 offset:63488\n"
 BL"v0,s[76:79],s59 offen offset:1024 sc1 lds\n" MF"v[224:227],a[192:195],v[64:67],v[224:227]\n" MF"v[228:231],a[196:199],v[64:67],v[228:231]\n" MF"v[232:235],a[200:203],v[64:67],v[232:235]\n"
 MF"v[236:239],a[204:207],v[64:67],v[236:239]\n" MF"v[224:227],a[208:211],v[68:71],v[224:227]\n" MF"v[228:231],a[212:215],v[68:71],v[228:231]\n" MF"v[232:235],a[216:219],v[68:71],v[232:235]\n"
 MF"v[236:239],a[220:223],v[68:71],v[236:239]\n" MF"v[224:227],a[224:227],v[72:75],v[224:227]\n" WL"(12)\n" MF"v[228:231],a[228:231],v[72:75],v[228:231]\n" WL"(10)\n"
 MF"v[232:235],a[232:235],v[72:75],v[232:235]\n" WL"(8)\n" MF"v[236:239],a[236:239],v[72:75],v[236:239]\n" WL"(6)\n" MF"v[224:227],a[240:243],v[76:79],v[224:227]\n" WL"(4)\n"
 MF"v[228:231],a[244:247],v[76:79],v[228:231]\n" WL"(2)\n" MF"v[232:235],a[248:251],v[76:79],v[232:235]\n" WL"(0)\n" MF"v[236:239],a[252:255],v[76:79],v[236:239]\n" WL"(0)\n""s_barrier\n"
 SA"m0,s63,0\n" SA"s58,s40,131072\n" BL"v2,s[72:75],s58 offen offset:0 lds\n" BL"v3,s[72:75],s58 offen offset:1024 lds\n" BL"v136,s[72:75],s58 offen offset:2048 lds\n"
 BL"v137,s[72:75],s58 offen offset:3072 lds\n" SA"m0,s61,0\n" SA"s59,s41,4096\n" BL"v0,s[76:79],s59 offen offset:0 sc1 lds\n" BL"v0,s[76:79],s59 offen offset:1024 sc1 lds\n" SA"m0,s61,2048\n"
 SA"s59,s43,4096\n" BL"v0,s[76:79],s59 offen offset:0 sc1 lds\n" BL"v0,s[76:79],s59 offen offset:1024 sc1 lds\n""v_mbcnt_lo_u32_b32 v24,-1,0\n""v_mbcnt_hi_u32_b32 v24,-1,v24\n"
"v_lshrrev_b32 v25,4,v24\n""v_bfe_u32 v26,v24,2,2\n""v_lshl_add_u32 v25,v25,2,v26\n""v_lshlrev_b32 v26,8,v25\n""v_and_b32 v27,3,v24\n""v_lshl_add_u32 v26,v27,3,v26\n""v_add_u32 v26,16384,v26\n"
"v_and_b32 v25,7,v25\n""s_and_b32 s57,s36,1\n""s_lshl_b32 s57,s57,2\n" SA"s58,s57,0\n""v_xor_b32 v27,s58,v25\n""v_lshl_add_u32 v4,v27,5,v26\n""v_add_u32 v10,0x10000,v4\n" SA"s58,s57,1\n"
"v_xor_b32 v27,s58,v25\n""v_lshl_add_u32 v5,v27,5,v26\n""v_add_u32 v11,0x10000,v5\n" SA"s58,s57,2\n""v_xor_b32 v27,s58,v25\n""v_lshl_add_u32 v6,v27,5,v26\n""v_add_u32 v12,0x10000,v6\n" SA"s58,s57,3\n"
"v_xor_b32 v27,s58,v25\n""v_lshl_add_u32 v7,v27,5,v26\n""v_add_u32 v13,0x10000,v7\n""s_lshr_b32 s57,s36,1\n""s_lshl_b32 s57,s57,13\n""v_add_u32 v8,s57,v0\n""v_add_u32 v9,0x10000,v8\n" WV"(45)\n"
 WV"(16)\n""s_barrier\n" DR"v[64:67],v9 offset:0\n" DR"v[68:71],v9 offset:2048\n" DR"v[72:75],v9 offset:4096\n" DR"v[76:79],v9 offset:6144\n" TR"v[32:33],v10 offset:0\n" TR"v[34:35],v10 offset:4096\n"
 TR"v[36:37],v11 offset:0\n" TR"v[38:39],v11 offset:4096\n" TR"v[40:41],v12 offset:0\n" TR"v[42:43],v12 offset:4096\n" TR"v[44:45],v13 offset:0\n" TR"v[46:47],v13 offset:4096\n" WL"(6)\n"
 MF"a[0:3],v[32:35],v[64:67],0\n""v_add_f32 v138,v240,v242\n" VV"v16,v138\n" MF"a[4:7],v[32:35],v[68:71],0\n" DR"v[80:83],v9 offset:1024\n""s_nop 1\n""v_permlane16_swap_b32 v138,v16\n"
 MF"a[8:11],v[32:35],v[72:75],0\n" DR"v[84:87],v9 offset:3072\n""s_nop 1\n""v_add_f32 v138,v138,v16\n" MF"a[12:15],v[32:35],v[76:79],0\n" DR"v[88:91],v9 offset:5120\n" VV"v16,v138\n""s_nop 1\n"
 WL"(7)\n" MF"a[16:19],v[36:39],v[64:67],0\n" DR"v[92:95],v9 offset:7168\n""v_permlane32_swap_b32 v138,v16\n""s_nop 1\n" MF"a[20:23],v[36:39],v[68:71],0\n" SA"m0,s63,32768\n" SA"s58,s40,196608\n"
 BL"v2,s[72:75],s58 offen offset:0 lds\n""v_add_f32 v138,v138,v16\n" VM"v138,0x3e000000,v138\n" MF"a[24:27],v[36:39],v[72:75],0\n" TR"v[48:49],v10 offset:8192\n" TR"v[50:51],v10 offset:12288\n"
"v_sub_f32 v139,v240,v138\n""v_sub_f32 v140,v242,v138\n" MF"a[28:31],v[36:39],v[76:79],0\n" TR"v[52:53],v11 offset:8192\n" TR"v[54:55],v11 offset:12288\n" VM"v139,v139,v139\n"
"v_fmac_f32 v139,v140,v140\n" WL"(10)\n" MF"a[32:35],v[40:43],v[64:67],0\n" TR"v[56:57],v12 offset:8192\n" TR"v[58:59],v12 offset:12288\n""v_add_f32 v140,v241,v243\n"
"v_fmac_f32 v140,0x42800000,v139\n" MF"a[36:39],v[40:43],v[68:71],0\n" TR"v[60:61],v13 offset:8192\n" TR"v[62:63],v13 offset:12288\n" VV"v16,v140\n""s_nop 1\n" MF"a[40:43],v[40:43],v[72:75],0\n"
 BL"v3,s[72:75],s58 offen offset:1024 lds\n""v_permlane16_swap_b32 v140,v16\n""s_nop 1\n" MF"a[44:47],v[40:43],v[76:79],0\n""v_add_f32 v140,v140,v16\n" VV"v16,v140\n" WL"(12)\n"
 MF"a[48:51],v[44:47],v[64:67],0\n""s_nop 1\n""v_permlane32_swap_b32 v140,v16\n" MF"a[52:55],v[44:47],v[68:71],0\n" BL"v136,s[72:75],s58 offen offset:2048 lds\n""s_nop 1\n""v_add_f32 v140,v140,v16\n"
 MF"a[56:59],v[44:47],v[72:75],0\n" VV"v141,0x3727c5ac\n""v_fmac_f32 v141,0x3b000000,v140\n" MF"a[60:63],v[44:47],v[76:79],0\n" BL"v137,s[72:75],s58 offen offset:3072 lds\n""v_rsq_f32 v141,v141\n"
"s_nop 0\n" WL"(6)\n" MF"a[0:3],v[48:51],v[80:83],a[0:3]\n" WV"(12) lgkmcnt(0)\n""s_barrier\n" VM"v142,v138,v141\n""v_sub_f32 v142,0,v142\n" MF"a[4:7],v[48:51],v[84:87],a[4:7]\n"
 DR"v[64:67],v9 offset:32768\n" SA"s88,s52,14680064\n""s_addc_u32 s89,s53,0\n" MF"a[8:11],v[48:51],v[88:91],a[8:11]\n" DR"v[68:71],v9 offset:34816\n""s_mov_b32 s90,5636096\n""s_mov_b32 s91,0x20000\n"
 MF"a[12:15],v[48:51],v[92:95],a[12:15]\n" DR"v[72:75],v9 offset:36864\n" VM"v192,v192,v141\n""v_fmac_f32 v192,v160,v142\n" MF"a[16:19],v[52:55],v[80:83],a[16:19]\n" DR"v[76:79],v9 offset:38912\n"
 VM"v193,v193,v141\n""v_fmac_f32 v193,v161,v142\n" MF"a[20:23],v[52:55],v[84:87],a[20:23]\n" SA"m0,s61,32768\n" SA"s59,s41,6144\n" BL"v0,s[76:79],s59 offen offset:0 sc1 lds\n" VM"v194,v194,v141\n"
"v_fmac_f32 v194,v162,v142\n" MF"a[24:27],v[52:55],v[88:91],a[24:27]\n" TR"v[32:33],v10 offset:32768\n" TR"v[34:35],v10 offset:36864\n" VM"v195,v195,v141\n""v_fmac_f32 v195,v163,v142\n"
 MF"a[28:31],v[52:55],v[92:95],a[28:31]\n" TR"v[36:37],v11 offset:32768\n" TR"v[38:39],v11 offset:36864\n" VM"v196,v196,v141\n""v_fmac_f32 v196,v164,v142\n" MF"a[32:35],v[56:59],v[80:83],a[32:35]\n"
 TR"v[40:41],v12 offset:32768\n" TR"v[42:43],v12 offset:36864\n" VM"v197,v197,v141\n""v_fmac_f32 v197,v165,v142\n" MF"a[36:39],v[56:59],v[84:87],a[36:39]\n" TR"v[44:45],v13 offset:32768\n"
 TR"v[46:47],v13 offset:36864\n" VM"v198,v198,v141\n""v_fmac_f32 v198,v166,v142\n" MF"a[40:43],v[56:59],v[88:91],a[40:43]\n" BL"v0,s[76:79],s59 offen offset:1024 sc1 lds\n" VM"v199,v199,v141\n"
"v_fmac_f32 v199,v167,v142\n" MF"a[44:47],v[56:59],v[92:95],a[44:47]\n" VM"v200,v200,v141\n""v_fmac_f32 v200,v168,v142\n" MF"a[48:51],v[60:63],v[80:83],a[48:51]\n" VM"v201,v201,v141\n"
"v_fmac_f32 v201,v169,v142\n" MF"a[52:55],v[60:63],v[84:87],a[52:55]\n" SA"m0,s61,34816\n" SA"s59,s43,6144\n" BL"v0,s[76:79],s59 offen offset:0 sc1 lds\n" VM"v202,v202,v141\n"
"v_fmac_f32 v202,v170,v142\n" MF"a[56:59],v[60:63],v[88:91],a[56:59]\n" VM"v203,v203,v141\n""v_fmac_f32 v203,v171,v142\n" MF"a[60:63],v[60:63],v[92:95],a[60:63]\n"
 BL"v0,s[76:79],s59 offen offset:1024 sc1 lds\n" VM"v204,v204,v141\n""v_fmac_f32 v204,v172,v142\n" WL"(6)\n" MF"a[0:3],v[32:35],v[64:67],a[0:3]\n" VM"v205,v205,v141\n""v_fmac_f32 v205,v173,v142\n"
 MF"a[4:7],v[32:35],v[68:71],a[4:7]\n" DR"v[80:83],v9 offset:33792\n" VM"v206,v206,v141\n""v_fmac_f32 v206,v174,v142\n" MF"a[8:11],v[32:35],v[72:75],a[8:11]\n" DR"v[84:87],v9 offset:35840\n"
 VM"v207,v207,v141\n""v_fmac_f32 v207,v175,v142\n" MF"a[12:15],v[32:35],v[76:79],a[12:15]\n" DR"v[88:91],v9 offset:37888\n""s_mov_b32 s60,0xbdd2d3e8\n" VV"v18,0xc0135761\n" WL"(7)\n"
 MF"a[16:19],v[36:39],v[64:67],a[16:19]\n" DR"v[92:95],v9 offset:39936\n" VM"v96,v192,v192\n" VM"v97,v193,v193\n" MF"a[20:23],v[36:39],v[68:71],a[20:23]\n" SA"m0,s63,65536\n" SA"s58,s40,262144\n"
 BL"v2,s[72:75],s58 offen offset:0 lds\n" VM"v98,v194,v194\n" VM"v99,v195,v195\n" MF"a[24:27],v[36:39],v[72:75],a[24:27]\n" TR"v[48:49],v10 offset:40960\n" TR"v[50:51],v10 offset:45056\n"
 VM"v100,v196,v196\n" VM"v101,v197,v197\n" MF"a[28:31],v[36:39],v[76:79],a[28:31]\n" TR"v[52:53],v11 offset:40960\n" TR"v[54:55],v11 offset:45056\n" VM"v102,v198,v198\n" VM"v103,v199,v199\n"
 WL"(10)\n" MF"a[32:35],v[40:43],v[64:67],a[32:35]\n" TR"v[56:57],v12 offset:40960\n" TR"v[58:59],v12 offset:45056\n" VM"v104,v200,v200\n" VM"v105,v201,v201\n"
 MF"a[36:39],v[40:43],v[68:71],a[36:39]\n" TR"v[60:61],v13 offset:40960\n" TR"v[62:63],v13 offset:45056\n" VM"v106,v202,v202\n" VM"v107,v203,v203\n" MF"a[40:43],v[40:43],v[72:75],a[40:43]\n"
 BL"v3,s[72:75],s58 offen offset:1024 lds\n" VM"v108,v204,v204\n" VM"v109,v205,v205\n" MF"a[44:47],v[40:43],v[76:79],a[44:47]\n" VM"v110,v206,v206\n" VM"v111,v207,v207\n" WL"(12)\n"
 MF"a[48:51],v[44:47],v[64:67],a[48:51]\n""v_fma_f32 v96,v96,s60,v18\n""v_fma_f32 v97,v97,s60,v18\n" MF"a[52:55],v[44:47],v[68:71],a[52:55]\n" BL"v136,s[72:75],s58 offen offset:2048 lds\n"
"v_fma_f32 v98,v98,s60,v18\n""v_fma_f32 v99,v99,s60,v18\n" MF"a[56:59],v[44:47],v[72:75],a[56:59]\n""v_fma_f32 v100,v100,s60,v18\n""v_fma_f32 v101,v101,s60,v18\n"
 MF"a[60:63],v[44:47],v[76:79],a[60:63]\n" BL"v137,s[72:75],s58 offen offset:3072 lds\n""v_fma_f32 v102,v102,s60,v18\n""v_fma_f32 v103,v103,s60,v18\n" WL"(6)\n" MF"a[0:3],v[48:51],v[80:83],a[0:3]\n"
 WV"(12) lgkmcnt(0)\n""s_barrier\n""v_fma_f32 v104,v104,s60,v18\n""v_fma_f32 v105,v105,s60,v18\n" MF"a[4:7],v[48:51],v[84:87],a[4:7]\n" DR"v[64:67],v8 offset:0\n""v_fma_f32 v106,v106,s60,v18\n"
"v_fma_f32 v107,v107,s60,v18\n" MF"a[8:11],v[48:51],v[88:91],a[8:11]\n" DR"v[68:71],v8 offset:2048\n""v_fma_f32 v108,v108,s60,v18\n""v_fma_f32 v109,v109,s60,v18\n"
 MF"a[12:15],v[48:51],v[92:95],a[12:15]\n" DR"v[72:75],v8 offset:4096\n""v_fma_f32 v110,v110,s60,v18\n""v_fma_f32 v111,v111,s60,v18\n" MF"a[16:19],v[52:55],v[80:83],a[16:19]\n"
 DR"v[76:79],v8 offset:6144\n" VM"v96,v96,v192\n" VM"v97,v97,v193\n" MF"a[20:23],v[52:55],v[84:87],a[20:23]\n" SA"m0,s61,65536\n" SA"s59,s41,8192\n" BL"v0,s[76:79],s59 offen offset:0 sc1 lds\n"
 VM"v98,v98,v194\n" VM"v99,v99,v195\n" MF"a[24:27],v[52:55],v[88:91],a[24:27]\n" TR"v[32:33],v4 offset:0\n" TR"v[34:35],v4 offset:4096\n" VM"v100,v100,v196\n" VM"v101,v101,v197\n"
 MF"a[28:31],v[52:55],v[92:95],a[28:31]\n" TR"v[36:37],v5 offset:0\n" TR"v[38:39],v5 offset:4096\n" VM"v102,v102,v198\n" VM"v103,v103,v199\n" MF"a[32:35],v[56:59],v[80:83],a[32:35]\n"
 TR"v[40:41],v6 offset:0\n" TR"v[42:43],v6 offset:4096\n" VM"v104,v104,v200\n" VM"v105,v105,v201\n" MF"a[36:39],v[56:59],v[84:87],a[36:39]\n" TR"v[44:45],v7 offset:0\n" TR"v[46:47],v7 offset:4096\n"
 VM"v106,v106,v202\n" VM"v107,v107,v203\n" MF"a[40:43],v[56:59],v[88:91],a[40:43]\n" BL"v0,s[76:79],s59 offen offset:1024 sc1 lds\n" VM"v108,v108,v204\n" VM"v109,v109,v205\n"
 MF"a[44:47],v[56:59],v[92:95],a[44:47]\n" VM"v110,v110,v206\n" VM"v111,v111,v207\n" MF"a[48:51],v[60:63],v[80:83],a[48:51]\n" EX"v96,v96\n" EX"v97,v97\n" MF"a[52:55],v[60:63],v[84:87],a[52:55]\n"
 SA"m0,s61,67584\n" SA"s59,s43,8192\n" BL"v0,s[76:79],s59 offen offset:0 sc1 lds\n" EX"v98,v98\n" EX"v99,v99\n" MF"a[56:59],v[60:63],v[88:91],a[56:59]\n" EX"v100,v100\n" EX"v101,v101\n"
 MF"a[60:63],v[60:63],v[92:95],a[60:63]\n" BL"v0,s[76:79],s59 offen offset:1024 sc1 lds\n" EX"v102,v102\n" EX"v103,v103\n" WL"(6)\n" MF"a[0:3],v[32:35],v[64:67],a[0:3]\n" EX"v104,v104\n"
 EX"v105,v105\n" MF"a[4:7],v[32:35],v[68:71],a[4:7]\n" DR"v[80:83],v8 offset:1024\n" EX"v106,v106\n" EX"v107,v107\n" MF"a[8:11],v[32:35],v[72:75],a[8:11]\n" DR"v[84:87],v8 offset:3072\n"
 EX"v108,v108\n" EX"v109,v109\n" MF"a[12:15],v[32:35],v[76:79],a[12:15]\n" DR"v[88:91],v8 offset:5120\n" EX"v110,v110\n" EX"v111,v111\n" WL"(7)\n" MF"a[16:19],v[36:39],v[64:67],a[16:19]\n"
 DR"v[92:95],v8 offset:7168\n""v_add_f32 v96,1.0,v96\n""v_add_f32 v97,1.0,v97\n" MF"a[20:23],v[36:39],v[68:71],a[20:23]\n" SA"m0,s63,98304\n" SA"s58,s40,327680\n"
 BL"v2,s[72:75],s58 offen offset:0 lds\n""v_add_f32 v98,1.0,v98\n""v_add_f32 v99,1.0,v99\n" MF"a[24:27],v[36:39],v[72:75],a[24:27]\n" TR"v[48:49],v4 offset:8192\n" TR"v[50:51],v4 offset:12288\n"
"v_add_f32 v100,1.0,v100\n""v_add_f32 v101,1.0,v101\n" MF"a[28:31],v[36:39],v[76:79],a[28:31]\n" TR"v[52:53],v5 offset:8192\n" TR"v[54:55],v5 offset:12288\n""v_add_f32 v102,1.0,v102\n"
"v_add_f32 v103,1.0,v103\n" WL"(10)\n" MF"a[32:35],v[40:43],v[64:67],a[32:35]\n" TR"v[56:57],v6 offset:8192\n" TR"v[58:59],v6 offset:12288\n""v_add_f32 v104,1.0,v104\n""v_add_f32 v105,1.0,v105\n"
 MF"a[36:39],v[40:43],v[68:71],a[36:39]\n" TR"v[60:61],v7 offset:8192\n" TR"v[62:63],v7 offset:12288\n""v_add_f32 v106,1.0,v106\n""v_add_f32 v107,1.0,v107\n" MF"a[40:43],v[40:43],v[72:75],a[40:43]\n"
 BL"v3,s[72:75],s58 offen offset:1024 lds\n""v_add_f32 v108,1.0,v108\n""v_add_f32 v109,1.0,v109\n" MF"a[44:47],v[40:43],v[76:79],a[44:47]\n""v_add_f32 v110,1.0,v110\n""v_add_f32 v111,1.0,v111\n"
 WL"(12)\n" MF"a[48:51],v[44:47],v[64:67],a[48:51]\n""v_rcp_f32 v96,v96\n""v_rcp_f32 v97,v97\n" MF"a[52:55],v[44:47],v[68:71],a[52:55]\n" BL"v136,s[72:75],s58 offen offset:2048 lds\n"
"v_rcp_f32 v98,v98\n""v_rcp_f32 v99,v99\n" MF"a[56:59],v[44:47],v[72:75],a[56:59]\n""v_rcp_f32 v100,v100\n""v_rcp_f32 v101,v101\n" MF"a[60:63],v[44:47],v[76:79],a[60:63]\n"
 BL"v137,s[72:75],s58 offen offset:3072 lds\n""v_rcp_f32 v102,v102\n""v_rcp_f32 v103,v103\n" WL"(6)\n" MF"a[0:3],v[48:51],v[80:83],a[0:3]\n" WV"(12) lgkmcnt(0)\n""s_barrier\n""v_rcp_f32 v104,v104\n"
"v_rcp_f32 v105,v105\n" MF"a[4:7],v[48:51],v[84:87],a[4:7]\n" DR"v[64:67],v8 offset:32768\n""v_rcp_f32 v106,v106\n""v_rcp_f32 v107,v107\n" MF"a[8:11],v[48:51],v[88:91],a[8:11]\n"
 DR"v[68:71],v8 offset:34816\n""v_rcp_f32 v108,v108\n""v_rcp_f32 v109,v109\n" MF"a[12:15],v[48:51],v[92:95],a[12:15]\n" DR"v[72:75],v8 offset:36864\n""v_rcp_f32 v110,v110\n""v_rcp_f32 v111,v111\n"
 MF"a[16:19],v[52:55],v[80:83],a[16:19]\n" DR"v[76:79],v8 offset:38912\n" VM"v192,v192,v96\n" VM"v193,v193,v97\n" MF"a[20:23],v[52:55],v[84:87],a[20:23]\n" SA"m0,s61,98304\n" SA"s59,s41,10240\n"
 BL"v0,s[76:79],s59 offen offset:0 sc1 lds\n" VM"v194,v194,v98\n" VM"v195,v195,v99\n" MF"a[24:27],v[52:55],v[88:91],a[24:27]\n" TR"v[32:33],v4 offset:32768\n" TR"v[34:35],v4 offset:36864\n"
 VM"v196,v196,v100\n" VM"v197,v197,v101\n" MF"a[28:31],v[52:55],v[92:95],a[28:31]\n" TR"v[36:37],v5 offset:32768\n" TR"v[38:39],v5 offset:36864\n" VM"v198,v198,v102\n" VM"v199,v199,v103\n"
 MF"a[32:35],v[56:59],v[80:83],a[32:35]\n" TR"v[40:41],v6 offset:32768\n" TR"v[42:43],v6 offset:36864\n" VM"v200,v200,v104\n" VM"v201,v201,v105\n" MF"a[36:39],v[56:59],v[84:87],a[36:39]\n"
 TR"v[44:45],v7 offset:32768\n" TR"v[46:47],v7 offset:36864\n" VM"v202,v202,v106\n" VM"v203,v203,v107\n" MF"a[40:43],v[56:59],v[88:91],a[40:43]\n" BL"v0,s[76:79],s59 offen offset:1024 sc1 lds\n"
 VM"v204,v204,v108\n" VM"v205,v205,v109\n" MF"a[44:47],v[56:59],v[92:95],a[44:47]\n" VM"v206,v206,v110\n" VM"v207,v207,v111\n" MF"a[48:51],v[60:63],v[80:83],a[48:51]\n" CV"v112,v192,v193\n"
 CV"v113,v194,v195\n" MF"a[52:55],v[60:63],v[84:87],a[52:55]\n" SA"m0,s61,100352\n" SA"s59,s43,10240\n" BL"v0,s[76:79],s59 offen offset:0 sc1 lds\n" CV"v114,v196,v197\n" CV"v115,v198,v199\n"
 MF"a[56:59],v[60:63],v[88:91],a[56:59]\n" CV"v116,v200,v201\n" CV"v117,v202,v203\n" MF"a[60:63],v[60:63],v[92:95],a[60:63]\n" BL"v0,s[76:79],s59 offen offset:1024 sc1 lds\n" CV"v118,v204,v205\n"
 CV"v119,v206,v207\n" WL"(6)\n" MF"a[0:3],v[32:35],v[64:67],a[0:3]\n" SA"s50,s97,0\n" BS"v[112:115],v0,s[88:91],s50 offen sc1\n" MF"a[4:7],v[32:35],v[68:71],a[4:7]\n" DR"v[80:83],v8 offset:33792\n"
 BS"v[116:119],v0,s[88:91],s50 offen offset:1024 sc1\n" VM"v208,v208,v141\n" MF"a[8:11],v[32:35],v[72:75],a[8:11]\n" DR"v[84:87],v8 offset:35840\n""v_fmac_f32 v208,v176,v142\n" VM"v209,v209,v141\n"
 MF"a[12:15],v[32:35],v[76:79],a[12:15]\n" DR"v[88:91],v8 offset:37888\n""v_fmac_f32 v209,v177,v142\n" VM"v210,v210,v141\n" WL"(7)\n" MF"a[16:19],v[36:39],v[64:67],a[16:19]\n"
 DR"v[92:95],v8 offset:39936\n""v_fmac_f32 v210,v178,v142\n" VM"v211,v211,v141\n" MF"a[20:23],v[36:39],v[68:71],a[20:23]\n" SA"m0,s63,0\n" SA"s58,s40,393216\n" BL"v2,s[72:75],s58 offen offset:0 lds\n"
"v_fmac_f32 v211,v179,v142\n" VM"v212,v212,v141\n" MF"a[24:27],v[36:39],v[72:75],a[24:27]\n" TR"v[48:49],v4 offset:40960\n" TR"v[50:51],v4 offset:45056\n""v_fmac_f32 v212,v180,v142\n"
 VM"v213,v213,v141\n" MF"a[28:31],v[36:39],v[76:79],a[28:31]\n" TR"v[52:53],v5 offset:40960\n" TR"v[54:55],v5 offset:45056\n""v_fmac_f32 v213,v181,v142\n" VM"v214,v214,v141\n" WL"(10)\n"
 MF"a[32:35],v[40:43],v[64:67],a[32:35]\n" TR"v[56:57],v6 offset:40960\n" TR"v[58:59],v6 offset:45056\n""v_fmac_f32 v214,v182,v142\n" VM"v215,v215,v141\n" MF"a[36:39],v[40:43],v[68:71],a[36:39]\n"
 TR"v[60:61],v7 offset:40960\n" TR"v[62:63],v7 offset:45056\n""v_fmac_f32 v215,v183,v142\n" VM"v216,v216,v141\n" MF"a[40:43],v[40:43],v[72:75],a[40:43]\n" BL"v3,s[72:75],s58 offen offset:1024 lds\n"
"v_fmac_f32 v216,v184,v142\n" VM"v217,v217,v141\n" MF"a[44:47],v[40:43],v[76:79],a[44:47]\n""v_fmac_f32 v217,v185,v142\n" VM"v218,v218,v141\n" WL"(12)\n" MF"a[48:51],v[44:47],v[64:67],a[48:51]\n"
"v_fmac_f32 v218,v186,v142\n" VM"v219,v219,v141\n" MF"a[52:55],v[44:47],v[68:71],a[52:55]\n" BL"v136,s[72:75],s58 offen offset:2048 lds\n""v_fmac_f32 v219,v187,v142\n" VM"v220,v220,v141\n"
 MF"a[56:59],v[44:47],v[72:75],a[56:59]\n""v_fmac_f32 v220,v188,v142\n" VM"v221,v221,v141\n" MF"a[60:63],v[44:47],v[76:79],a[60:63]\n" BL"v137,s[72:75],s58 offen offset:3072 lds\n"
"v_fmac_f32 v221,v189,v142\n" VM"v222,v222,v141\n" WL"(6)\n" MF"a[0:3],v[48:51],v[80:83],a[0:3]\n" WV"(14) lgkmcnt(0)\n""s_barrier\n""v_fmac_f32 v222,v190,v142\n" VM"v223,v223,v141\n"
 MF"a[4:7],v[48:51],v[84:87],a[4:7]\n" DR"v[64:67],v9 offset:0\n""v_fmac_f32 v223,v191,v142\n""s_mov_b32 s60,0xbdd2d3e8\n" MF"a[8:11],v[48:51],v[88:91],a[8:11]\n" DR"v[68:71],v9 offset:2048\n"
 VV"v18,0xc0135761\n" VM"v96,v208,v208\n" MF"a[12:15],v[48:51],v[92:95],a[12:15]\n" DR"v[72:75],v9 offset:4096\n" VM"v97,v209,v209\n" VM"v98,v210,v210\n" MF"a[16:19],v[52:55],v[80:83],a[16:19]\n"
 DR"v[76:79],v9 offset:6144\n" VM"v99,v211,v211\n" VM"v100,v212,v212\n" MF"a[20:23],v[52:55],v[84:87],a[20:23]\n" SA"m0,s61,0\n" SA"s59,s41,12288\n" BL"v0,s[76:79],s59 offen offset:0 sc1 lds\n"
 VM"v101,v213,v213\n" VM"v102,v214,v214\n" MF"a[24:27],v[52:55],v[88:91],a[24:27]\n" TR"v[32:33],v10 offset:0\n" TR"v[34:35],v10 offset:4096\n" VM"v103,v215,v215\n" VM"v104,v216,v216\n"
 MF"a[28:31],v[52:55],v[92:95],a[28:31]\n" TR"v[36:37],v11 offset:0\n" TR"v[38:39],v11 offset:4096\n" VM"v105,v217,v217\n" VM"v106,v218,v218\n" MF"a[32:35],v[56:59],v[80:83],a[32:35]\n"
 TR"v[40:41],v12 offset:0\n" TR"v[42:43],v12 offset:4096\n" VM"v107,v219,v219\n" VM"v108,v220,v220\n" MF"a[36:39],v[56:59],v[84:87],a[36:39]\n" TR"v[44:45],v13 offset:0\n"
 TR"v[46:47],v13 offset:4096\n" VM"v109,v221,v221\n" VM"v110,v222,v222\n" MF"a[40:43],v[56:59],v[88:91],a[40:43]\n" BL"v0,s[76:79],s59 offen offset:1024 sc1 lds\n" VM"v111,v223,v223\n"
"v_fma_f32 v96,v96,s60,v18\n" MF"a[44:47],v[56:59],v[92:95],a[44:47]\n""v_fma_f32 v97,v97,s60,v18\n""v_fma_f32 v98,v98,s60,v18\n" MF"a[48:51],v[60:63],v[80:83],a[48:51]\n""v_fma_f32 v99,v99,s60,v18\n"
"v_fma_f32 v100,v100,s60,v18\n" MF"a[52:55],v[60:63],v[84:87],a[52:55]\n" SA"m0,s61,2048\n" SA"s59,s43,12288\n" BL"v0,s[76:79],s59 offen offset:0 sc1 lds\n""v_fma_f32 v101,v101,s60,v18\n"
"v_fma_f32 v102,v102,s60,v18\n" MF"a[56:59],v[60:63],v[88:91],a[56:59]\n""v_fma_f32 v103,v103,s60,v18\n""v_fma_f32 v104,v104,s60,v18\n" MF"a[60:63],v[60:63],v[92:95],a[60:63]\n"
 BL"v0,s[76:79],s59 offen offset:1024 sc1 lds\n""v_fma_f32 v105,v105,s60,v18\n""v_fma_f32 v106,v106,s60,v18\n" WL"(6)\n" MF"a[0:3],v[32:35],v[64:67],a[0:3]\n""v_fma_f32 v107,v107,s60,v18\n"
"v_fma_f32 v108,v108,s60,v18\n" MF"a[4:7],v[32:35],v[68:71],a[4:7]\n" DR"v[80:83],v9 offset:1024\n""v_fma_f32 v109,v109,s60,v18\n""v_fma_f32 v110,v110,s60,v18\n"
 MF"a[8:11],v[32:35],v[72:75],a[8:11]\n" DR"v[84:87],v9 offset:3072\n""v_fma_f32 v111,v111,s60,v18\n" VM"v96,v96,v208\n" MF"a[12:15],v[32:35],v[76:79],a[12:15]\n" DR"v[88:91],v9 offset:5120\n"
 VM"v97,v97,v209\n" VM"v98,v98,v210\n" WL"(7)\n" MF"a[16:19],v[36:39],v[64:67],a[16:19]\n" DR"v[92:95],v9 offset:7168\n" VM"v99,v99,v211\n" VM"v100,v100,v212\n"
 MF"a[20:23],v[36:39],v[68:71],a[20:23]\n" SA"m0,s63,32768\n" SA"s58,s40,458752\n" BL"v2,s[72:75],s58 offen offset:0 lds\n" VM"v101,v101,v213\n" VM"v102,v102,v214\n"
 MF"a[24:27],v[36:39],v[72:75],a[24:27]\n" TR"v[48:49],v10 offset:8192\n" TR"v[50:51],v10 offset:12288\n" VM"v103,v103,v215\n" VM"v104,v104,v216\n" MF"a[28:31],v[36:39],v[76:79],a[28:31]\n"
 TR"v[52:53],v11 offset:8192\n" TR"v[54:55],v11 offset:12288\n" VM"v105,v105,v217\n" VM"v106,v106,v218\n" WL"(10)\n" MF"a[32:35],v[40:43],v[64:67],a[32:35]\n" TR"v[56:57],v12 offset:8192\n"
 TR"v[58:59],v12 offset:12288\n" VM"v107,v107,v219\n" VM"v108,v108,v220\n" MF"a[36:39],v[40:43],v[68:71],a[36:39]\n" TR"v[60:61],v13 offset:8192\n" TR"v[62:63],v13 offset:12288\n" VM"v109,v109,v221\n"
 VM"v110,v110,v222\n" MF"a[40:43],v[40:43],v[72:75],a[40:43]\n" BL"v3,s[72:75],s58 offen offset:1024 lds\n" VM"v111,v111,v223\n" EX"v96,v96\n" MF"a[44:47],v[40:43],v[76:79],a[44:47]\n" EX"v97,v97\n"
 EX"v98,v98\n" WL"(12)\n" MF"a[48:51],v[44:47],v[64:67],a[48:51]\n" EX"v99,v99\n" EX"v100,v100\n" MF"a[52:55],v[44:47],v[68:71],a[52:55]\n" BL"v136,s[72:75],s58 offen offset:2048 lds\n"
 EX"v101,v101\n" EX"v102,v102\n" MF"a[56:59],v[44:47],v[72:75],a[56:59]\n" EX"v103,v103\n" EX"v104,v104\n" MF"a[60:63],v[44:47],v[76:79],a[60:63]\n" BL"v137,s[72:75],s58 offen offset:3072 lds\n"
 EX"v105,v105\n" EX"v106,v106\n" WL"(6)\n" MF"a[0:3],v[48:51],v[80:83],a[0:3]\n" WV"(14) lgkmcnt(0)\n""s_barrier\n" EX"v107,v107\n" EX"v108,v108\n" MF"a[4:7],v[48:51],v[84:87],a[4:7]\n"
 DR"v[64:67],v9 offset:32768\n" EX"v109,v109\n" EX"v110,v110\n" MF"a[8:11],v[48:51],v[88:91],a[8:11]\n" DR"v[68:71],v9 offset:34816\n" EX"v111,v111\n""v_add_f32 v96,1.0,v96\n"
 MF"a[12:15],v[48:51],v[92:95],a[12:15]\n" DR"v[72:75],v9 offset:36864\n""v_add_f32 v97,1.0,v97\n""v_add_f32 v98,1.0,v98\n" MF"a[16:19],v[52:55],v[80:83],a[16:19]\n" DR"v[76:79],v9 offset:38912\n"
"v_add_f32 v99,1.0,v99\n""v_add_f32 v100,1.0,v100\n" MF"a[20:23],v[52:55],v[84:87],a[20:23]\n" SA"m0,s61,32768\n" SA"s59,s41,14336\n" BL"v0,s[76:79],s59 offen offset:0 sc1 lds\n"
"v_add_f32 v101,1.0,v101\n""v_add_f32 v102,1.0,v102\n" MF"a[24:27],v[52:55],v[88:91],a[24:27]\n" TR"v[32:33],v10 offset:32768\n" TR"v[34:35],v10 offset:36864\n""v_add_f32 v103,1.0,v103\n"
"v_add_f32 v104,1.0,v104\n" MF"a[28:31],v[52:55],v[92:95],a[28:31]\n" TR"v[36:37],v11 offset:32768\n" TR"v[38:39],v11 offset:36864\n""v_add_f32 v105,1.0,v105\n""v_add_f32 v106,1.0,v106\n"
 MF"a[32:35],v[56:59],v[80:83],a[32:35]\n" TR"v[40:41],v12 offset:32768\n" TR"v[42:43],v12 offset:36864\n""v_add_f32 v107,1.0,v107\n""v_add_f32 v108,1.0,v108\n"
 MF"a[36:39],v[56:59],v[84:87],a[36:39]\n" TR"v[44:45],v13 offset:32768\n" TR"v[46:47],v13 offset:36864\n""v_add_f32 v109,1.0,v109\n""v_add_f32 v110,1.0,v110\n"
 MF"a[40:43],v[56:59],v[88:91],a[40:43]\n" BL"v0,s[76:79],s59 offen offset:1024 sc1 lds\n""v_add_f32 v111,1.0,v111\n""v_rcp_f32 v96,v96\n" MF"a[44:47],v[56:59],v[92:95],a[44:47]\n"
"v_rcp_f32 v97,v97\n""v_rcp_f32 v98,v98\n" MF"a[48:51],v[60:63],v[80:83],a[48:51]\n""v_rcp_f32 v99,v99\n""v_rcp_f32 v100,v100\n" MF"a[52:55],v[60:63],v[84:87],a[52:55]\n" SA"m0,s61,34816\n"
 SA"s59,s43,14336\n" BL"v0,s[76:79],s59 offen offset:0 sc1 lds\n""v_rcp_f32 v101,v101\n""v_rcp_f32 v102,v102\n" MF"a[56:59],v[60:63],v[88:91],a[56:59]\n""v_rcp_f32 v103,v103\n""v_rcp_f32 v104,v104\n"
 MF"a[60:63],v[60:63],v[92:95],a[60:63]\n" BL"v0,s[76:79],s59 offen offset:1024 sc1 lds\n""v_rcp_f32 v105,v105\n""v_rcp_f32 v106,v106\n" WL"(6)\n" MF"a[0:3],v[32:35],v[64:67],a[0:3]\n"
"v_rcp_f32 v107,v107\n""v_rcp_f32 v108,v108\n" MF"a[4:7],v[32:35],v[68:71],a[4:7]\n" DR"v[80:83],v9 offset:33792\n""v_rcp_f32 v109,v109\n""v_rcp_f32 v110,v110\n"
 MF"a[8:11],v[32:35],v[72:75],a[8:11]\n" DR"v[84:87],v9 offset:35840\n""v_rcp_f32 v111,v111\n" VM"v208,v208,v96\n" MF"a[12:15],v[32:35],v[76:79],a[12:15]\n" DR"v[88:91],v9 offset:37888\n"
 VM"v209,v209,v97\n" VM"v210,v210,v98\n" WL"(7)\n" MF"a[16:19],v[36:39],v[64:67],a[16:19]\n" DR"v[92:95],v9 offset:39936\n" VM"v211,v211,v99\n" VM"v212,v212,v100\n"
 MF"a[20:23],v[36:39],v[68:71],a[20:23]\n" SA"m0,s63,65536\n" SA"s58,s40,524288\n" BL"v2,s[72:75],s58 offen offset:0 lds\n" VM"v213,v213,v101\n" VM"v214,v214,v102\n"
 MF"a[24:27],v[36:39],v[72:75],a[24:27]\n" TR"v[48:49],v10 offset:40960\n" TR"v[50:51],v10 offset:45056\n" VM"v215,v215,v103\n" VM"v216,v216,v104\n" MF"a[28:31],v[36:39],v[76:79],a[28:31]\n"
 TR"v[52:53],v11 offset:40960\n" TR"v[54:55],v11 offset:45056\n" VM"v217,v217,v105\n" VM"v218,v218,v106\n" WL"(10)\n" MF"a[32:35],v[40:43],v[64:67],a[32:35]\n" TR"v[56:57],v12 offset:40960\n"
 TR"v[58:59],v12 offset:45056\n" VM"v219,v219,v107\n" VM"v220,v220,v108\n" MF"a[36:39],v[40:43],v[68:71],a[36:39]\n" TR"v[60:61],v13 offset:40960\n" TR"v[62:63],v13 offset:45056\n"
 VM"v221,v221,v109\n" VM"v222,v222,v110\n" MF"a[40:43],v[40:43],v[72:75],a[40:43]\n" BL"v3,s[72:75],s58 offen offset:1024 lds\n" VM"v223,v223,v111\n" CV"v120,v208,v209\n"
 MF"a[44:47],v[40:43],v[76:79],a[44:47]\n" CV"v121,v210,v211\n" CV"v122,v212,v213\n" WL"(12)\n" MF"a[48:51],v[44:47],v[64:67],a[48:51]\n" CV"v123,v214,v215\n" CV"v124,v216,v217\n"
 MF"a[52:55],v[44:47],v[68:71],a[52:55]\n" BL"v136,s[72:75],s58 offen offset:2048 lds\n" CV"v125,v218,v219\n" CV"v126,v220,v221\n" MF"a[56:59],v[44:47],v[72:75],a[56:59]\n" CV"v127,v222,v223\n"
 SA"s50,s97,16384\n" MF"a[60:63],v[44:47],v[76:79],a[60:63]\n" BL"v137,s[72:75],s58 offen offset:3072 lds\n" BS"v[120:123],v0,s[88:91],s50 offen sc1\n"
 BS"v[124:127],v0,s[88:91],s50 offen offset:1024 sc1\n" WL"(6)\n" MF"a[0:3],v[48:51],v[80:83],a[0:3]\n" WV"(14) lgkmcnt(0)\n""s_barrier\n" VM"v224,v224,v141\n""v_fmac_f32 v224,v144,v142\n"
 MF"a[4:7],v[48:51],v[84:87],a[4:7]\n" DR"v[64:67],v8 offset:0\n" VM"v225,v225,v141\n""v_fmac_f32 v225,v145,v142\n" MF"a[8:11],v[48:51],v[88:91],a[8:11]\n" DR"v[68:71],v8 offset:2048\n"
 VM"v226,v226,v141\n""v_fmac_f32 v226,v146,v142\n" MF"a[12:15],v[48:51],v[92:95],a[12:15]\n" DR"v[72:75],v8 offset:4096\n" VM"v227,v227,v141\n""v_fmac_f32 v227,v147,v142\n"
 MF"a[16:19],v[52:55],v[80:83],a[16:19]\n" DR"v[76:79],v8 offset:6144\n" VM"v228,v228,v141\n""v_fmac_f32 v228,v148,v142\n" MF"a[20:23],v[52:55],v[84:87],a[20:23]\n" SA"m0,s61,65536\n"
 SA"s59,s41,16384\n" BL"v0,s[76:79],s59 offen offset:0 sc1 lds\n" VM"v229,v229,v141\n""v_fmac_f32 v229,v149,v142\n" MF"a[24:27],v[52:55],v[88:91],a[24:27]\n" TR"v[32:33],v4 offset:0\n"
 TR"v[34:35],v4 offset:4096\n" VM"v230,v230,v141\n""v_fmac_f32 v230,v150,v142\n" MF"a[28:31],v[52:55],v[92:95],a[28:31]\n" TR"v[36:37],v5 offset:0\n" TR"v[38:39],v5 offset:4096\n" VM"v231,v231,v141\n"
"v_fmac_f32 v231,v151,v142\n" MF"a[32:35],v[56:59],v[80:83],a[32:35]\n" TR"v[40:41],v6 offset:0\n" TR"v[42:43],v6 offset:4096\n" VM"v232,v232,v141\n""v_fmac_f32 v232,v152,v142\n"
 MF"a[36:39],v[56:59],v[84:87],a[36:39]\n" TR"v[44:45],v7 offset:0\n" TR"v[46:47],v7 offset:4096\n" VM"v233,v233,v141\n""v_fmac_f32 v233,v153,v142\n" MF"a[40:43],v[56:59],v[88:91],a[40:43]\n"
 BL"v0,s[76:79],s59 offen offset:1024 sc1 lds\n" VM"v234,v234,v141\n""v_fmac_f32 v234,v154,v142\n" MF"a[44:47],v[56:59],v[92:95],a[44:47]\n" VM"v235,v235,v141\n""v_fmac_f32 v235,v155,v142\n"
 MF"a[48:51],v[60:63],v[80:83],a[48:51]\n" VM"v236,v236,v141\n""v_fmac_f32 v236,v156,v142\n" MF"a[52:55],v[60:63],v[84:87],a[52:55]\n" SA"m0,s61,67584\n" SA"s59,s43,16384\n"
 BL"v0,s[76:79],s59 offen offset:0 sc1 lds\n" VM"v237,v237,v141\n""v_fmac_f32 v237,v157,v142\n" MF"a[56:59],v[60:63],v[88:91],a[56:59]\n" VM"v238,v238,v141\n""v_fmac_f32 v238,v158,v142\n"
 MF"a[60:63],v[60:63],v[92:95],a[60:63]\n" BL"v0,s[76:79],s59 offen offset:1024 sc1 lds\n" VM"v239,v239,v141\n""v_fmac_f32 v239,v159,v142\n" WL"(6)\n" MF"a[0:3],v[32:35],v[64:67],a[0:3]\n"
"s_mov_b32 s60,0xbdd2d3e8\n" VV"v18,0xc0135761\n" MF"a[4:7],v[32:35],v[68:71],a[4:7]\n" DR"v[80:83],v8 offset:1024\n" VM"v96,v224,v224\n" VM"v97,v225,v225\n" MF"a[8:11],v[32:35],v[72:75],a[8:11]\n"
 DR"v[84:87],v8 offset:3072\n" VM"v98,v226,v226\n" VM"v99,v227,v227\n" MF"a[12:15],v[32:35],v[76:79],a[12:15]\n" DR"v[88:91],v8 offset:5120\n" VM"v100,v228,v228\n" VM"v101,v229,v229\n" WL"(7)\n"
 MF"a[16:19],v[36:39],v[64:67],a[16:19]\n" DR"v[92:95],v8 offset:7168\n" VM"v102,v230,v230\n" VM"v103,v231,v231\n" MF"a[20:23],v[36:39],v[68:71],a[20:23]\n" SA"m0,s63,98304\n" SA"s58,s40,589824\n"
 BL"v2,s[72:75],s58 offen offset:0 lds\n" VM"v104,v232,v232\n" VM"v105,v233,v233\n" MF"a[24:27],v[36:39],v[72:75],a[24:27]\n" TR"v[48:49],v4 offset:8192\n" TR"v[50:51],v4 offset:12288\n"
 VM"v106,v234,v234\n" VM"v107,v235,v235\n" MF"a[28:31],v[36:39],v[76:79],a[28:31]\n" TR"v[52:53],v5 offset:8192\n" TR"v[54:55],v5 offset:12288\n" VM"v108,v236,v236\n" VM"v109,v237,v237\n" WL"(10)\n"
 MF"a[32:35],v[40:43],v[64:67],a[32:35]\n" TR"v[56:57],v6 offset:8192\n" TR"v[58:59],v6 offset:12288\n" VM"v110,v238,v238\n" VM"v111,v239,v239\n" MF"a[36:39],v[40:43],v[68:71],a[36:39]\n"
 TR"v[60:61],v7 offset:8192\n" TR"v[62:63],v7 offset:12288\n""v_fma_f32 v96,v96,s60,v18\n""v_fma_f32 v97,v97,s60,v18\n" MF"a[40:43],v[40:43],v[72:75],a[40:43]\n"
 BL"v3,s[72:75],s58 offen offset:1024 lds\n""v_fma_f32 v98,v98,s60,v18\n""v_fma_f32 v99,v99,s60,v18\n" MF"a[44:47],v[40:43],v[76:79],a[44:47]\n""v_fma_f32 v100,v100,s60,v18\n"
"v_fma_f32 v101,v101,s60,v18\n" WL"(12)\n" MF"a[48:51],v[44:47],v[64:67],a[48:51]\n""v_fma_f32 v102,v102,s60,v18\n""v_fma_f32 v103,v103,s60,v18\n" MF"a[52:55],v[44:47],v[68:71],a[52:55]\n"
 BL"v136,s[72:75],s58 offen offset:2048 lds\n""v_fma_f32 v104,v104,s60,v18\n""v_fma_f32 v105,v105,s60,v18\n" MF"a[56:59],v[44:47],v[72:75],a[56:59]\n""v_fma_f32 v106,v106,s60,v18\n"
"v_fma_f32 v107,v107,s60,v18\n" MF"a[60:63],v[44:47],v[76:79],a[60:63]\n" BL"v137,s[72:75],s58 offen offset:3072 lds\n""v_fma_f32 v108,v108,s60,v18\n""v_fma_f32 v109,v109,s60,v18\n" WL"(6)\n"
 MF"a[0:3],v[48:51],v[80:83],a[0:3]\n" WV"(14) lgkmcnt(0)\n""s_barrier\n""v_fma_f32 v110,v110,s60,v18\n""v_fma_f32 v111,v111,s60,v18\n" MF"a[4:7],v[48:51],v[84:87],a[4:7]\n"
 DR"v[64:67],v8 offset:32768\n" VM"v96,v96,v224\n" VM"v97,v97,v225\n" MF"a[8:11],v[48:51],v[88:91],a[8:11]\n" DR"v[68:71],v8 offset:34816\n" VM"v98,v98,v226\n" VM"v99,v99,v227\n"
 MF"a[12:15],v[48:51],v[92:95],a[12:15]\n" DR"v[72:75],v8 offset:36864\n" VM"v100,v100,v228\n" VM"v101,v101,v229\n" MF"a[16:19],v[52:55],v[80:83],a[16:19]\n" DR"v[76:79],v8 offset:38912\n"
 VM"v102,v102,v230\n" VM"v103,v103,v231\n" MF"a[20:23],v[52:55],v[84:87],a[20:23]\n" SA"m0,s61,98304\n" SA"s59,s41,18432\n" BL"v0,s[76:79],s59 offen offset:0 sc1 lds\n" VM"v104,v104,v232\n"
 VM"v105,v105,v233\n" MF"a[24:27],v[52:55],v[88:91],a[24:27]\n" TR"v[32:33],v4 offset:32768\n" TR"v[34:35],v4 offset:36864\n" VM"v106,v106,v234\n" VM"v107,v107,v235\n"
 MF"a[28:31],v[52:55],v[92:95],a[28:31]\n" TR"v[36:37],v5 offset:32768\n" TR"v[38:39],v5 offset:36864\n" VM"v108,v108,v236\n" VM"v109,v109,v237\n" MF"a[32:35],v[56:59],v[80:83],a[32:35]\n"
 TR"v[40:41],v6 offset:32768\n" TR"v[42:43],v6 offset:36864\n" VM"v110,v110,v238\n" VM"v111,v111,v239\n" MF"a[36:39],v[56:59],v[84:87],a[36:39]\n" TR"v[44:45],v7 offset:32768\n"
 TR"v[46:47],v7 offset:36864\n" EX"v96,v96\n" EX"v97,v97\n" MF"a[40:43],v[56:59],v[88:91],a[40:43]\n" BL"v0,s[76:79],s59 offen offset:1024 sc1 lds\n" EX"v98,v98\n" EX"v99,v99\n"
 MF"a[44:47],v[56:59],v[92:95],a[44:47]\n" EX"v100,v100\n" EX"v101,v101\n" MF"a[48:51],v[60:63],v[80:83],a[48:51]\n" EX"v102,v102\n" EX"v103,v103\n" MF"a[52:55],v[60:63],v[84:87],a[52:55]\n"
 SA"m0,s61,100352\n" SA"s59,s43,18432\n" BL"v0,s[76:79],s59 offen offset:0 sc1 lds\n" EX"v104,v104\n" EX"v105,v105\n" MF"a[56:59],v[60:63],v[88:91],a[56:59]\n" EX"v106,v106\n" EX"v107,v107\n"
 MF"a[60:63],v[60:63],v[92:95],a[60:63]\n" BL"v0,s[76:79],s59 offen offset:1024 sc1 lds\n" EX"v108,v108\n" EX"v109,v109\n" WL"(6)\n" MF"a[0:3],v[32:35],v[64:67],a[0:3]\n" EX"v110,v110\n"
 EX"v111,v111\n" MF"a[4:7],v[32:35],v[68:71],a[4:7]\n" DR"v[80:83],v8 offset:33792\n""v_add_f32 v96,1.0,v96\n""v_add_f32 v97,1.0,v97\n" MF"a[8:11],v[32:35],v[72:75],a[8:11]\n"
 DR"v[84:87],v8 offset:35840\n""v_add_f32 v98,1.0,v98\n""v_add_f32 v99,1.0,v99\n" MF"a[12:15],v[32:35],v[76:79],a[12:15]\n" DR"v[88:91],v8 offset:37888\n""v_add_f32 v100,1.0,v100\n"
"v_add_f32 v101,1.0,v101\n" WL"(7)\n" MF"a[16:19],v[36:39],v[64:67],a[16:19]\n" DR"v[92:95],v8 offset:39936\n""v_add_f32 v102,1.0,v102\n""v_add_f32 v103,1.0,v103\n"
 MF"a[20:23],v[36:39],v[68:71],a[20:23]\n" SA"m0,s63,0\n" SA"s58,s40,655360\n" BL"v2,s[72:75],s58 offen offset:0 lds\n""v_add_f32 v104,1.0,v104\n""v_add_f32 v105,1.0,v105\n"
 MF"a[24:27],v[36:39],v[72:75],a[24:27]\n" TR"v[48:49],v4 offset:40960\n" TR"v[50:51],v4 offset:45056\n""v_add_f32 v106,1.0,v106\n""v_add_f32 v107,1.0,v107\n" MF"a[28:31],v[36:39],v[76:79],a[28:31]\n"
 TR"v[52:53],v5 offset:40960\n" TR"v[54:55],v5 offset:45056\n""v_add_f32 v108,1.0,v108\n""v_add_f32 v109,1.0,v109\n" WL"(10)\n" MF"a[32:35],v[40:43],v[64:67],a[32:35]\n" TR"v[56:57],v6 offset:40960\n"
 TR"v[58:59],v6 offset:45056\n""v_add_f32 v110,1.0,v110\n""v_add_f32 v111,1.0,v111\n" MF"a[36:39],v[40:43],v[68:71],a[36:39]\n" TR"v[60:61],v7 offset:40960\n" TR"v[62:63],v7 offset:45056\n"
"v_rcp_f32 v96,v96\n""v_rcp_f32 v97,v97\n" MF"a[40:43],v[40:43],v[72:75],a[40:43]\n" BL"v3,s[72:75],s58 offen offset:1024 lds\n""v_rcp_f32 v98,v98\n""v_rcp_f32 v99,v99\n"
 MF"a[44:47],v[40:43],v[76:79],a[44:47]\n""v_rcp_f32 v100,v100\n""v_rcp_f32 v101,v101\n" WL"(12)\n" MF"a[48:51],v[44:47],v[64:67],a[48:51]\n""v_rcp_f32 v102,v102\n""v_rcp_f32 v103,v103\n"
 MF"a[52:55],v[44:47],v[68:71],a[52:55]\n" BL"v136,s[72:75],s58 offen offset:2048 lds\n""v_rcp_f32 v104,v104\n""v_rcp_f32 v105,v105\n" MF"a[56:59],v[44:47],v[72:75],a[56:59]\n""v_rcp_f32 v106,v106\n"
"v_rcp_f32 v107,v107\n" MF"a[60:63],v[44:47],v[76:79],a[60:63]\n" BL"v137,s[72:75],s58 offen offset:3072 lds\n""v_rcp_f32 v108,v108\n""v_rcp_f32 v109,v109\n" WL"(6)\n"
 MF"a[0:3],v[48:51],v[80:83],a[0:3]\n" WV"(12) lgkmcnt(0)\n""s_barrier\n""v_rcp_f32 v110,v110\n""v_rcp_f32 v111,v111\n" MF"a[4:7],v[48:51],v[84:87],a[4:7]\n" DR"v[64:67],v9 offset:0\n"
 VM"v224,v224,v96\n" VM"v225,v225,v97\n" MF"a[8:11],v[48:51],v[88:91],a[8:11]\n" DR"v[68:71],v9 offset:2048\n" VM"v226,v226,v98\n" VM"v227,v227,v99\n" MF"a[12:15],v[48:51],v[92:95],a[12:15]\n"
 DR"v[72:75],v9 offset:4096\n" VM"v228,v228,v100\n" VM"v229,v229,v101\n" MF"a[16:19],v[52:55],v[80:83],a[16:19]\n" DR"v[76:79],v9 offset:6144\n" VM"v230,v230,v102\n" VM"v231,v231,v103\n"
 MF"a[20:23],v[52:55],v[84:87],a[20:23]\n" SA"m0,s61,0\n" SA"s59,s41,20480\n" BL"v0,s[76:79],s59 offen offset:0 sc1 lds\n" VM"v232,v232,v104\n" VM"v233,v233,v105\n"
 MF"a[24:27],v[52:55],v[88:91],a[24:27]\n" TR"v[32:33],v10 offset:0\n" TR"v[34:35],v10 offset:4096\n" VM"v234,v234,v106\n" VM"v235,v235,v107\n" MF"a[28:31],v[52:55],v[92:95],a[28:31]\n"
 TR"v[36:37],v11 offset:0\n" TR"v[38:39],v11 offset:4096\n" VM"v236,v236,v108\n" VM"v237,v237,v109\n" MF"a[32:35],v[56:59],v[80:83],a[32:35]\n" TR"v[40:41],v12 offset:0\n"
 TR"v[42:43],v12 offset:4096\n" VM"v238,v238,v110\n" VM"v239,v239,v111\n" MF"a[36:39],v[56:59],v[84:87],a[36:39]\n" TR"v[44:45],v13 offset:0\n" TR"v[46:47],v13 offset:4096\n" CV"v128,v224,v225\n"
 CV"v129,v226,v227\n" MF"a[40:43],v[56:59],v[88:91],a[40:43]\n" BL"v0,s[76:79],s59 offen offset:1024 sc1 lds\n" CV"v130,v228,v229\n" CV"v131,v230,v231\n" MF"a[44:47],v[56:59],v[92:95],a[44:47]\n"
 CV"v132,v232,v233\n" CV"v133,v234,v235\n" MF"a[48:51],v[60:63],v[80:83],a[48:51]\n" CV"v134,v236,v237\n" CV"v135,v238,v239\n" MF"a[52:55],v[60:63],v[84:87],a[52:55]\n" SA"m0,s61,2048\n"
 SA"s59,s43,20480\n" BL"v0,s[76:79],s59 offen offset:0 sc1 lds\n" SA"s50,s97,32768\n" SA"s51,s50,0\n" MF"a[56:59],v[60:63],v[88:91],a[56:59]\n""s_cmp_gt_u32 s65,5\n"
"s_cselect_b32 s50,0x70000000,s50\n""s_cmp_ge_u32 s65,5\n""s_cselect_b32 s51,0x70000000,s51\n" MF"a[60:63],v[60:63],v[92:95],a[60:63]\n" BL"v0,s[76:79],s59 offen offset:1024 sc1 lds\n"
 BS"v[128:131],v0,s[88:91],s50 offen sc1\n" BS"v[132:135],v0,s[88:91],s51 offen offset:1024 sc1\n" WL"(6)\n" MF"a[0:3],v[32:35],v[64:67],a[0:3]\n" MF"a[4:7],v[32:35],v[68:71],a[4:7]\n"
 DR"v[80:83],v9 offset:1024\n" MF"a[8:11],v[32:35],v[72:75],a[8:11]\n" DR"v[84:87],v9 offset:3072\n" MF"a[12:15],v[32:35],v[76:79],a[12:15]\n" DR"v[88:91],v9 offset:5120\n" WL"(7)\n"
 MF"a[16:19],v[36:39],v[64:67],a[16:19]\n" DR"v[92:95],v9 offset:7168\n" MF"a[20:23],v[36:39],v[68:71],a[20:23]\n" SA"m0,s63,32768\n" SA"s58,s40,720896\n" BL"v2,s[72:75],s58 offen offset:0 lds\n"
 MF"a[24:27],v[36:39],v[72:75],a[24:27]\n" TR"v[48:49],v10 offset:8192\n" TR"v[50:51],v10 offset:12288\n" MF"a[28:31],v[36:39],v[76:79],a[28:31]\n" TR"v[52:53],v11 offset:8192\n"
 TR"v[54:55],v11 offset:12288\n" WL"(10)\n" MF"a[32:35],v[40:43],v[64:67],a[32:35]\n" TR"v[56:57],v12 offset:8192\n" TR"v[58:59],v12 offset:12288\n" MF"a[36:39],v[40:43],v[68:71],a[36:39]\n"
 TR"v[60:61],v13 offset:8192\n" TR"v[62:63],v13 offset:12288\n" MF"a[40:43],v[40:43],v[72:75],a[40:43]\n" BL"v3,s[72:75],s58 offen offset:1024 lds\n" MF"a[44:47],v[40:43],v[76:79],a[44:47]\n"
 WL"(12)\n" MF"a[48:51],v[44:47],v[64:67],a[48:51]\n" MF"a[52:55],v[44:47],v[68:71],a[52:55]\n" BL"v136,s[72:75],s58 offen offset:2048 lds\n" MF"a[56:59],v[44:47],v[72:75],a[56:59]\n"
 MF"a[60:63],v[44:47],v[76:79],a[60:63]\n" BL"v137,s[72:75],s58 offen offset:3072 lds\n" WL"(6)\n" MF"a[0:3],v[48:51],v[80:83],a[0:3]\n" WV"(14) lgkmcnt(0)\n""s_barrier\n"
 MF"a[4:7],v[48:51],v[84:87],a[4:7]\n" DR"v[64:67],v9 offset:32768\n" MF"a[8:11],v[48:51],v[88:91],a[8:11]\n" DR"v[68:71],v9 offset:34816\n" MF"a[12:15],v[48:51],v[92:95],a[12:15]\n"
 DR"v[72:75],v9 offset:36864\n" MF"a[16:19],v[52:55],v[80:83],a[16:19]\n" DR"v[76:79],v9 offset:38912\n" MF"a[20:23],v[52:55],v[84:87],a[20:23]\n" SA"m0,s61,32768\n" SA"s59,s41,22528\n"
 BL"v0,s[76:79],s59 offen offset:0 sc1 lds\n" MF"a[24:27],v[52:55],v[88:91],a[24:27]\n" TR"v[32:33],v10 offset:32768\n" TR"v[34:35],v10 offset:36864\n" MF"a[28:31],v[52:55],v[92:95],a[28:31]\n"
 TR"v[36:37],v11 offset:32768\n" TR"v[38:39],v11 offset:36864\n" MF"a[32:35],v[56:59],v[80:83],a[32:35]\n" TR"v[40:41],v12 offset:32768\n" TR"v[42:43],v12 offset:36864\n"
 MF"a[36:39],v[56:59],v[84:87],a[36:39]\n" TR"v[44:45],v13 offset:32768\n" TR"v[46:47],v13 offset:36864\n" MF"a[40:43],v[56:59],v[88:91],a[40:43]\n" BL"v0,s[76:79],s59 offen offset:1024 sc1 lds\n"
 MF"a[44:47],v[56:59],v[92:95],a[44:47]\n" MF"a[48:51],v[60:63],v[80:83],a[48:51]\n" MF"a[52:55],v[60:63],v[84:87],a[52:55]\n" SA"m0,s61,34816\n" SA"s59,s43,22528\n"
 BL"v0,s[76:79],s59 offen offset:0 sc1 lds\n" MF"a[56:59],v[60:63],v[88:91],a[56:59]\n" MF"a[60:63],v[60:63],v[92:95],a[60:63]\n" BL"v0,s[76:79],s59 offen offset:1024 sc1 lds\n" WL"(6)\n"
 MF"a[0:3],v[32:35],v[64:67],a[0:3]\n" MF"a[4:7],v[32:35],v[68:71],a[4:7]\n" DR"v[80:83],v9 offset:33792\n" MF"a[8:11],v[32:35],v[72:75],a[8:11]\n" DR"v[84:87],v9 offset:35840\n"
 MF"a[12:15],v[32:35],v[76:79],a[12:15]\n" DR"v[88:91],v9 offset:37888\n" WL"(7)\n" MF"a[16:19],v[36:39],v[64:67],a[16:19]\n" DR"v[92:95],v9 offset:39936\n" MF"a[20:23],v[36:39],v[68:71],a[20:23]\n"
 SA"m0,s63,65536\n" SA"s58,s40,786432\n" BL"v2,s[72:75],s58 offen offset:0 lds\n" MF"a[24:27],v[36:39],v[72:75],a[24:27]\n" TR"v[48:49],v10 offset:40960\n" TR"v[50:51],v10 offset:45056\n"
 MF"a[28:31],v[36:39],v[76:79],a[28:31]\n" TR"v[52:53],v11 offset:40960\n" TR"v[54:55],v11 offset:45056\n" WL"(10)\n" MF"a[32:35],v[40:43],v[64:67],a[32:35]\n" TR"v[56:57],v12 offset:40960\n"
 TR"v[58:59],v12 offset:45056\n" MF"a[36:39],v[40:43],v[68:71],a[36:39]\n" TR"v[60:61],v13 offset:40960\n" TR"v[62:63],v13 offset:45056\n" MF"a[40:43],v[40:43],v[72:75],a[40:43]\n"
 BL"v3,s[72:75],s58 offen offset:1024 lds\n" MF"a[44:47],v[40:43],v[76:79],a[44:47]\n" WL"(12)\n" MF"a[48:51],v[44:47],v[64:67],a[48:51]\n" MF"a[52:55],v[44:47],v[68:71],a[52:55]\n"
 BL"v136,s[72:75],s58 offen offset:2048 lds\n" MF"a[56:59],v[44:47],v[72:75],a[56:59]\n" MF"a[60:63],v[44:47],v[76:79],a[60:63]\n" BL"v137,s[72:75],s58 offen offset:3072 lds\n" WL"(6)\n"
 MF"a[0:3],v[48:51],v[80:83],a[0:3]\n" WV"(14) lgkmcnt(0)\n""s_barrier\n" MF"a[4:7],v[48:51],v[84:87],a[4:7]\n" DR"v[64:67],v8 offset:0\n" MF"a[8:11],v[48:51],v[88:91],a[8:11]\n"
 DR"v[68:71],v8 offset:2048\n" MF"a[12:15],v[48:51],v[92:95],a[12:15]\n" DR"v[72:75],v8 offset:4096\n" MF"a[16:19],v[52:55],v[80:83],a[16:19]\n" DR"v[76:79],v8 offset:6144\n"
 MF"a[20:23],v[52:55],v[84:87],a[20:23]\n" SA"m0,s61,65536\n" SA"s59,s41,24576\n" BL"v0,s[76:79],s59 offen offset:0 sc1 lds\n" MF"a[24:27],v[52:55],v[88:91],a[24:27]\n" TR"v[32:33],v4 offset:0\n"
 TR"v[34:35],v4 offset:4096\n" MF"a[28:31],v[52:55],v[92:95],a[28:31]\n" TR"v[36:37],v5 offset:0\n" TR"v[38:39],v5 offset:4096\n" MF"a[32:35],v[56:59],v[80:83],a[32:35]\n" TR"v[40:41],v6 offset:0\n"
 TR"v[42:43],v6 offset:4096\n" MF"a[36:39],v[56:59],v[84:87],a[36:39]\n" TR"v[44:45],v7 offset:0\n" TR"v[46:47],v7 offset:4096\n" MF"a[40:43],v[56:59],v[88:91],a[40:43]\n"
 BL"v0,s[76:79],s59 offen offset:1024 sc1 lds\n" MF"a[44:47],v[56:59],v[92:95],a[44:47]\n" MF"a[48:51],v[60:63],v[80:83],a[48:51]\n" MF"a[52:55],v[60:63],v[84:87],a[52:55]\n" SA"m0,s61,67584\n"
 SA"s59,s43,24576\n" BL"v0,s[76:79],s59 offen offset:0 sc1 lds\n" MF"a[56:59],v[60:63],v[88:91],a[56:59]\n" MF"a[60:63],v[60:63],v[92:95],a[60:63]\n" BL"v0,s[76:79],s59 offen offset:1024 sc1 lds\n"
 WL"(6)\n" MF"a[0:3],v[32:35],v[64:67],a[0:3]\n" MF"a[4:7],v[32:35],v[68:71],a[4:7]\n" DR"v[80:83],v8 offset:1024\n" MF"a[8:11],v[32:35],v[72:75],a[8:11]\n" DR"v[84:87],v8 offset:3072\n"
 MF"a[12:15],v[32:35],v[76:79],a[12:15]\n" DR"v[88:91],v8 offset:5120\n" WL"(7)\n" MF"a[16:19],v[36:39],v[64:67],a[16:19]\n" DR"v[92:95],v8 offset:7168\n" MF"a[20:23],v[36:39],v[68:71],a[20:23]\n"
 SA"m0,s63,98304\n" SA"s58,s40,851968\n" BL"v2,s[72:75],s58 offen offset:0 lds\n" MF"a[24:27],v[36:39],v[72:75],a[24:27]\n" TR"v[48:49],v4 offset:8192\n" TR"v[50:51],v4 offset:12288\n"
 MF"a[28:31],v[36:39],v[76:79],a[28:31]\n" TR"v[52:53],v5 offset:8192\n" TR"v[54:55],v5 offset:12288\n" WL"(10)\n" MF"a[32:35],v[40:43],v[64:67],a[32:35]\n" TR"v[56:57],v6 offset:8192\n"
 TR"v[58:59],v6 offset:12288\n" MF"a[36:39],v[40:43],v[68:71],a[36:39]\n" TR"v[60:61],v7 offset:8192\n" TR"v[62:63],v7 offset:12288\n" MF"a[40:43],v[40:43],v[72:75],a[40:43]\n"
 BL"v3,s[72:75],s58 offen offset:1024 lds\n" MF"a[44:47],v[40:43],v[76:79],a[44:47]\n" WL"(12)\n" MF"a[48:51],v[44:47],v[64:67],a[48:51]\n" MF"a[52:55],v[44:47],v[68:71],a[52:55]\n"
 BL"v136,s[72:75],s58 offen offset:2048 lds\n" MF"a[56:59],v[44:47],v[72:75],a[56:59]\n" MF"a[60:63],v[44:47],v[76:79],a[60:63]\n" BL"v137,s[72:75],s58 offen offset:3072 lds\n" WL"(6)\n"
 MF"a[0:3],v[48:51],v[80:83],a[0:3]\n" WV"(12) lgkmcnt(0)\n""s_barrier\n""s_lshr_b32 s44,s96,5\n""s_and_b32 s45,s96,7\n""s_lshl_b32 s45,s45,2\n""s_bfe_u32 s57,s96,0x20003\n" SA"s45,s45,s57\n"
"s_lshl_b32 s47,s45,2\n" SA"s47,s47,s36\n""s_lshl_b32 s57,s45,3\n" SA"s57,s57,s44\n""s_lshl_b32 s57,s57,3\n" SA"s57,s57,20480\n" VV"v244,s98\n" VV"v245,s99\n" VV"v246,0\n"
"buffer_store_dwordx2 v[244:245],v246,s[92:95],s57 offen sc1\n" MF"a[4:7],v[48:51],v[84:87],a[4:7]\n" DR"v[64:67],v8 offset:32768\n" MF"a[8:11],v[48:51],v[88:91],a[8:11]\n"
 DR"v[68:71],v8 offset:34816\n" MF"a[12:15],v[48:51],v[92:95],a[12:15]\n" DR"v[72:75],v8 offset:36864\n" MF"a[16:19],v[52:55],v[80:83],a[16:19]\n" DR"v[76:79],v8 offset:38912\n"
 MF"a[20:23],v[52:55],v[84:87],a[20:23]\n" SA"m0,s61,98304\n" SA"s59,s41,26624\n" BL"v0,s[76:79],s59 offen offset:0 sc1 lds\n" MF"a[24:27],v[52:55],v[88:91],a[24:27]\n" TR"v[32:33],v4 offset:32768\n"
 TR"v[34:35],v4 offset:36864\n" MF"a[28:31],v[52:55],v[92:95],a[28:31]\n" TR"v[36:37],v5 offset:32768\n" TR"v[38:39],v5 offset:36864\n" MF"a[32:35],v[56:59],v[80:83],a[32:35]\n"
 TR"v[40:41],v6 offset:32768\n" TR"v[42:43],v6 offset:36864\n" MF"a[36:39],v[56:59],v[84:87],a[36:39]\n" TR"v[44:45],v7 offset:32768\n" TR"v[46:47],v7 offset:36864\n"
 MF"a[40:43],v[56:59],v[88:91],a[40:43]\n" BL"v0,s[76:79],s59 offen offset:1024 sc1 lds\n" MF"a[44:47],v[56:59],v[92:95],a[44:47]\n" MF"a[48:51],v[60:63],v[80:83],a[48:51]\n"
 MF"a[52:55],v[60:63],v[84:87],a[52:55]\n" SA"m0,s61,100352\n" SA"s59,s43,26624\n" BL"v0,s[76:79],s59 offen offset:0 sc1 lds\n" MF"a[56:59],v[60:63],v[88:91],a[56:59]\n"
 MF"a[60:63],v[60:63],v[92:95],a[60:63]\n" BL"v0,s[76:79],s59 offen offset:1024 sc1 lds\n" WL"(6)\n" MF"a[0:3],v[32:35],v[64:67],a[0:3]\n" MF"a[4:7],v[32:35],v[68:71],a[4:7]\n"
 DR"v[80:83],v8 offset:33792\n" MF"a[8:11],v[32:35],v[72:75],a[8:11]\n" DR"v[84:87],v8 offset:35840\n" MF"a[12:15],v[32:35],v[76:79],a[12:15]\n" DR"v[88:91],v8 offset:37888\n" WL"(7)\n"
 MF"a[16:19],v[36:39],v[64:67],a[16:19]\n" DR"v[92:95],v8 offset:39936\n" MF"a[20:23],v[36:39],v[68:71],a[20:23]\n" SA"m0,s63,0\n" SA"s58,s40,917504\n" BL"v2,s[72:75],s58 offen offset:0 lds\n"
 MF"a[24:27],v[36:39],v[72:75],a[24:27]\n" TR"v[48:49],v4 offset:40960\n" TR"v[50:51],v4 offset:45056\n" MF"a[28:31],v[36:39],v[76:79],a[28:31]\n" TR"v[52:53],v5 offset:40960\n"
 TR"v[54:55],v5 offset:45056\n" WL"(10)\n" MF"a[32:35],v[40:43],v[64:67],a[32:35]\n" TR"v[56:57],v6 offset:40960\n" TR"v[58:59],v6 offset:45056\n" MF"a[36:39],v[40:43],v[68:71],a[36:39]\n"
 TR"v[60:61],v7 offset:40960\n" TR"v[62:63],v7 offset:45056\n" MF"a[40:43],v[40:43],v[72:75],a[40:43]\n" BL"v3,s[72:75],s58 offen offset:1024 lds\n" MF"a[44:47],v[40:43],v[76:79],a[44:47]\n"
 WL"(12)\n" MF"a[48:51],v[44:47],v[64:67],a[48:51]\n" MF"a[52:55],v[44:47],v[68:71],a[52:55]\n" BL"v136,s[72:75],s58 offen offset:2048 lds\n" MF"a[56:59],v[44:47],v[72:75],a[56:59]\n"
 MF"a[60:63],v[44:47],v[76:79],a[60:63]\n" BL"v137,s[72:75],s58 offen offset:3072 lds\n" WL"(6)\n" MF"a[0:3],v[48:51],v[80:83],a[0:3]\n" WV"(13) lgkmcnt(0)\n""s_barrier\n"
 MF"a[4:7],v[48:51],v[84:87],a[4:7]\n" DR"v[64:67],v9 offset:0\n" MF"a[8:11],v[48:51],v[88:91],a[8:11]\n" DR"v[68:71],v9 offset:2048\n" MF"a[12:15],v[48:51],v[92:95],a[12:15]\n"
 DR"v[72:75],v9 offset:4096\n" MF"a[16:19],v[52:55],v[80:83],a[16:19]\n" DR"v[76:79],v9 offset:6144\n" MF"a[20:23],v[52:55],v[84:87],a[20:23]\n" SA"m0,s61,0\n" SA"s59,s41,28672\n"
 BL"v0,s[76:79],s59 offen offset:0 sc1 lds\n" MF"a[24:27],v[52:55],v[88:91],a[24:27]\n" TR"v[32:33],v10 offset:0\n" TR"v[34:35],v10 offset:4096\n" MF"a[28:31],v[52:55],v[92:95],a[28:31]\n"
 TR"v[36:37],v11 offset:0\n" TR"v[38:39],v11 offset:4096\n" MF"a[32:35],v[56:59],v[80:83],a[32:35]\n" TR"v[40:41],v12 offset:0\n" TR"v[42:43],v12 offset:4096\n"
 MF"a[36:39],v[56:59],v[84:87],a[36:39]\n" TR"v[44:45],v13 offset:0\n" TR"v[46:47],v13 offset:4096\n" MF"a[40:43],v[56:59],v[88:91],a[40:43]\n" BL"v0,s[76:79],s59 offen offset:1024 sc1 lds\n"
 MF"a[44:47],v[56:59],v[92:95],a[44:47]\n" MF"a[48:51],v[60:63],v[80:83],a[48:51]\n" MF"a[52:55],v[60:63],v[84:87],a[52:55]\n" SA"m0,s61,2048\n" SA"s59,s43,28672\n"
 BL"v0,s[76:79],s59 offen offset:0 sc1 lds\n" MF"a[56:59],v[60:63],v[88:91],a[56:59]\n" MF"a[60:63],v[60:63],v[92:95],a[60:63]\n" BL"v0,s[76:79],s59 offen offset:1024 sc1 lds\n"
"v_mbcnt_lo_u32_b32 v250,-1,0\n""v_mbcnt_hi_u32_b32 v250,-1,v250\n""v_lshlrev_b32 v0,4,v250\n""v_lshrrev_b32 v251,3,v250\n""v_mul_u32_u24 v251,1024,v251\n""v_lshrrev_b32 v252,4,v250\n"
"v_lshlrev_b32 v253,1,v252\n""v_and_b32 v14,7,v250\n""v_xor_b32 v253,v253,v14\n""v_lshl_add_u32 v14,v253,4,v251\n""s_lshl_b32 s62,s36,12\n""s_lshr_b32 s48,s96,5\n""s_and_b32 s49,s96,7\n"
"s_lshl_b32 s49,s49,2\n""s_bfe_u32 s57,s96,0x20003\n" SA"s49,s49,s57\n""s_lshl_b32 s51,s49,2\n" SA"s51,s51,s36\n""v_readlane_b32 s80,v254,18\n""v_readlane_b32 s81,v254,19\n""s_mov_b32 s82,1409024\n"
"s_mov_b32 s83,0x20000\n" SA"s64,s52,14680064\n""s_addc_u32 s65,s53,0\n""s_mov_b32 s66,5636096\n""s_mov_b32 s67,0x20000\n""s_lshl_b32 s44,s36,15\n""s_lshl_b32 s57,s48,7\n" SA"s44,s44,s57\n"
"s_mul_i32 s46,s51,44032\n""s_lshl_b32 s47,s51,14\n""s_lshl_b32 s57,s48,11\n" SA"s47,s47,s57\n""v_mbcnt_lo_u32_b32 v251,-1,0\n""v_mbcnt_hi_u32_b32 v251,-1,v251\n""v_min_u32 v251,7,v251\n"
"s_lshl_b32 s57,s49,6\n" SA"s57,s57,20480\n" VV"v248,s57\n""v_lshl_add_u32 v248,v251,3,v248\n""buffer_load_dwordx2 v[252:253],v248,s[92:95],0 offen sc1\n" WL"(6)\n"
 MF"a[0:3],v[32:35],v[64:67],a[0:3]\n" MF"a[4:7],v[32:35],v[68:71],a[4:7]\n" DR"v[80:83],v9 offset:1024\n" MF"a[8:11],v[32:35],v[72:75],a[8:11]\n" DR"v[84:87],v9 offset:3072\n"
 MF"a[12:15],v[32:35],v[76:79],a[12:15]\n" DR"v[88:91],v9 offset:5120\n" WL"(7)\n" MF"a[16:19],v[36:39],v[64:67],a[16:19]\n" DR"v[92:95],v9 offset:7168\n" MF"a[20:23],v[36:39],v[68:71],a[20:23]\n"
 SA"m0,s63,32768\n" SA"s58,s40,983040\n" BL"v2,s[72:75],s58 offen offset:0 lds\n" MF"a[24:27],v[36:39],v[72:75],a[24:27]\n" TR"v[48:49],v10 offset:8192\n" TR"v[50:51],v10 offset:12288\n"
 MF"a[28:31],v[36:39],v[76:79],a[28:31]\n" TR"v[52:53],v11 offset:8192\n" TR"v[54:55],v11 offset:12288\n" WL"(10)\n" MF"a[32:35],v[40:43],v[64:67],a[32:35]\n" TR"v[56:57],v12 offset:8192\n"
 TR"v[58:59],v12 offset:12288\n" MF"a[36:39],v[40:43],v[68:71],a[36:39]\n" TR"v[60:61],v13 offset:8192\n" TR"v[62:63],v13 offset:12288\n" MF"a[40:43],v[40:43],v[72:75],a[40:43]\n"
 BL"v3,s[72:75],s58 offen offset:1024 lds\n" MF"a[44:47],v[40:43],v[76:79],a[44:47]\n" WL"(12)\n" MF"a[48:51],v[44:47],v[64:67],a[48:51]\n" MF"a[52:55],v[44:47],v[68:71],a[52:55]\n"
 BL"v136,s[72:75],s58 offen offset:2048 lds\n" MF"a[56:59],v[44:47],v[72:75],a[56:59]\n" MF"a[60:63],v[44:47],v[76:79],a[60:63]\n" BL"v137,s[72:75],s58 offen offset:3072 lds\n" WL"(6)\n"
 MF"a[0:3],v[48:51],v[80:83],a[0:3]\n" WV"(13) lgkmcnt(0)\n""s_barrier\n" MF"a[4:7],v[48:51],v[84:87],a[4:7]\n" DR"v[64:67],v9 offset:32768\n" MF"a[8:11],v[48:51],v[88:91],a[8:11]\n"
 DR"v[68:71],v9 offset:34816\n" MF"a[12:15],v[48:51],v[92:95],a[12:15]\n" DR"v[72:75],v9 offset:36864\n" MF"a[16:19],v[52:55],v[80:83],a[16:19]\n" DR"v[76:79],v9 offset:38912\n"
 MF"a[20:23],v[52:55],v[84:87],a[20:23]\n" SA"m0,s61,32768\n" SA"s59,s41,30720\n" BL"v0,s[76:79],s59 offen offset:0 sc1 lds\n" MF"a[24:27],v[52:55],v[88:91],a[24:27]\n" TR"v[32:33],v10 offset:32768\n"
 TR"v[34:35],v10 offset:36864\n" MF"a[28:31],v[52:55],v[92:95],a[28:31]\n" TR"v[36:37],v11 offset:32768\n" TR"v[38:39],v11 offset:36864\n" MF"a[32:35],v[56:59],v[80:83],a[32:35]\n"
 TR"v[40:41],v12 offset:32768\n" TR"v[42:43],v12 offset:36864\n" MF"a[36:39],v[56:59],v[84:87],a[36:39]\n" TR"v[44:45],v13 offset:32768\n" TR"v[46:47],v13 offset:36864\n"
 MF"a[40:43],v[56:59],v[88:91],a[40:43]\n" BL"v0,s[76:79],s59 offen offset:1024 sc1 lds\n" MF"a[44:47],v[56:59],v[92:95],a[44:47]\n" MF"a[48:51],v[60:63],v[80:83],a[48:51]\n"
 MF"a[52:55],v[60:63],v[84:87],a[52:55]\n" SA"m0,s61,34816\n" SA"s59,s43,30720\n" BL"v0,s[76:79],s59 offen offset:0 sc1 lds\n" MF"a[56:59],v[60:63],v[88:91],a[56:59]\n"
 MF"a[60:63],v[60:63],v[92:95],a[60:63]\n" BL"v0,s[76:79],s59 offen offset:1024 sc1 lds\n" SA"m0,s62,131072\n" SA"s58,s44,0\n" BL"v14,s[80:83],s58 offen lds\n" SA"m0,s62,132096\n" SA"s58,s44,8192\n"
 BL"v14,s[80:83],s58 offen lds\n" SA"m0,s62,133120\n" SA"s58,s44,16384\n" BL"v14,s[80:83],s58 offen lds\n" SA"m0,s62,134144\n" SA"s58,s44,24576\n" BL"v14,s[80:83],s58 offen lds\n" SA"m0,s62,147456\n"
 SA"s58,s44,131072\n" BL"v14,s[80:83],s58 offen lds\n" SA"m0,s62,148480\n" SA"s58,s44,139264\n" BL"v14,s[80:83],s58 offen lds\n" SA"m0,s62,149504\n" SA"s58,s44,147456\n"
 BL"v14,s[80:83],s58 offen lds\n" SA"m0,s62,150528\n" SA"s58,s44,155648\n" BL"v14,s[80:83],s58 offen lds\n" WL"(6)\n" MF"a[0:3],v[32:35],v[64:67],a[0:3]\n" MF"a[4:7],v[32:35],v[68:71],a[4:7]\n"
 DR"v[80:83],v9 offset:33792\n" MF"a[8:11],v[32:35],v[72:75],a[8:11]\n" DR"v[84:87],v9 offset:35840\n" MF"a[12:15],v[32:35],v[76:79],a[12:15]\n" DR"v[88:91],v9 offset:37888\n" WL"(7)\n"
 MF"a[16:19],v[36:39],v[64:67],a[16:19]\n" DR"v[92:95],v9 offset:39936\n" MF"a[20:23],v[36:39],v[68:71],a[20:23]\n" MF"a[24:27],v[36:39],v[72:75],a[24:27]\n" TR"v[48:49],v10 offset:40960\n"
 TR"v[50:51],v10 offset:45056\n" MF"a[28:31],v[36:39],v[76:79],a[28:31]\n" TR"v[52:53],v11 offset:40960\n" TR"v[54:55],v11 offset:45056\n" WL"(10)\n" MF"a[32:35],v[40:43],v[64:67],a[32:35]\n"
 TR"v[56:57],v12 offset:40960\n" TR"v[58:59],v12 offset:45056\n" MF"a[36:39],v[40:43],v[68:71],a[36:39]\n" TR"v[60:61],v13 offset:40960\n" TR"v[62:63],v13 offset:45056\n"
 MF"a[40:43],v[40:43],v[72:75],a[40:43]\n" MF"a[44:47],v[40:43],v[76:79],a[44:47]\n" WL"(12)\n" MF"a[48:51],v[44:47],v[64:67],a[48:51]\n" MF"a[52:55],v[44:47],v[68:71],a[52:55]\n"
 MF"a[56:59],v[44:47],v[72:75],a[56:59]\n" MF"a[60:63],v[44:47],v[76:79],a[60:63]\n" WL"(6)\n" MF"a[0:3],v[48:51],v[80:83],a[0:3]\n" WV"(17) lgkmcnt(0)\n""s_barrier\n" WV"(16)\n"
"v_cmp_ne_u64 vcc,s[98:99],v[252:253]\n""s_nop 1\n""s_cmp_eq_u64 vcc,0\n""s_cbranch_scc1 Lokp5_%=\n""s_mov_b32 s57,32768\n""Lspp5_%=:\n""s_sleep 2\n"
"buffer_load_dwordx2 v[252:253],v248,s[92:95],0 offen sc1\n" WV"(0)\n""v_cmp_ne_u64 vcc,s[98:99],v[252:253]\n""s_nop 1\n""s_cmp_eq_u64 vcc,0\n""s_cbranch_scc1 Lokp5_%=\n""s_sub_u32 s57,s57,1\n"
"s_cmp_lg_u32 s57,0\n""s_cbranch_scc1 Lspp5_%=\n""Lokp5_%=:\n" MF"a[4:7],v[48:51],v[84:87],a[4:7]\n" DR"v[64:67],v8 offset:0\n" MF"a[8:11],v[48:51],v[88:91],a[8:11]\n" DR"v[68:71],v8 offset:2048\n"
 MF"a[12:15],v[48:51],v[92:95],a[12:15]\n" DR"v[72:75],v8 offset:4096\n" MF"a[16:19],v[52:55],v[80:83],a[16:19]\n" DR"v[76:79],v8 offset:6144\n" MF"a[20:23],v[52:55],v[84:87],a[20:23]\n"
 MF"a[24:27],v[52:55],v[88:91],a[24:27]\n" TR"v[32:33],v4 offset:0\n" TR"v[34:35],v4 offset:4096\n" MF"a[28:31],v[52:55],v[92:95],a[28:31]\n" TR"v[36:37],v5 offset:0\n" TR"v[38:39],v5 offset:4096\n"
 MF"a[32:35],v[56:59],v[80:83],a[32:35]\n" TR"v[40:41],v6 offset:0\n" TR"v[42:43],v6 offset:4096\n" MF"a[36:39],v[56:59],v[84:87],a[36:39]\n" TR"v[44:45],v7 offset:0\n" TR"v[46:47],v7 offset:4096\n"
 MF"a[40:43],v[56:59],v[88:91],a[40:43]\n" MF"a[44:47],v[56:59],v[92:95],a[44:47]\n" MF"a[48:51],v[60:63],v[80:83],a[48:51]\n" MF"a[52:55],v[60:63],v[84:87],a[52:55]\n"
 MF"a[56:59],v[60:63],v[88:91],a[56:59]\n" MF"a[60:63],v[60:63],v[92:95],a[60:63]\n" WL"(6)\n" MF"a[0:3],v[32:35],v[64:67],a[0:3]\n" MF"a[4:7],v[32:35],v[68:71],a[4:7]\n" DR"v[80:83],v8 offset:1024\n"
 MF"a[8:11],v[32:35],v[72:75],a[8:11]\n" DR"v[84:87],v8 offset:3072\n" MF"a[12:15],v[32:35],v[76:79],a[12:15]\n" DR"v[88:91],v8 offset:5120\n" WL"(7)\n" MF"a[16:19],v[36:39],v[64:67],a[16:19]\n"
 DR"v[92:95],v8 offset:7168\n" MF"a[20:23],v[36:39],v[68:71],a[20:23]\n" SA"s59,s46,0\n" BL"a[128:131],v0,s[64:67],s59 offen offset:0 sc1\n" MF"a[24:27],v[36:39],v[72:75],a[24:27]\n"
 TR"v[48:49],v4 offset:8192\n" TR"v[50:51],v4 offset:12288\n" MF"a[28:31],v[36:39],v[76:79],a[28:31]\n" TR"v[52:53],v5 offset:8192\n" TR"v[54:55],v5 offset:12288\n" WL"(10)\n"
 MF"a[32:35],v[40:43],v[64:67],a[32:35]\n" TR"v[56:57],v6 offset:8192\n" TR"v[58:59],v6 offset:12288\n" MF"a[36:39],v[40:43],v[68:71],a[36:39]\n" TR"v[60:61],v7 offset:8192\n"
 TR"v[62:63],v7 offset:12288\n" MF"a[40:43],v[40:43],v[72:75],a[40:43]\n" BL"a[132:135],v0,s[64:67],s59 offen offset:1024 sc1\n" MF"a[44:47],v[40:43],v[76:79],a[44:47]\n" WL"(12)\n"
 MF"a[48:51],v[44:47],v[64:67],a[48:51]\n" MF"a[52:55],v[44:47],v[68:71],a[52:55]\n" BL"a[136:139],v0,s[64:67],s59 offen offset:2048 sc1\n" MF"a[56:59],v[44:47],v[72:75],a[56:59]\n"
 MF"a[60:63],v[44:47],v[76:79],a[60:63]\n" BL"a[140:143],v0,s[64:67],s59 offen offset:3072 sc1\n" WL"(6)\n" MF"a[0:3],v[48:51],v[80:83],a[0:3]\n" WV"(12) lgkmcnt(0)\n""s_barrier\n"
 SA"s84,s52,100663296\n""s_addc_u32 s85,s53,0\n""s_mov_b32 s86,8388608\n""s_mov_b32 s87,0x20000\n""s_lshl_b32 s56,s42,7\n""s_lshl_b32 s57,s39,3\n" SA"s56,s56,s57\n""s_lshr_b32 s57,s36,1\n"
"s_lshl_b32 s57,s57,2\n" SA"s56,s56,s57\n""s_lshl_b32 s56,s56,4\n""s_lshl_b32 s57,s38,2\n" SA"s56,s56,s57\n""s_and_b32 s57,s36,1\n""s_lshl_b32 s57,s57,1\n" SA"s56,s56,s57\n""s_lshl_b32 s56,s56,10\n"
 MF"a[4:7],v[48:51],v[84:87],a[4:7]\n" DR"v[64:67],v8 offset:32768\n" MF"a[8:11],v[48:51],v[88:91],a[8:11]\n" DR"v[68:71],v8 offset:34816\n" MF"a[12:15],v[48:51],v[92:95],a[12:15]\n"
 DR"v[72:75],v8 offset:36864\n" MF"a[16:19],v[52:55],v[80:83],a[16:19]\n" DR"v[76:79],v8 offset:38912\n" MF"a[20:23],v[52:55],v[84:87],a[20:23]\n" SA"s59,s46,4096\n"
 BL"a[144:147],v0,s[64:67],s59 offen offset:0 sc1\n" MF"a[24:27],v[52:55],v[88:91],a[24:27]\n" TR"v[32:33],v4 offset:32768\n" TR"v[34:35],v4 offset:36864\n" MF"a[28:31],v[52:55],v[92:95],a[28:31]\n"
 TR"v[36:37],v5 offset:32768\n" TR"v[38:39],v5 offset:36864\n" MF"a[32:35],v[56:59],v[80:83],a[32:35]\n" TR"v[40:41],v6 offset:32768\n" TR"v[42:43],v6 offset:36864\n"
 MF"a[36:39],v[56:59],v[84:87],a[36:39]\n" TR"v[44:45],v7 offset:32768\n" TR"v[46:47],v7 offset:36864\n" MF"a[40:43],v[56:59],v[88:91],a[40:43]\n"
 BL"a[148:151],v0,s[64:67],s59 offen offset:1024 sc1\n" MF"a[44:47],v[56:59],v[92:95],a[44:47]\n" MF"a[48:51],v[60:63],v[80:83],a[48:51]\n" MF"a[52:55],v[60:63],v[84:87],a[52:55]\n"
 BL"a[152:155],v0,s[64:67],s59 offen offset:2048 sc1\n" MF"a[56:59],v[60:63],v[88:91],a[56:59]\n" MF"a[60:63],v[60:63],v[92:95],a[60:63]\n" BL"a[156:159],v0,s[64:67],s59 offen offset:3072 sc1\n"
 SA"s59,s46,8192\n" BL"a[160:163],v0,s[64:67],s59 offen offset:0 sc1\n" BL"a[164:167],v0,s[64:67],s59 offen offset:1024 sc1\n" BL"a[168:171],v0,s[64:67],s59 offen offset:2048 sc1\n"
 BL"a[172:175],v0,s[64:67],s59 offen offset:3072 sc1\n" WL"(6)\n" MF"a[0:3],v[32:35],v[64:67],a[0:3]\n" MF"a[4:7],v[32:35],v[68:71],a[4:7]\n" DR"v[80:83],v8 offset:33792\n"
 MF"a[8:11],v[32:35],v[72:75],a[8:11]\n" DR"v[84:87],v8 offset:35840\n" MF"a[12:15],v[32:35],v[76:79],a[12:15]\n" DR"v[88:91],v8 offset:37888\n" WL"(7)\n" MF"a[16:19],v[36:39],v[64:67],a[16:19]\n"
 DR"v[92:95],v8 offset:39936\n" MF"a[20:23],v[36:39],v[68:71],a[20:23]\n" SA"s59,s46,12288\n" BL"a[176:179],v0,s[64:67],s59 offen offset:0 sc1\n" MF"a[24:27],v[36:39],v[72:75],a[24:27]\n"
 TR"v[48:49],v4 offset:40960\n" TR"v[50:51],v4 offset:45056\n" MF"a[28:31],v[36:39],v[76:79],a[28:31]\n" TR"v[52:53],v5 offset:40960\n" TR"v[54:55],v5 offset:45056\n" WL"(10)\n"
 MF"a[32:35],v[40:43],v[64:67],a[32:35]\n" TR"v[56:57],v6 offset:40960\n" TR"v[58:59],v6 offset:45056\n" MF"a[36:39],v[40:43],v[68:71],a[36:39]\n" TR"v[60:61],v7 offset:40960\n"
 TR"v[62:63],v7 offset:45056\n" MF"a[40:43],v[40:43],v[72:75],a[40:43]\n" BL"a[180:183],v0,s[64:67],s59 offen offset:1024 sc1\n" MF"a[44:47],v[40:43],v[76:79],a[44:47]\n" WL"(12)\n"
 MF"a[48:51],v[44:47],v[64:67],a[48:51]\n" MF"a[52:55],v[44:47],v[68:71],a[52:55]\n" BL"a[184:187],v0,s[64:67],s59 offen offset:2048 sc1\n" MF"a[56:59],v[44:47],v[72:75],a[56:59]\n"
 MF"a[60:63],v[44:47],v[76:79],a[60:63]\n" BL"a[188:191],v0,s[64:67],s59 offen offset:3072 sc1\n" WL"(6)\n" MF"a[0:3],v[48:51],v[80:83],a[0:3]\n" MF"a[4:7],v[48:51],v[84:87],a[4:7]\n"
 MF"a[8:11],v[48:51],v[88:91],a[8:11]\n" MF"a[12:15],v[48:51],v[92:95],a[12:15]\n" WL"(4)\n" MF"a[16:19],v[52:55],v[80:83],a[16:19]\n" MF"a[20:23],v[52:55],v[84:87],a[20:23]\n" SA"s59,s46,16384\n"
 BL"a[192:195],v0,s[64:67],s59 offen offset:0 sc1\n" MF"a[24:27],v[52:55],v[88:91],a[24:27]\n" MF"a[28:31],v[52:55],v[92:95],a[28:31]\n" WL"(2)\n" MF"a[32:35],v[56:59],v[80:83],a[32:35]\n"
 MF"a[36:39],v[56:59],v[84:87],a[36:39]\n" MF"a[40:43],v[56:59],v[88:91],a[40:43]\n" BL"a[196:199],v0,s[64:67],s59 offen offset:1024 sc1\n" MF"a[44:47],v[56:59],v[92:95],a[44:47]\n" WL"(0)\n"
 MF"a[48:51],v[60:63],v[80:83],a[48:51]\n" MF"a[52:55],v[60:63],v[84:87],a[52:55]\n" BL"a[200:203],v0,s[64:67],s59 offen offset:2048 sc1\n" MF"a[56:59],v[60:63],v[88:91],a[56:59]\n"
 MF"a[60:63],v[60:63],v[92:95],a[60:63]\n" BL"a[204:207],v0,s[64:67],s59 offen offset:3072 sc1\n" SA"s59,s46,20480\n" BL"a[208:211],v0,s[64:67],s59 offen offset:0 sc1\n"
 BL"a[212:215],v0,s[64:67],s59 offen offset:1024 sc1\n" BL"a[216:219],v0,s[64:67],s59 offen offset:2048 sc1\n" BL"a[220:223],v0,s[64:67],s59 offen offset:3072 sc1\n" WL"(0)\n""s_barrier\n"
 SA"m0,s62,0\n" SA"s58,s44,262144\n" BL"v14,s[80:83],s58 offen lds\n" SA"m0,s62,1024\n" SA"s58,s44,270336\n" BL"v14,s[80:83],s58 offen lds\n" SA"m0,s62,2048\n" SA"s58,s44,278528\n"
 BL"v14,s[80:83],s58 offen lds\n" SA"m0,s62,3072\n" SA"s58,s44,286720\n" BL"v14,s[80:83],s58 offen lds\n" AR"v32,a0\n" AR"v33,a1\n" AR"v34,a2\n" AR"v35,a3\n" AR"v36,a16\n" AR"v37,a17\n" AR"v38,a18\n"
 AR"v39,a19\n" CV"v40,v32,v33\n" CV"v41,v34,v35\n" CV"v42,v36,v37\n" CV"v43,v38,v39\n" SA"s45,s56,0\n" BS"v[40:43],v0,s[84:87],s45 offen offset:0 sc1\n" AR"v44,a32\n" AR"v45,a33\n" AR"v46,a34\n"
 AR"v47,a35\n" AR"v48,a48\n" AR"v49,a49\n" AR"v50,a50\n" AR"v51,a51\n" CV"v52,v44,v45\n" CV"v53,v46,v47\n" CV"v54,v48,v49\n" CV"v55,v50,v51\n" BS"v[52:55],v0,s[84:87],s45 offen offset:1024 sc1\n"
 SA"m0,s62,16384\n" SA"s58,s44,393216\n" BL"v14,s[80:83],s58 offen lds\n" SA"m0,s62,17408\n" SA"s58,s44,401408\n" BL"v14,s[80:83],s58 offen lds\n" SA"m0,s62,18432\n" SA"s58,s44,409600\n"
 BL"v14,s[80:83],s58 offen lds\n" SA"m0,s62,19456\n" SA"s58,s44,417792\n" BL"v14,s[80:83],s58 offen lds\n" AR"v32,a4\n" AR"v33,a5\n" AR"v34,a6\n" AR"v35,a7\n" AR"v36,a20\n" AR"v37,a21\n" AR"v38,a22\n"
 AR"v39,a23\n" CV"v40,v32,v33\n" CV"v41,v34,v35\n" CV"v42,v36,v37\n" CV"v43,v38,v39\n" SA"s45,s56,16384\n" BS"v[40:43],v0,s[84:87],s45 offen offset:0 sc1\n" AR"v44,a36\n" AR"v45,a37\n" AR"v46,a38\n"
 AR"v47,a39\n" AR"v48,a52\n" AR"v49,a53\n" AR"v50,a54\n" AR"v51,a55\n" CV"v52,v44,v45\n" CV"v53,v46,v47\n" CV"v54,v48,v49\n" CV"v55,v50,v51\n" BS"v[52:55],v0,s[84:87],s45 offen offset:1024 sc1\n"
 SA"m0,s62,32768\n" SA"s58,s44,524288\n" BL"v14,s[80:83],s58 offen lds\n" SA"m0,s62,33792\n" SA"s58,s44,532480\n" BL"v14,s[80:83],s58 offen lds\n" SA"m0,s62,34816\n" SA"s58,s44,540672\n"
 BL"v14,s[80:83],s58 offen lds\n" SA"m0,s62,35840\n" SA"s58,s44,548864\n" BL"v14,s[80:83],s58 offen lds\n" AR"v32,a8\n" AR"v33,a9\n" AR"v34,a10\n" AR"v35,a11\n" AR"v36,a24\n" AR"v37,a25\n"
 AR"v38,a26\n" AR"v39,a27\n" CV"v40,v32,v33\n" CV"v41,v34,v35\n" CV"v42,v36,v37\n" CV"v43,v38,v39\n" SA"s45,s56,32768\n" BS"v[40:43],v0,s[84:87],s45 offen offset:0 sc1\n" AR"v44,a40\n" AR"v45,a41\n"
 AR"v46,a42\n" AR"v47,a43\n" AR"v48,a56\n" AR"v49,a57\n" AR"v50,a58\n" AR"v51,a59\n" CV"v52,v44,v45\n" CV"v53,v46,v47\n" CV"v54,v48,v49\n" CV"v55,v50,v51\n"
 BS"v[52:55],v0,s[84:87],s45 offen offset:1024 sc1\n" SA"m0,s62,49152\n" SA"s58,s44,655360\n" BL"v14,s[80:83],s58 offen lds\n" SA"m0,s62,50176\n" SA"s58,s44,663552\n" BL"v14,s[80:83],s58 offen lds\n"
 SA"m0,s62,51200\n" SA"s58,s44,671744\n" BL"v14,s[80:83],s58 offen lds\n" SA"m0,s62,52224\n" SA"s58,s44,679936\n" BL"v14,s[80:83],s58 offen lds\n" AR"v32,a12\n" AR"v33,a13\n" AR"v34,a14\n"
 AR"v35,a15\n" AR"v36,a28\n" AR"v37,a29\n" AR"v38,a30\n" AR"v39,a31\n" CV"v40,v32,v33\n" CV"v41,v34,v35\n" CV"v42,v36,v37\n" CV"v43,v38,v39\n" SA"s45,s56,49152\n"
 BS"v[40:43],v0,s[84:87],s45 offen offset:0 sc1\n" AR"v44,a44\n" AR"v45,a45\n" AR"v46,a46\n" AR"v47,a47\n" AR"v48,a60\n" AR"v49,a61\n" AR"v50,a62\n" AR"v51,a63\n" CV"v52,v44,v45\n" CV"v53,v46,v47\n"
 CV"v54,v48,v49\n" CV"v55,v50,v51\n" BS"v[52:55],v0,s[84:87],s45 offen offset:1024 sc1\n""v_mbcnt_lo_u32_b32 v20,-1,0\n""v_mbcnt_hi_u32_b32 v20,-1,v20\n""v_lshrrev_b32 v22,4,v20\n"
"v_bfe_u32 v23,v20,2,2\n""v_lshl_add_u32 v23,v22,2,v23\n""v_lshlrev_b32 v21,7,v23\n""v_and_b32 v22,3,v20\n""v_lshl_add_u32 v21,v22,3,v21\n""v_bfe_u32 v23,v23,1,2\n""v_xor_b32 v22,0,v23\n"
"v_lshl_add_u32 v4,v22,5,v21\n""v_add_u32 v8,0x10000,v4\n""v_add_u32 v16,0x20000,v4\n""v_xor_b32 v22,1,v23\n""v_lshl_add_u32 v5,v22,5,v21\n""v_add_u32 v9,0x10000,v5\n""v_add_u32 v17,0x20000,v5\n"
"v_xor_b32 v22,2,v23\n""v_lshl_add_u32 v6,v22,5,v21\n""v_add_u32 v10,0x10000,v6\n""v_add_u32 v18,0x20000,v6\n""v_xor_b32 v22,3,v23\n""v_lshl_add_u32 v7,v22,5,v21\n""v_add_u32 v11,0x10000,v7\n"
"v_add_u32 v19,0x20000,v7\n" SA"s72,s52,5242880\n""s_addc_u32 s73,s53,0\n""s_mov_b32 s74,2097152\n""s_mov_b32 s75,0x20000\n" SA"s76,s52,7340032\n""s_addc_u32 s77,s53,0\n""s_mov_b32 s78,2097152\n"
"s_mov_b32 s79,0x20000\n" BL"v[56:59],v0,s[72:75],s47 offen sc1\n" BL"v[60:63],v0,s[72:75],s47 offen offset:1024 sc1\n" BL"v[24:27],v0,s[76:79],s47 offen sc1\n"
 BL"v[28:31],v0,s[76:79],s47 offen offset:1024 sc1\n" WV"(48)\n""s_barrier\n" TR"v[96:97],v16 offset:0\n" TR"v[98:99],v16 offset:2048\n" TR"v[100:101],v17 offset:0\n" TR"v[102:103],v17 offset:2048\n"
 TR"v[104:105],v18 offset:0\n" TR"v[106:107],v18 offset:2048\n" TR"v[108:109],v19 offset:0\n" TR"v[110:111],v19 offset:2048\n" TR"v[112:113],v16 offset:4096\n" TR"v[114:115],v16 offset:6144\n"
 TR"v[116:117],v17 offset:4096\n" TR"v[118:119],v17 offset:6144\n" TR"v[120:121],v18 offset:4096\n" TR"v[122:123],v18 offset:6144\n" TR"v[124:125],v19 offset:4096\n" WL"(8)\n"
 TR"v[126:127],v19 offset:6144\n" TR"v[128:129],v16 offset:8192\n" TR"v[130:131],v16 offset:10240\n" TR"v[132:133],v17 offset:8192\n" TR"v[134:135],v17 offset:10240\n" TR"v[136:137],v18 offset:8192\n"
 TR"v[138:139],v18 offset:10240\n" WL"(8)\n" TR"v[140:141],v19 offset:8192\n" TR"v[142:143],v19 offset:10240\n" TR"v[144:145],v16 offset:12288\n" TR"v[146:147],v16 offset:14336\n"
 TR"v[148:149],v17 offset:12288\n" TR"v[150:151],v17 offset:14336\n" TR"v[152:153],v18 offset:12288\n" WL"(8)\n" TR"v[154:155],v18 offset:14336\n" TR"v[156:157],v19 offset:12288\n"
 TR"v[158:159],v19 offset:14336\n" WV"(44)\n""s_barrier\n" MF"v[224:227],v[96:99],a[128:131],0\n" TR"v[160:161],v16 offset:16384\n" TR"v[162:163],v16 offset:18432\n"
 MF"v[228:231],v[100:103],a[128:131],0\n" TR"v[164:165],v17 offset:16384\n" TR"v[166:167],v17 offset:18432\n" SA"m0,s62,65536\n" SA"s58,s44,786432\n" BL"v14,s[80:83],s58 offen lds\n"
 MF"v[232:235],v[104:107],a[128:131],0\n" WL"(8)\n" TR"v[168:169],v18 offset:16384\n" TR"v[170:171],v18 offset:18432\n" MF"v[236:239],v[108:111],a[128:131],0\n" TR"v[172:173],v19 offset:16384\n"
 TR"v[174:175],v19 offset:18432\n" SA"m0,s62,66560\n" SA"s58,s44,794624\n" BL"v14,s[80:83],s58 offen lds\n" MF"v[224:227],v[112:115],a[132:135],v[224:227]\n" TR"v[176:177],v16 offset:20480\n"
 TR"v[178:179],v16 offset:22528\n" MF"v[228:231],v[116:119],a[132:135],v[228:231]\n" TR"v[180:181],v17 offset:20480\n" WL"(8)\n" TR"v[182:183],v17 offset:22528\n" SA"m0,s62,67584\n"
 SA"s58,s44,802816\n" BL"v14,s[80:83],s58 offen lds\n" MF"v[232:235],v[120:123],a[132:135],v[232:235]\n" TR"v[184:185],v18 offset:20480\n" TR"v[186:187],v18 offset:22528\n"
 MF"v[236:239],v[124:127],a[132:135],v[236:239]\n" TR"v[188:189],v19 offset:20480\n" TR"v[190:191],v19 offset:22528\n" SA"m0,s62,68608\n" SA"s58,s44,811008\n" BL"v14,s[80:83],s58 offen lds\n"
 MF"v[224:227],v[128:131],a[136:139],v[224:227]\n" TR"v[192:193],v16 offset:24576\n" TR"v[194:195],v16 offset:26624\n" MF"v[228:231],v[132:135],a[136:139],v[228:231]\n" WL"(8)\n"
 TR"v[196:197],v17 offset:24576\n" TR"v[198:199],v17 offset:26624\n" SA"s59,s46,24576\n" BL"a[224:227],v0,s[64:67],s59 offen offset:0 sc1\n" MF"v[232:235],v[136:139],a[136:139],v[232:235]\n"
 TR"v[200:201],v18 offset:24576\n" TR"v[202:203],v18 offset:26624\n" MF"v[236:239],v[140:143],a[136:139],v[236:239]\n" TR"v[204:205],v19 offset:24576\n" TR"v[206:207],v19 offset:26624\n"
 BL"a[228:231],v0,s[64:67],s59 offen offset:1024 sc1\n" MF"v[224:227],v[144:147],a[140:143],v[224:227]\n" TR"v[208:209],v16 offset:28672\n" WL"(8)\n" TR"v[210:211],v16 offset:30720\n"
 MF"v[228:231],v[148:151],a[140:143],v[228:231]\n" TR"v[212:213],v17 offset:28672\n" TR"v[214:215],v17 offset:30720\n" BL"a[232:235],v0,s[64:67],s59 offen offset:2048 sc1\n"
 MF"v[232:235],v[152:155],a[140:143],v[232:235]\n" TR"v[216:217],v18 offset:28672\n" TR"v[218:219],v18 offset:30720\n" MF"v[236:239],v[156:159],a[140:143],v[236:239]\n"
 TR"v[220:221],v19 offset:28672\n" TR"v[222:223],v19 offset:30720\n" BL"a[236:239],v0,s[64:67],s59 offen offset:3072 sc1\n" WV"(32)\n""s_barrier\n" MF"v[224:227],v[160:163],a[144:147],v[224:227]\n"
 WL"(8)\n" TR"v[96:97],v4 offset:0\n" TR"v[98:99],v4 offset:2048\n" MF"v[228:231],v[164:167],a[144:147],v[228:231]\n" TR"v[100:101],v5 offset:0\n" TR"v[102:103],v5 offset:2048\n" SA"m0,s62,81920\n"
 SA"s58,s44,917504\n" BL"v14,s[80:83],s58 offen lds\n" MF"v[232:235],v[168:171],a[144:147],v[232:235]\n" TR"v[104:105],v6 offset:0\n" TR"v[106:107],v6 offset:2048\n"
 MF"v[236:239],v[172:175],a[144:147],v[236:239]\n" TR"v[108:109],v7 offset:0\n" WL"(8)\n" TR"v[110:111],v7 offset:2048\n" SA"m0,s62,82944\n" SA"s58,s44,925696\n" BL"v14,s[80:83],s58 offen lds\n"
 MF"v[224:227],v[176:179],a[148:151],v[224:227]\n" TR"v[112:113],v4 offset:4096\n" TR"v[114:115],v4 offset:6144\n" MF"v[228:231],v[180:183],a[148:151],v[228:231]\n" TR"v[116:117],v5 offset:4096\n"
 TR"v[118:119],v5 offset:6144\n" SA"m0,s62,83968\n" SA"s58,s44,933888\n" BL"v14,s[80:83],s58 offen lds\n" MF"v[232:235],v[184:187],a[148:151],v[232:235]\n" TR"v[120:121],v6 offset:4096\n"
 TR"v[122:123],v6 offset:6144\n" MF"v[236:239],v[188:191],a[148:151],v[236:239]\n" WL"(8)\n" TR"v[124:125],v7 offset:4096\n" TR"v[126:127],v7 offset:6144\n" SA"m0,s62,84992\n" SA"s58,s44,942080\n"
 BL"v14,s[80:83],s58 offen lds\n" MF"v[224:227],v[192:195],a[152:155],v[224:227]\n" TR"v[128:129],v4 offset:8192\n" TR"v[130:131],v4 offset:10240\n" MF"v[228:231],v[196:199],a[152:155],v[228:231]\n"
 TR"v[132:133],v5 offset:8192\n" TR"v[134:135],v5 offset:10240\n" SA"s59,s46,28672\n" BL"a[240:243],v0,s[64:67],s59 offen offset:0 sc1\n" MF"v[232:235],v[200:203],a[152:155],v[232:235]\n"
 TR"v[136:137],v6 offset:8192\n" WL"(8)\n" TR"v[138:139],v6 offset:10240\n" MF"v[236:239],v[204:207],a[152:155],v[236:239]\n" TR"v[140:141],v7 offset:8192\n" TR"v[142:143],v7 offset:10240\n"
 BL"a[244:247],v0,s[64:67],s59 offen offset:1024 sc1\n" MF"v[224:227],v[208:211],a[156:159],v[224:227]\n" TR"v[144:145],v4 offset:12288\n" TR"v[146:147],v4 offset:14336\n"
 MF"v[228:231],v[212:215],a[156:159],v[228:231]\n" TR"v[148:149],v5 offset:12288\n" TR"v[150:151],v5 offset:14336\n" BL"a[248:251],v0,s[64:67],s59 offen offset:2048 sc1\n"
 MF"v[232:235],v[216:219],a[156:159],v[232:235]\n" WL"(8)\n" TR"v[152:153],v6 offset:12288\n" TR"v[154:155],v6 offset:14336\n" MF"v[236:239],v[220:223],a[156:159],v[236:239]\n"
 TR"v[156:157],v7 offset:12288\n" TR"v[158:159],v7 offset:14336\n" BL"a[252:255],v0,s[64:67],s59 offen offset:3072 sc1\n" WV"(20)\n""s_barrier\n""s_and_b32 s57,s96,3\n""s_bfe_u32 s58,s96,0x10002\n"
"s_lshl_b32 s58,s58,7\n" SA"s57,s57,s58\n""s_bfe_u32 s58,s96,0x30003\n""s_lshl_b32 s58,s58,4\n" SA"s57,s57,s58\n""s_lshr_b32 s58,s96,6\n""s_lshl_b32 s58,s58,2\n" SA"s57,s57,s58\n"
"s_lshl_b32 s57,s57,3\n" SA"s57,s57,28672\n" VV"v244,s98\n" VV"v245,s99\n" VV"v246,0\n""buffer_store_dwordx2 v[244:245],v246,s[92:95],s57 offen sc1\n" MF"v[224:227],v[96:99],a[160:163],v[224:227]\n"
 TR"v[160:161],v4 offset:16384\n" TR"v[162:163],v4 offset:18432\n" MF"v[228:231],v[100:103],a[160:163],v[228:231]\n" TR"v[164:165],v5 offset:16384\n" WL"(8)\n" TR"v[166:167],v5 offset:18432\n"
 SA"m0,s62,131072\n" SA"s58,s44,1048576\n" BL"v14,s[80:83],s58 offen lds\n" MF"v[232:235],v[104:107],a[160:163],v[232:235]\n" TR"v[168:169],v6 offset:16384\n" TR"v[170:171],v6 offset:18432\n"
 MF"v[236:239],v[108:111],a[160:163],v[236:239]\n" TR"v[172:173],v7 offset:16384\n" TR"v[174:175],v7 offset:18432\n" SA"m0,s62,132096\n" SA"s58,s44,1056768\n" BL"v14,s[80:83],s58 offen lds\n"
 MF"v[224:227],v[112:115],a[164:167],v[224:227]\n" TR"v[176:177],v4 offset:20480\n" TR"v[178:179],v4 offset:22528\n" MF"v[228:231],v[116:119],a[164:167],v[228:231]\n" WL"(8)\n"
 TR"v[180:181],v5 offset:20480\n" TR"v[182:183],v5 offset:22528\n" SA"m0,s62,133120\n" SA"s58,s44,1064960\n" BL"v14,s[80:83],s58 offen lds\n" MF"v[232:235],v[120:123],a[164:167],v[232:235]\n"
 TR"v[184:185],v6 offset:20480\n" TR"v[186:187],v6 offset:22528\n" MF"v[236:239],v[124:127],a[164:167],v[236:239]\n" TR"v[188:189],v7 offset:20480\n" TR"v[190:191],v7 offset:22528\n"
 SA"m0,s62,134144\n" SA"s58,s44,1073152\n" BL"v14,s[80:83],s58 offen lds\n" MF"v[224:227],v[128:131],a[168:171],v[224:227]\n" TR"v[192:193],v4 offset:24576\n" WL"(8)\n"
 TR"v[194:195],v4 offset:26624\n" MF"v[228:231],v[132:135],a[168:171],v[228:231]\n" TR"v[196:197],v5 offset:24576\n" TR"v[198:199],v5 offset:26624\n" SA"s59,s46,32768\n"
 BL"a[128:131],v0,s[64:67],s59 offen offset:0 sc1\n" MF"v[232:235],v[136:139],a[168:171],v[232:235]\n" TR"v[200:201],v6 offset:24576\n" TR"v[202:203],v6 offset:26624\n"
 MF"v[236:239],v[140:143],a[168:171],v[236:239]\n" TR"v[204:205],v7 offset:24576\n" TR"v[206:207],v7 offset:26624\n" BL"a[132:135],v0,s[64:67],s59 offen offset:1024 sc1\n"
 MF"v[224:227],v[144:147],a[172:175],v[224:227]\n" WL"(8)\n" TR"v[208:209],v4 offset:28672\n" TR"v[210:211],v4 offset:30720\n" MF"v[228:231],v[148:151],a[172:175],v[228:231]\n"
 TR"v[212:213],v5 offset:28672\n" TR"v[214:215],v5 offset:30720\n" BL"a[136:139],v0,s[64:67],s59 offen offset:2048 sc1\n" MF"v[232:235],v[152:155],a[172:175],v[232:235]\n"
 TR"v[216:217],v6 offset:28672\n" TR"v[218:219],v6 offset:30720\n" MF"v[236:239],v[156:159],a[172:175],v[236:239]\n" TR"v[220:221],v7 offset:28672\n" WL"(8)\n" TR"v[222:223],v7 offset:30720\n"
 BL"a[140:143],v0,s[64:67],s59 offen offset:3072 sc1\n" WV"(37)\n""s_barrier\n" MF"v[224:227],v[160:163],a[176:179],v[224:227]\n" TR"v[96:97],v4 offset:32768\n" TR"v[98:99],v4 offset:34816\n"
 MF"v[228:231],v[164:167],a[176:179],v[228:231]\n" TR"v[100:101],v5 offset:32768\n" TR"v[102:103],v5 offset:34816\n" SA"m0,s62,147456\n" SA"s58,s44,1179648\n" BL"v14,s[80:83],s58 offen lds\n"
 MF"v[232:235],v[168:171],a[176:179],v[232:235]\n" TR"v[104:105],v6 offset:32768\n" TR"v[106:107],v6 offset:34816\n" MF"v[236:239],v[172:175],a[176:179],v[236:239]\n" WL"(8)\n"
 TR"v[108:109],v7 offset:32768\n" TR"v[110:111],v7 offset:34816\n" SA"m0,s62,148480\n" SA"s58,s44,1187840\n" BL"v14,s[80:83],s58 offen lds\n" MF"v[224:227],v[176:179],a[180:183],v[224:227]\n"
 TR"v[112:113],v4 offset:36864\n" TR"v[114:115],v4 offset:38912\n" MF"v[228:231],v[180:183],a[180:183],v[228:231]\n" TR"v[116:117],v5 offset:36864\n" TR"v[118:119],v5 offset:38912\n"
 SA"m0,s62,149504\n" SA"s58,s44,1196032\n" BL"v14,s[80:83],s58 offen lds\n" MF"v[232:235],v[184:187],a[180:183],v[232:235]\n" TR"v[120:121],v6 offset:36864\n" WL"(8)\n"
 TR"v[122:123],v6 offset:38912\n" MF"v[236:239],v[188:191],a[180:183],v[236:239]\n" TR"v[124:125],v7 offset:36864\n" TR"v[126:127],v7 offset:38912\n" SA"m0,s62,150528\n" SA"s58,s44,1204224\n"
 BL"v14,s[80:83],s58 offen lds\n" MF"v[224:227],v[192:195],a[184:187],v[224:227]\n" TR"v[128:129],v4 offset:40960\n" TR"v[130:131],v4 offset:43008\n" MF"v[228:231],v[196:199],a[184:187],v[228:231]\n"
 TR"v[132:133],v5 offset:40960\n" TR"v[134:135],v5 offset:43008\n" SA"s59,s46,36864\n" BL"a[144:147],v0,s[64:67],s59 offen offset:0 sc1\n" MF"v[232:235],v[200:203],a[184:187],v[232:235]\n" WL"(8)\n"
 TR"v[136:137],v6 offset:40960\n" TR"v[138:139],v6 offset:43008\n" MF"v[236:239],v[204:207],a[184:187],v[236:239]\n" TR"v[140:141],v7 offset:40960\n" TR"v[142:143],v7 offset:43008\n"
 BL"a[148:151],v0,s[64:67],s59 offen offset:1024 sc1\n" MF"v[224:227],v[208:211],a[188:191],v[224:227]\n" TR"v[144:145],v4 offset:45056\n" TR"v[146:147],v4 offset:47104\n"
 MF"v[228:231],v[212:215],a[188:191],v[228:231]\n" TR"v[148:149],v5 offset:45056\n" WL"(8)\n" TR"v[150:151],v5 offset:47104\n" BL"a[152:155],v0,s[64:67],s59 offen offset:2048 sc1\n"
 MF"v[232:235],v[216:219],a[188:191],v[232:235]\n" TR"v[152:153],v6 offset:45056\n" TR"v[154:155],v6 offset:47104\n" MF"v[236:239],v[220:223],a[188:191],v[236:239]\n" TR"v[156:157],v7 offset:45056\n"
 TR"v[158:159],v7 offset:47104\n" BL"a[156:159],v0,s[64:67],s59 offen offset:3072 sc1\n" WV"(39)\n""s_barrier\n" MF"v[224:227],v[96:99],a[192:195],v[224:227]\n" TR"v[160:161],v4 offset:49152\n"
 TR"v[162:163],v4 offset:51200\n" MF"v[228:231],v[100:103],a[192:195],v[228:231]\n" WL"(8)\n" TR"v[164:165],v5 offset:49152\n" TR"v[166:167],v5 offset:51200\n" SA"m0,s62,0\n" SA"s58,s44,1310720\n"
 BL"v14,s[80:83],s58 offen lds\n" MF"v[232:235],v[104:107],a[192:195],v[232:235]\n" TR"v[168:169],v6 offset:49152\n" TR"v[170:171],v6 offset:51200\n" MF"v[236:239],v[108:111],a[192:195],v[236:239]\n"
 TR"v[172:173],v7 offset:49152\n" TR"v[174:175],v7 offset:51200\n" SA"m0,s62,1024\n" SA"s58,s44,1318912\n" BL"v14,s[80:83],s58 offen lds\n" MF"v[224:227],v[112:115],a[196:199],v[224:227]\n"
 TR"v[176:177],v4 offset:53248\n" WL"(8)\n" TR"v[178:179],v4 offset:55296\n" MF"v[228:231],v[116:119],a[196:199],v[228:231]\n" TR"v[180:181],v5 offset:53248\n" TR"v[182:183],v5 offset:55296\n"
 SA"m0,s62,2048\n" SA"s58,s44,1327104\n" BL"v14,s[80:83],s58 offen lds\n" MF"v[232:235],v[120:123],a[196:199],v[232:235]\n" TR"v[184:185],v6 offset:53248\n" TR"v[186:187],v6 offset:55296\n"
 MF"v[236:239],v[124:127],a[196:199],v[236:239]\n" TR"v[188:189],v7 offset:53248\n" TR"v[190:191],v7 offset:55296\n" SA"m0,s62,3072\n" SA"s58,s44,1335296\n" BL"v14,s[80:83],s58 offen lds\n"
 MF"v[224:227],v[128:131],a[200:203],v[224:227]\n" WL"(8)\n" TR"v[192:193],v4 offset:57344\n" TR"v[194:195],v4 offset:59392\n" MF"v[228:231],v[132:135],a[200:203],v[228:231]\n"
 TR"v[196:197],v5 offset:57344\n" TR"v[198:199],v5 offset:59392\n" SA"s59,s46,40960\n" BL"a[160:163],v0,s[64:67],s59 offen offset:0 sc1\n" MF"v[232:235],v[136:139],a[200:203],v[232:235]\n"
 TR"v[200:201],v6 offset:57344\n" TR"v[202:203],v6 offset:59392\n" MF"v[236:239],v[140:143],a[200:203],v[236:239]\n" TR"v[204:205],v7 offset:57344\n" WL"(8)\n" TR"v[206:207],v7 offset:59392\n"
 BL"a[164:167],v0,s[64:67],s59 offen offset:1024 sc1\n" MF"v[224:227],v[144:147],a[204:207],v[224:227]\n" TR"v[208:209],v4 offset:61440\n" TR"v[210:211],v4 offset:63488\n"
 MF"v[228:231],v[148:151],a[204:207],v[228:231]\n" TR"v[212:213],v5 offset:61440\n" TR"v[214:215],v5 offset:63488\n" BL"a[168:171],v0,s[64:67],s59 offen offset:2048 sc1\n"
 MF"v[232:235],v[152:155],a[204:207],v[232:235]\n" TR"v[216:217],v6 offset:61440\n" TR"v[218:219],v6 offset:63488\n" MF"v[236:239],v[156:159],a[204:207],v[236:239]\n" WL"(8)\n"
 TR"v[220:221],v7 offset:61440\n" TR"v[222:223],v7 offset:63488\n" WV"(32)\n""s_barrier\n" MF"v[224:227],v[160:163],a[208:211],v[224:227]\n" TR"v[96:97],v8 offset:0\n" TR"v[98:99],v8 offset:2048\n"
 MF"v[228:231],v[164:167],a[208:211],v[228:231]\n" TR"v[100:101],v9 offset:0\n" TR"v[102:103],v9 offset:2048\n""v_mbcnt_lo_u32_b32 v251,-1,0\n""v_mbcnt_hi_u32_b32 v251,-1,v251\n"
"v_min_u32 v251,3,v251\n""s_lshr_b32 s57,s49,1\n""s_lshl_b32 s57,s57,4\n""s_lshr_b32 s58,s48,1\n""s_lshl_b32 s58,s58,2\n" SA"s57,s57,s58\n""s_lshl_b32 s57,s57,3\n" SA"s57,s57,28672\n" VV"v248,s57\n"
"v_lshl_add_u32 v248,v251,3,v248\n""buffer_load_dwordx2 v[252:253],v248,s[92:95],0 offen sc1\n" MF"v[232:235],v[168:171],a[208:211],v[232:235]\n" TR"v[104:105],v10 offset:0\n" WL"(8)\n"
 TR"v[106:107],v10 offset:2048\n" MF"v[236:239],v[172:175],a[208:211],v[236:239]\n" TR"v[108:109],v11 offset:0\n" TR"v[110:111],v11 offset:2048\n" MF"v[224:227],v[176:179],a[212:215],v[224:227]\n"
 TR"v[112:113],v8 offset:4096\n" TR"v[114:115],v8 offset:6144\n" MF"v[228:231],v[180:183],a[212:215],v[228:231]\n" TR"v[116:117],v9 offset:4096\n" TR"v[118:119],v9 offset:6144\n"
 MF"v[232:235],v[184:187],a[212:215],v[232:235]\n" WL"(8)\n" TR"v[120:121],v10 offset:4096\n" TR"v[122:123],v10 offset:6144\n" MF"v[236:239],v[188:191],a[212:215],v[236:239]\n"
 TR"v[124:125],v11 offset:4096\n" TR"v[126:127],v11 offset:6144\n" MF"v[224:227],v[192:195],a[216:219],v[224:227]\n" TR"v[128:129],v8 offset:8192\n" TR"v[130:131],v8 offset:10240\n"
 MF"v[228:231],v[196:199],a[216:219],v[228:231]\n" TR"v[132:133],v9 offset:8192\n" WL"(8)\n" TR"v[134:135],v9 offset:10240\n" MF"v[232:235],v[200:203],a[216:219],v[232:235]\n"
 TR"v[136:137],v10 offset:8192\n" TR"v[138:139],v10 offset:10240\n" MF"v[236:239],v[204:207],a[216:219],v[236:239]\n" TR"v[140:141],v11 offset:8192\n" TR"v[142:143],v11 offset:10240\n"
 MF"v[224:227],v[208:211],a[220:223],v[224:227]\n" TR"v[144:145],v8 offset:12288\n" TR"v[146:147],v8 offset:14336\n" MF"v[228:231],v[212:215],a[220:223],v[228:231]\n" WL"(8)\n"
 TR"v[148:149],v9 offset:12288\n" TR"v[150:151],v9 offset:14336\n" MF"v[232:235],v[216:219],a[220:223],v[232:235]\n" TR"v[152:153],v10 offset:12288\n" TR"v[154:155],v10 offset:14336\n"
 MF"v[236:239],v[220:223],a[220:223],v[236:239]\n" TR"v[156:157],v11 offset:12288\n" TR"v[158:159],v11 offset:14336\n" WV"(25)\n""s_barrier\n" MF"v[224:227],v[96:99],a[224:227],v[224:227]\n"
 TR"v[160:161],v8 offset:16384\n" WL"(8)\n" TR"v[162:163],v8 offset:18432\n" MF"v[228:231],v[100:103],a[224:227],v[228:231]\n" TR"v[164:165],v9 offset:16384\n" TR"v[166:167],v9 offset:18432\n"
 MF"v[232:235],v[104:107],a[224:227],v[232:235]\n" TR"v[168:169],v10 offset:16384\n" TR"v[170:171],v10 offset:18432\n" MF"v[236:239],v[108:111],a[224:227],v[236:239]\n"
 TR"v[172:173],v11 offset:16384\n" TR"v[174:175],v11 offset:18432\n" MF"v[224:227],v[112:115],a[228:231],v[224:227]\n" WL"(8)\n" TR"v[176:177],v8 offset:20480\n" TR"v[178:179],v8 offset:22528\n"
 MF"v[228:231],v[116:119],a[228:231],v[228:231]\n" TR"v[180:181],v9 offset:20480\n" TR"v[182:183],v9 offset:22528\n" MF"v[232:235],v[120:123],a[228:231],v[232:235]\n" TR"v[184:185],v10 offset:20480\n"
 TR"v[186:187],v10 offset:22528\n" MF"v[236:239],v[124:127],a[228:231],v[236:239]\n" TR"v[188:189],v11 offset:20480\n" WL"(8)\n" TR"v[190:191],v11 offset:22528\n"
 MF"v[224:227],v[128:131],a[232:235],v[224:227]\n" TR"v[192:193],v8 offset:24576\n" TR"v[194:195],v8 offset:26624\n" MF"v[228:231],v[132:135],a[232:235],v[228:231]\n" TR"v[196:197],v9 offset:24576\n"
 TR"v[198:199],v9 offset:26624\n" MF"v[232:235],v[136:139],a[232:235],v[232:235]\n" TR"v[200:201],v10 offset:24576\n" TR"v[202:203],v10 offset:26624\n"
 MF"v[236:239],v[140:143],a[232:235],v[236:239]\n" WL"(8)\n" TR"v[204:205],v11 offset:24576\n" TR"v[206:207],v11 offset:26624\n" MF"v[224:227],v[144:147],a[236:239],v[224:227]\n"
 TR"v[208:209],v8 offset:28672\n" TR"v[210:211],v8 offset:30720\n" MF"v[228:231],v[148:151],a[236:239],v[228:231]\n" TR"v[212:213],v9 offset:28672\n" TR"v[214:215],v9 offset:30720\n"
 MF"v[232:235],v[152:155],a[236:239],v[232:235]\n" TR"v[216:217],v10 offset:28672\n" WL"(8)\n" TR"v[218:219],v10 offset:30720\n" MF"v[236:239],v[156:159],a[236:239],v[236:239]\n"
 TR"v[220:221],v11 offset:28672\n" TR"v[222:223],v11 offset:30720\n" WV"(16)\n""s_barrier\n" WV"(0)\n""v_cmp_ne_u64 vcc,s[98:99],v[252:253]\n""s_nop 1\n""s_cmp_eq_u64 vcc,0\n"
"s_cbranch_scc1 Lokpx_%=\n""s_mov_b32 s57,32768\n""Lsppx_%=:\n""s_sleep 2\n""buffer_load_dwordx2 v[252:253],v248,s[92:95],0 offen sc1\n" WV"(0)\n""v_cmp_ne_u64 vcc,s[98:99],v[252:253]\n""s_nop 1\n"
"s_cmp_eq_u64 vcc,0\n""s_cbranch_scc1 Lokpx_%=\n""s_sub_u32 s57,s57,1\n""s_cmp_lg_u32 s57,0\n""s_cbranch_scc1 Lsppx_%=\n""Lokpx_%=:\n" MF"v[224:227],v[160:163],a[240:243],v[224:227]\n"
 TR"v[96:97],v16 offset:0\n" TR"v[98:99],v16 offset:2048\n" MF"v[228:231],v[164:167],a[240:243],v[228:231]\n" TR"v[100:101],v17 offset:0\n" TR"v[102:103],v17 offset:2048\n""s_lshl_b32 s50,s51,4\n"
"s_lshl_b32 s57,s48,1\n" SA"s50,s50,s57\n""s_lshl_b32 s50,s50,10\n" SA"s45,s50,0\n" BL"v[64:67],v0,s[84:87],s45 offen offset:0 sc1\n" MF"v[232:235],v[168:171],a[240:243],v[232:235]\n" WL"(8)\n"
 TR"v[104:105],v18 offset:0\n" TR"v[106:107],v18 offset:2048\n" MF"v[236:239],v[172:175],a[240:243],v[236:239]\n" TR"v[108:109],v19 offset:0\n" TR"v[110:111],v19 offset:2048\n"
 BL"v[68:71],v0,s[84:87],s45 offen offset:1024 sc1\n" MF"v[224:227],v[176:179],a[244:247],v[224:227]\n" TR"v[112:113],v16 offset:4096\n" TR"v[114:115],v16 offset:6144\n"
 MF"v[228:231],v[180:183],a[244:247],v[228:231]\n" TR"v[116:117],v17 offset:4096\n" WL"(8)\n" TR"v[118:119],v17 offset:6144\n" SA"s45,s50,2097152\n" BL"v[72:75],v0,s[84:87],s45 offen offset:0 sc1\n"
 MF"v[232:235],v[184:187],a[244:247],v[232:235]\n" TR"v[120:121],v18 offset:4096\n" TR"v[122:123],v18 offset:6144\n" MF"v[236:239],v[188:191],a[244:247],v[236:239]\n" TR"v[124:125],v19 offset:4096\n"
 TR"v[126:127],v19 offset:6144\n" BL"v[76:79],v0,s[84:87],s45 offen offset:1024 sc1\n" MF"v[224:227],v[192:195],a[248:251],v[224:227]\n" TR"v[128:129],v16 offset:8192\n"
 TR"v[130:131],v16 offset:10240\n" MF"v[228:231],v[196:199],a[248:251],v[228:231]\n" WL"(8)\n" TR"v[132:133],v17 offset:8192\n" TR"v[134:135],v17 offset:10240\n" SA"s45,s50,4194304\n"
 BL"v[80:83],v0,s[84:87],s45 offen offset:0 sc1\n" MF"v[232:235],v[200:203],a[248:251],v[232:235]\n" TR"v[136:137],v18 offset:8192\n" TR"v[138:139],v18 offset:10240\n"
 MF"v[236:239],v[204:207],a[248:251],v[236:239]\n" TR"v[140:141],v19 offset:8192\n" TR"v[142:143],v19 offset:10240\n" BL"v[84:87],v0,s[84:87],s45 offen offset:1024 sc1\n"
 MF"v[224:227],v[208:211],a[252:255],v[224:227]\n" TR"v[144:145],v16 offset:12288\n" WL"(8)\n" TR"v[146:147],v16 offset:14336\n" MF"v[228:231],v[212:215],a[252:255],v[228:231]\n"
 TR"v[148:149],v17 offset:12288\n" TR"v[150:151],v17 offset:14336\n" SA"s45,s50,6291456\n" BL"v[88:91],v0,s[84:87],s45 offen offset:0 sc1\n" MF"v[232:235],v[216:219],a[252:255],v[232:235]\n"
 TR"v[152:153],v18 offset:12288\n" TR"v[154:155],v18 offset:14336\n" MF"v[236:239],v[220:223],a[252:255],v[236:239]\n" TR"v[156:157],v19 offset:12288\n" TR"v[158:159],v19 offset:14336\n"
 BL"v[92:95],v0,s[84:87],s45 offen offset:1024 sc1\n" WV"(16)\n""s_barrier\n" MF"v[224:227],v[96:99],a[128:131],v[224:227]\n" WL"(8)\n" TR"v[160:161],v16 offset:16384\n"
 TR"v[162:163],v16 offset:18432\n" MF"v[228:231],v[100:103],a[128:131],v[228:231]\n" TR"v[164:165],v17 offset:16384\n" TR"v[166:167],v17 offset:18432\n"
 MF"v[232:235],v[104:107],a[128:131],v[232:235]\n" TR"v[168:169],v18 offset:16384\n" TR"v[170:171],v18 offset:18432\n" MF"v[236:239],v[108:111],a[128:131],v[236:239]\n"
 TR"v[172:173],v19 offset:16384\n" WL"(8)\n" TR"v[174:175],v19 offset:18432\n" MF"v[224:227],v[112:115],a[132:135],v[224:227]\n" TR"v[176:177],v16 offset:20480\n" TR"v[178:179],v16 offset:22528\n"
 MF"v[228:231],v[116:119],a[132:135],v[228:231]\n" TR"v[180:181],v17 offset:20480\n" TR"v[182:183],v17 offset:22528\n" MF"v[232:235],v[120:123],a[132:135],v[232:235]\n"
 TR"v[184:185],v18 offset:20480\n" TR"v[186:187],v18 offset:22528\n" MF"v[236:239],v[124:127],a[132:135],v[236:239]\n" WL"(8)\n" TR"v[188:189],v19 offset:20480\n" TR"v[190:191],v19 offset:22528\n"
 MF"v[224:227],v[128:131],a[136:139],v[224:227]\n" TR"v[192:193],v16 offset:24576\n" TR"v[194:195],v16 offset:26624\n" MF"v[228:231],v[132:135],a[136:139],v[228:231]\n"
 TR"v[196:197],v17 offset:24576\n" TR"v[198:199],v17 offset:26624\n" MF"v[232:235],v[136:139],a[136:139],v[232:235]\n" TR"v[200:201],v18 offset:24576\n" WL"(8)\n" TR"v[202:203],v18 offset:26624\n"
 MF"v[236:239],v[140:143],a[136:139],v[236:239]\n" TR"v[204:205],v19 offset:24576\n" TR"v[206:207],v19 offset:26624\n" MF"v[224:227],v[144:147],a[140:143],v[224:227]\n"
 TR"v[208:209],v16 offset:28672\n" TR"v[210:211],v16 offset:30720\n" MF"v[228:231],v[148:151],a[140:143],v[228:231]\n" TR"v[212:213],v17 offset:28672\n" TR"v[214:215],v17 offset:30720\n"
 MF"v[232:235],v[152:155],a[140:143],v[232:235]\n" WL"(8)\n" TR"v[216:217],v18 offset:28672\n" TR"v[218:219],v18 offset:30720\n" MF"v[236:239],v[156:159],a[140:143],v[236:239]\n"
 TR"v[220:221],v19 offset:28672\n" TR"v[222:223],v19 offset:30720\n" WV"(9)\n""s_barrier\n" MF"v[224:227],v[160:163],a[144:147],v[224:227]\n" TR"v[96:97],v4 offset:0\n" TR"v[98:99],v4 offset:2048\n"
 MF"v[228:231],v[164:167],a[144:147],v[228:231]\n" TR"v[100:101],v5 offset:0\n" WL"(8)\n" TR"v[102:103],v5 offset:2048\n" MF"v[232:235],v[168:171],a[144:147],v[232:235]\n" TR"v[104:105],v6 offset:0\n"
 TR"v[106:107],v6 offset:2048\n" MF"v[236:239],v[172:175],a[144:147],v[236:239]\n" TR"v[108:109],v7 offset:0\n" TR"v[110:111],v7 offset:2048\n" MF"v[224:227],v[176:179],a[148:151],v[224:227]\n"
 TR"v[112:113],v4 offset:4096\n" TR"v[114:115],v4 offset:6144\n" MF"v[228:231],v[180:183],a[148:151],v[228:231]\n" WL"(8)\n" TR"v[116:117],v5 offset:4096\n" TR"v[118:119],v5 offset:6144\n"
 MF"v[232:235],v[184:187],a[148:151],v[232:235]\n" TR"v[120:121],v6 offset:4096\n" TR"v[122:123],v6 offset:6144\n" MF"v[236:239],v[188:191],a[148:151],v[236:239]\n" TR"v[124:125],v7 offset:4096\n"
 TR"v[126:127],v7 offset:6144\n" MF"v[224:227],v[192:195],a[152:155],v[224:227]\n" TR"v[128:129],v4 offset:8192\n" WL"(8)\n" TR"v[130:131],v4 offset:10240\n"
 MF"v[228:231],v[196:199],a[152:155],v[228:231]\n" TR"v[132:133],v5 offset:8192\n" TR"v[134:135],v5 offset:10240\n" MF"v[232:235],v[200:203],a[152:155],v[232:235]\n" TR"v[136:137],v6 offset:8192\n"
 TR"v[138:139],v6 offset:10240\n" MF"v[236:239],v[204:207],a[152:155],v[236:239]\n" TR"v[140:141],v7 offset:8192\n" TR"v[142:143],v7 offset:10240\n" MF"v[224:227],v[208:211],a[156:159],v[224:227]\n"
 MF"v[228:231],v[212:215],a[156:159],v[228:231]\n" MF"v[232:235],v[216:219],a[156:159],v[232:235]\n" MF"v[236:239],v[220:223],a[156:159],v[236:239]\n" MF"v[224:227],v[96:99],a[160:163],v[224:227]\n"
 MF"v[228:231],v[100:103],a[160:163],v[228:231]\n" MF"v[232:235],v[104:107],a[160:163],v[232:235]\n" MF"v[236:239],v[108:111],a[160:163],v[236:239]\n" WL"(14)\n"
 MF"v[224:227],v[112:115],a[164:167],v[224:227]\n" WL"(12)\n" MF"v[228:231],v[116:119],a[164:167],v[228:231]\n" WL"(10)\n" MF"v[232:235],v[120:123],a[164:167],v[232:235]\n" WL"(8)\n"
 MF"v[236:239],v[124:127],a[164:167],v[236:239]\n" WL"(6)\n" MF"v[224:227],v[128:131],a[168:171],v[224:227]\n" WL"(4)\n" MF"v[228:231],v[132:135],a[168:171],v[228:231]\n" WL"(2)\n"
 MF"v[232:235],v[136:139],a[168:171],v[232:235]\n" WL"(0)\n" MF"v[236:239],v[140:143],a[168:171],v[236:239]\n" WV"(0)\n""v_lshlrev_b32 v96,16,v64\n""v_and_b32 v97,0xffff0000,v64\n"
"v_lshlrev_b32 v98,16,v65\n""v_and_b32 v99,0xffff0000,v65\n""v_lshlrev_b32 v100,16,v66\n""v_and_b32 v101,0xffff0000,v66\n""v_lshlrev_b32 v102,16,v67\n""v_and_b32 v103,0xffff0000,v67\n"
"v_lshlrev_b32 v104,16,v68\n""v_and_b32 v105,0xffff0000,v68\n""v_lshlrev_b32 v106,16,v69\n""v_and_b32 v107,0xffff0000,v69\n""v_lshlrev_b32 v108,16,v70\n""v_and_b32 v109,0xffff0000,v70\n"
"v_lshlrev_b32 v110,16,v71\n""v_and_b32 v111,0xffff0000,v71\n""v_lshlrev_b32 v13,16,v72\n""v_and_b32 v15,0xffff0000,v72\n""v_add_f32 v96,v96,v13\n""v_add_f32 v97,v97,v15\n""v_lshlrev_b32 v13,16,v73\n"
"v_and_b32 v15,0xffff0000,v73\n""v_add_f32 v98,v98,v13\n""v_add_f32 v99,v99,v15\n""v_lshlrev_b32 v13,16,v74\n""v_and_b32 v15,0xffff0000,v74\n""v_add_f32 v100,v100,v13\n""v_add_f32 v101,v101,v15\n"
"v_lshlrev_b32 v13,16,v75\n""v_and_b32 v15,0xffff0000,v75\n""v_add_f32 v102,v102,v13\n""v_add_f32 v103,v103,v15\n""v_lshlrev_b32 v13,16,v76\n""v_and_b32 v15,0xffff0000,v76\n"
"v_add_f32 v104,v104,v13\n""v_add_f32 v105,v105,v15\n""v_lshlrev_b32 v13,16,v77\n""v_and_b32 v15,0xffff0000,v77\n""v_add_f32 v106,v106,v13\n""v_add_f32 v107,v107,v15\n""v_lshlrev_b32 v13,16,v78\n"
"v_and_b32 v15,0xffff0000,v78\n""v_add_f32 v108,v108,v13\n""v_add_f32 v109,v109,v15\n""v_lshlrev_b32 v13,16,v79\n""v_and_b32 v15,0xffff0000,v79\n""v_add_f32 v110,v110,v13\n""v_add_f32 v111,v111,v15\n"
"v_lshlrev_b32 v13,16,v80\n""v_and_b32 v15,0xffff0000,v80\n""v_add_f32 v96,v96,v13\n""v_add_f32 v97,v97,v15\n""v_lshlrev_b32 v13,16,v81\n""v_and_b32 v15,0xffff0000,v81\n""v_add_f32 v98,v98,v13\n"
"v_add_f32 v99,v99,v15\n""v_lshlrev_b32 v13,16,v82\n""v_and_b32 v15,0xffff0000,v82\n""v_add_f32 v100,v100,v13\n""v_add_f32 v101,v101,v15\n""v_lshlrev_b32 v13,16,v83\n""v_and_b32 v15,0xffff0000,v83\n"
"v_add_f32 v102,v102,v13\n""v_add_f32 v103,v103,v15\n""v_lshlrev_b32 v13,16,v84\n""v_and_b32 v15,0xffff0000,v84\n""v_add_f32 v104,v104,v13\n""v_add_f32 v105,v105,v15\n""v_lshlrev_b32 v13,16,v85\n"
"v_and_b32 v15,0xffff0000,v85\n""v_add_f32 v106,v106,v13\n""v_add_f32 v107,v107,v15\n""v_lshlrev_b32 v13,16,v86\n""v_and_b32 v15,0xffff0000,v86\n""v_add_f32 v108,v108,v13\n""v_add_f32 v109,v109,v15\n"
"v_lshlrev_b32 v13,16,v87\n""v_and_b32 v15,0xffff0000,v87\n""v_add_f32 v110,v110,v13\n""v_add_f32 v111,v111,v15\n""v_lshlrev_b32 v13,16,v88\n""v_and_b32 v15,0xffff0000,v88\n""v_add_f32 v96,v96,v13\n"
"v_add_f32 v97,v97,v15\n""v_lshlrev_b32 v13,16,v89\n""v_and_b32 v15,0xffff0000,v89\n""v_add_f32 v98,v98,v13\n""v_add_f32 v99,v99,v15\n""v_lshlrev_b32 v13,16,v90\n""v_and_b32 v15,0xffff0000,v90\n"
"v_add_f32 v100,v100,v13\n""v_add_f32 v101,v101,v15\n""v_lshlrev_b32 v13,16,v91\n""v_and_b32 v15,0xffff0000,v91\n""v_add_f32 v102,v102,v13\n""v_add_f32 v103,v103,v15\n""v_lshlrev_b32 v13,16,v92\n"
"v_and_b32 v15,0xffff0000,v92\n""v_add_f32 v104,v104,v13\n""v_add_f32 v105,v105,v15\n""v_lshlrev_b32 v13,16,v93\n""v_and_b32 v15,0xffff0000,v93\n""v_add_f32 v106,v106,v13\n""v_add_f32 v107,v107,v15\n"
"v_lshlrev_b32 v13,16,v94\n""v_and_b32 v15,0xffff0000,v94\n""v_add_f32 v108,v108,v13\n""v_add_f32 v109,v109,v15\n""v_lshlrev_b32 v13,16,v95\n""v_and_b32 v15,0xffff0000,v95\n"
"v_add_f32 v110,v110,v13\n""v_add_f32 v111,v111,v15\n""v_lshlrev_b32 v13,16,v24\n""v_and_b32 v15,0xffff0000,v24\n""v_add_f32 v224,v224,v13\n""v_add_f32 v225,v225,v15\n""v_lshlrev_b32 v13,16,v25\n"
"v_and_b32 v15,0xffff0000,v25\n""v_add_f32 v226,v226,v13\n""v_add_f32 v227,v227,v15\n""v_lshlrev_b32 v13,16,v26\n""v_and_b32 v15,0xffff0000,v26\n""v_add_f32 v228,v228,v13\n""v_add_f32 v229,v229,v15\n"
"v_lshlrev_b32 v13,16,v27\n""v_and_b32 v15,0xffff0000,v27\n""v_add_f32 v230,v230,v13\n""v_add_f32 v231,v231,v15\n""v_lshlrev_b32 v13,16,v28\n""v_and_b32 v15,0xffff0000,v28\n"
"v_add_f32 v232,v232,v13\n""v_add_f32 v233,v233,v15\n""v_lshlrev_b32 v13,16,v29\n""v_and_b32 v15,0xffff0000,v29\n""v_add_f32 v234,v234,v13\n""v_add_f32 v235,v235,v15\n""v_lshlrev_b32 v13,16,v30\n"
"v_and_b32 v15,0xffff0000,v30\n""v_add_f32 v236,v236,v13\n""v_add_f32 v237,v237,v15\n""v_lshlrev_b32 v13,16,v31\n""v_and_b32 v15,0xffff0000,v31\n""v_add_f32 v238,v238,v13\n""v_add_f32 v239,v239,v15\n"
 VM"v112,0xbfb8aa3b,v224\n" VM"v113,0xbfb8aa3b,v225\n" VM"v114,0xbfb8aa3b,v226\n" VM"v115,0xbfb8aa3b,v227\n" VM"v116,0xbfb8aa3b,v228\n" VM"v117,0xbfb8aa3b,v229\n" VM"v118,0xbfb8aa3b,v230\n"
 VM"v119,0xbfb8aa3b,v231\n" VM"v120,0xbfb8aa3b,v232\n" VM"v121,0xbfb8aa3b,v233\n" VM"v122,0xbfb8aa3b,v234\n" VM"v123,0xbfb8aa3b,v235\n" VM"v124,0xbfb8aa3b,v236\n" VM"v125,0xbfb8aa3b,v237\n"
 VM"v126,0xbfb8aa3b,v238\n" VM"v127,0xbfb8aa3b,v239\n" EX"v112,v112\n" EX"v113,v113\n" EX"v114,v114\n" EX"v115,v115\n" EX"v116,v116\n" EX"v117,v117\n" EX"v118,v118\n" EX"v119,v119\n" EX"v120,v120\n"
 EX"v121,v121\n" EX"v122,v122\n" EX"v123,v123\n" EX"v124,v124\n" EX"v125,v125\n" EX"v126,v126\n" EX"v127,v127\n""v_add_f32 v112,1.0,v112\n""v_add_f32 v113,1.0,v113\n""v_add_f32 v114,1.0,v114\n"
"v_add_f32 v115,1.0,v115\n""v_add_f32 v116,1.0,v116\n""v_add_f32 v117,1.0,v117\n""v_add_f32 v118,1.0,v118\n""v_add_f32 v119,1.0,v119\n""v_add_f32 v120,1.0,v120\n""v_add_f32 v121,1.0,v121\n"
"v_add_f32 v122,1.0,v122\n""v_add_f32 v123,1.0,v123\n""v_add_f32 v124,1.0,v124\n""v_add_f32 v125,1.0,v125\n""v_add_f32 v126,1.0,v126\n""v_add_f32 v127,1.0,v127\n""v_rcp_f32 v112,v112\n"
"v_rcp_f32 v113,v113\n""v_rcp_f32 v114,v114\n""v_rcp_f32 v115,v115\n""v_rcp_f32 v116,v116\n""v_rcp_f32 v117,v117\n""v_rcp_f32 v118,v118\n""v_rcp_f32 v119,v119\n""v_rcp_f32 v120,v120\n"
"v_rcp_f32 v121,v121\n""v_rcp_f32 v122,v122\n""v_rcp_f32 v123,v123\n""v_rcp_f32 v124,v124\n""v_rcp_f32 v125,v125\n""v_rcp_f32 v126,v126\n""v_rcp_f32 v127,v127\n" VM"v112,v112,v224\n"
 VM"v113,v113,v225\n" VM"v114,v114,v226\n" VM"v115,v115,v227\n" VM"v116,v116,v228\n" VM"v117,v117,v229\n" VM"v118,v118,v230\n" VM"v119,v119,v231\n" VM"v120,v120,v232\n" VM"v121,v121,v233\n"
 VM"v122,v122,v234\n" VM"v123,v123,v235\n" VM"v124,v124,v236\n" VM"v125,v125,v237\n" VM"v126,v126,v238\n" VM"v127,v127,v239\n""v_lshlrev_b32 v13,16,v56\n""v_and_b32 v15,0xffff0000,v56\n"
"v_fma_f32 v112,v112,v96,v13\n""v_fma_f32 v113,v113,v97,v15\n""v_lshlrev_b32 v13,16,v57\n""v_and_b32 v15,0xffff0000,v57\n""v_fma_f32 v114,v114,v98,v13\n""v_fma_f32 v115,v115,v99,v15\n"
"v_lshlrev_b32 v13,16,v58\n""v_and_b32 v15,0xffff0000,v58\n""v_fma_f32 v116,v116,v100,v13\n""v_fma_f32 v117,v117,v101,v15\n""v_lshlrev_b32 v13,16,v59\n""v_and_b32 v15,0xffff0000,v59\n"
"v_fma_f32 v118,v118,v102,v13\n""v_fma_f32 v119,v119,v103,v15\n""v_lshlrev_b32 v13,16,v60\n""v_and_b32 v15,0xffff0000,v60\n""v_fma_f32 v120,v120,v104,v13\n""v_fma_f32 v121,v121,v105,v15\n"
"v_lshlrev_b32 v13,16,v61\n""v_and_b32 v15,0xffff0000,v61\n""v_fma_f32 v122,v122,v106,v13\n""v_fma_f32 v123,v123,v107,v15\n""v_lshlrev_b32 v13,16,v62\n""v_and_b32 v15,0xffff0000,v62\n"
"v_fma_f32 v124,v124,v108,v13\n""v_fma_f32 v125,v125,v109,v15\n""v_lshlrev_b32 v13,16,v63\n""v_and_b32 v15,0xffff0000,v63\n""v_fma_f32 v126,v126,v110,v13\n""v_fma_f32 v127,v127,v111,v15\n"
 CV"v128,v112,v113\n" CV"v129,v114,v115\n" CV"v130,v116,v117\n" CV"v131,v118,v119\n" CV"v132,v120,v121\n" CV"v133,v122,v123\n" CV"v134,v124,v125\n" CV"v135,v126,v127\n""v_readlane_b32 s72,v254,20\n"
"v_readlane_b32 s73,v254,21\n""s_mov_b32 s74,2097152\n""s_mov_b32 s75,0x20000\n""v_bfe_u32 v13,v0,4,4\n""v_lshlrev_b32 v13,10,v13\n""v_lshrrev_b32 v15,8,v0\n""v_lshl_add_u32 v13,v15,3,v13\n"
"s_lshl_b32 s58,s51,14\n""s_lshl_b32 s59,s48,7\n" SA"s58,s58,s59\n""buffer_store_dwordx2 v[128:129],v13,s[72:75],s58 offen offset:0\n"
"buffer_store_dwordx2 v[130:131],v13,s[72:75],s58 offen offset:32\n""buffer_store_dwordx2 v[132:133],v13,s[72:75],s58 offen offset:64\n"
"buffer_store_dwordx2 v[134:135],v13,s[72:75],s58 offen offset:96\n""s_cmp_lg_u32 s96,0\n""s_cbranch_scc1 Lep_%=\n" VV"v12,s98\n" VV"v13,s99\n" VV"v14,0\n"
"buffer_store_dwordx2 v[12:13],v14,s[92:95],0 offen sc1\n""Lep_%=:\n" WV"(0)\n""s_branch .Lmk_final\n"".Lmk_re:\n""s_load_dwordx8 s[60:67],s[34:35],0\n""s_load_dwordx8 s[68:75],s[34:35],32\n"
"s_load_dwordx8 s[76:83],s[34:35],64\n" WL"(0)\n""v_writelane_b32 v254,s60,0\n""v_writelane_b32 v254,s61,1\n""v_writelane_b32 v254,s62,2\n""v_writelane_b32 v254,s63,3\n""v_writelane_b32 v254,s64,4\n"
"v_writelane_b32 v254,s65,5\n""v_writelane_b32 v254,s66,6\n""v_writelane_b32 v254,s67,7\n""v_writelane_b32 v254,s68,8\n""v_writelane_b32 v254,s69,9\n""v_writelane_b32 v254,s70,10\n"
"v_writelane_b32 v254,s71,11\n""v_writelane_b32 v254,s72,12\n""v_writelane_b32 v254,s73,13\n""v_writelane_b32 v254,s74,14\n""v_writelane_b32 v254,s75,15\n""v_writelane_b32 v254,s76,16\n"
"v_writelane_b32 v254,s77,17\n""v_writelane_b32 v254,s78,18\n""v_writelane_b32 v254,s79,19\n""v_writelane_b32 v254,s80,20\n""v_writelane_b32 v254,s81,21\n""v_writelane_b32 v254,s82,22\n"
"v_writelane_b32 v254,s83,23\n""s_mov_b64 s[52:53],s[82:83]\n""s_mov_b64 s[92:93],s[82:83]\n""s_mov_b32 s94,65536\n""s_mov_b32 s95,0x20000\n""s_lshl_b32 s57,s96,3\n" SA"s57,s57,4096\n" VV"v14,0\n"
"buffer_load_dwordx2 v[12:13],v14,s[92:95],s57 offen sc1\n" WV"(0)\n""v_readfirstlane_b32 s98,v12\n""v_readfirstlane_b32 s99,v13\n""s_nop 3\n""v_mbcnt_lo_u32_b32 v247,-1,0\n"
"v_mbcnt_hi_u32_b32 v247,-1,v247\n""v_min_u32 v247,19,v247\n""s_lshr_b32 s62,s96,5\n""s_and_b32 s63,s96,7\n""s_lshl_b32 s63,s63,2\n""s_bfe_u32 s57,s96,0x20003\n" SA"s63,s63,s57\n"
"s_lshl_b32 s61,s63,2\n" SA"s61,s61,s36\n""s_lshl_b32 s57,s63,6\n" SA"s57,s57,16384\n" VV"v244,s57\n" SA"s57,s62,0\n""s_lshl_b32 s57,s57,5\n" SA"s57,s57,4096\n""s_sub_u32 s57,s57,64\n" VV"v245,s57\n"
"v_cmp_le_u32 vcc,8,v247\n""s_nop 1\n""v_cndmask_b32 v244,v244,v245,vcc\n" SA"s57,s62,8\n""s_lshl_b32 s57,s57,5\n" SA"s57,s57,4096\n""s_sub_u32 s57,s57,96\n" VV"v245,s57\n""v_cmp_le_u32 vcc,12,v247\n"
"s_nop 1\n""v_cndmask_b32 v244,v244,v245,vcc\n" SA"s57,s62,16\n""s_lshl_b32 s57,s57,5\n" SA"s57,s57,4096\n""s_sub_u32 s57,s57,128\n" VV"v245,s57\n""v_cmp_le_u32 vcc,16,v247\n""s_nop 1\n"
"v_cndmask_b32 v244,v244,v245,vcc\n""v_lshl_add_u32 v244,v247,3,v244\n""buffer_load_dwordx2 v[248:249],v244,s[92:95],0 offen sc1\n" WV"(0)\n""v_cmp_ne_u64 vcc,s[98:99],v[248:249]\n""s_nop 1\n"
"s_cmp_eq_u64 vcc,0\n""s_cbranch_scc1 Lokp3b_%=\n""s_mov_b32 s57,32768\n""Lspp3b_%=:\n""s_sleep 2\n""buffer_load_dwordx2 v[248:249],v244,s[92:95],0 offen sc1\n" WV"(0)\n"
"v_cmp_ne_u64 vcc,s[98:99],v[248:249]\n""s_nop 1\n""s_cmp_eq_u64 vcc,0\n""s_cbranch_scc1 Lokp3b_%=\n""s_sub_u32 s57,s57,1\n""s_cmp_lg_u32 s57,0\n""s_cbranch_scc1 Lspp3b_%=\n""Lokp3b_%=:\n"
"v_mbcnt_lo_u32_b32 v12,-1,0\n""v_mbcnt_hi_u32_b32 v12,-1,v12\n""v_lshlrev_b32 v0,4,v12\n""v_lshrrev_b32 v13,3,v12\n""v_mul_u32_u24 v13,2752,v13\n""v_lshrrev_b32 v14,4,v12\n"
"v_lshlrev_b32 v15,1,v14\n""v_and_b32 v2,7,v12\n""v_xor_b32 v15,v15,v2\n""v_lshl_add_u32 v2,v15,4,v13\n" VV"v3,v0\n""s_lshl_b32 s63,s36,12\n""s_lshr_b32 s38,s96,5\n""s_and_b32 s39,s96,7\n"
"s_lshl_b32 s39,s39,2\n""s_bfe_u32 s57,s96,0x20003\n" SA"s39,s39,s57\n""s_lshl_b32 s42,s39,2\n" SA"s42,s42,s36\n""v_readlane_b32 s72,v254,16\n""v_readlane_b32 s73,v254,17\n""s_mov_b32 s74,1409024\n"
"s_mov_b32 s75,0x20000\n" SA"s76,s52,7340032\n""s_addc_u32 s77,s53,0\n""s_mov_b32 s78,2097152\n""s_mov_b32 s79,0x20000\n""s_mul_i32 s40,s36,88064\n""s_lshl_b32 s57,s38,7\n" SA"s40,s40,s57\n"
 SA"s46,s40,1024\n" SA"s47,s40,2048\n""s_cmp_gt_u32 s38,5\n""s_cselect_b32 s47,0x70000000,s47\n""s_lshl_b32 s41,s42,14\n" SA"m0,s63,0\n" SA"s58,s40,0\n" BL"v2,s[72:75],s58 offen lds\n"
 SA"m0,s63,1024\n" SA"s58,s40,22016\n" BL"v2,s[72:75],s58 offen lds\n" SA"m0,s63,2048\n" SA"s58,s40,44032\n" BL"v2,s[72:75],s58 offen lds\n" SA"m0,s63,3072\n" SA"s58,s40,66048\n"
 BL"v2,s[72:75],s58 offen lds\n" SA"s59,s41,0\n" BL"v[16:19],v3,s[76:79],s59 offen offset:0 sc1\n" BL"v[20:23],v3,s[76:79],s59 offen offset:1024 sc1\n"
 BL"v[24:27],v3,s[76:79],s59 offen offset:2048 sc1\n" BL"v[28:31],v3,s[76:79],s59 offen offset:3072 sc1\n" SA"m0,s63,16384\n" SA"s58,s40,352256\n" BL"v2,s[72:75],s58 offen lds\n" SA"m0,s63,17408\n"
 SA"s58,s40,374272\n" BL"v2,s[72:75],s58 offen lds\n" SA"m0,s63,18432\n" SA"s58,s40,396288\n" BL"v2,s[72:75],s58 offen lds\n" SA"m0,s63,19456\n" SA"s58,s40,418304\n" BL"v2,s[72:75],s58 offen lds\n"
 SA"s59,s41,4096\n" BL"v[32:35],v3,s[76:79],s59 offen offset:0 sc1\n" BL"v[36:39],v3,s[76:79],s59 offen offset:1024 sc1\n" BL"v[40:43],v3,s[76:79],s59 offen offset:2048 sc1\n"
 BL"v[44:47],v3,s[76:79],s59 offen offset:3072 sc1\n" SA"m0,s63,32768\n" SA"s58,s40,704512\n" BL"v2,s[72:75],s58 offen lds\n" SA"m0,s63,33792\n" SA"s58,s40,726528\n" BL"v2,s[72:75],s58 offen lds\n"
 SA"m0,s63,34816\n" SA"s58,s40,748544\n" BL"v2,s[72:75],s58 offen lds\n" SA"m0,s63,35840\n" SA"s58,s40,770560\n" BL"v2,s[72:75],s58 offen lds\n" SA"s59,s41,8192\n"
 BL"v[48:51],v3,s[76:79],s59 offen offset:0 sc1\n" BL"v[52:55],v3,s[76:79],s59 offen offset:1024 sc1\n" BL"v[56:59],v3,s[76:79],s59 offen offset:2048 sc1\n"
 BL"v[60:63],v3,s[76:79],s59 offen offset:3072 sc1\n" SA"m0,s63,49152\n" SA"s58,s40,1056768\n" BL"v2,s[72:75],s58 offen lds\n" SA"m0,s63,50176\n" SA"s58,s40,1078784\n" BL"v2,s[72:75],s58 offen lds\n"
 SA"m0,s63,51200\n" SA"s58,s40,1100800\n" BL"v2,s[72:75],s58 offen lds\n" SA"m0,s63,52224\n" SA"s58,s40,1122816\n" BL"v2,s[72:75],s58 offen lds\n" SA"s59,s41,12288\n"
 BL"v[64:67],v3,s[76:79],s59 offen offset:0 sc1\n" BL"v[68:71],v3,s[76:79],s59 offen offset:1024 sc1\n" BL"v[72:75],v3,s[76:79],s59 offen offset:2048 sc1\n"
 BL"v[76:79],v3,s[76:79],s59 offen offset:3072 sc1\n" SA"m0,s63,65536\n" SA"s58,s46,0\n" BL"v2,s[72:75],s58 offen lds\n" SA"m0,s63,66560\n" SA"s58,s46,22016\n" BL"v2,s[72:75],s58 offen lds\n"
 SA"m0,s63,67584\n" SA"s58,s46,44032\n" BL"v2,s[72:75],s58 offen lds\n" SA"m0,s63,68608\n" SA"s58,s46,66048\n" BL"v2,s[72:75],s58 offen lds\n" SA"m0,s63,81920\n" SA"s58,s46,352256\n"
 BL"v2,s[72:75],s58 offen lds\n" SA"m0,s63,82944\n" SA"s58,s46,374272\n" BL"v2,s[72:75],s58 offen lds\n" SA"m0,s63,83968\n" SA"s58,s46,396288\n" BL"v2,s[72:75],s58 offen lds\n" SA"m0,s63,84992\n"
 SA"s58,s46,418304\n" BL"v2,s[72:75],s58 offen lds\n" WV"(0)\n""s_barrier\n""s_branch Lg3_%=\n""Lexit_%=:\n" WV"(0)\n""s_mov_b32 %[st],s54\n"
  : [st] "=s"(st) : [ka] "s"(ka), [w] "s"(w), [b] "s"(b) : CLOBBERS);
  asm volatile("" : "+s"(ka));
  {
    const unsigned lane = lane_id(), h = b & 31, p = b >> 5;
    const u16* wq = KARG(const u16*, 2); char* ws = KARG(char*, 11);
    __builtin_amdgcn_fence(__ATOMIC_ACQUIRE, "agent");
    asm volatile("s_waitcnt vmcnt(0)" ::: "memory");
    __syncthreads();
    if (st & 1) attn_slow_item(h, p, w * 64 + lane, wq, ws, (LAS char*)dyn_lds + 1024);
    if (st & 2) attn_slow_item(h, 15 - p, w * 64 + lane, wq, ws, (LAS char*)dyn_lds + 1024);
    __builtin_amdgcn_fence(__ATOMIC_RELEASE, "agent");
    asm volatile("s_waitcnt vmcnt(0)" ::: "memory");
    __syncthreads();
  }
  asm volatile("s_mov_b32 s36, %[w]\n\ts_mov_b32 s96, %[b]\n\ts_mov_b64 s[34:35], %[ka]\n\ts_branch .Lmk_re\n.Lmk_final:"
  :: [ka] "s"(ka), [w] "s"(w), [b] "s"(b) : CLOBBERS);
}

extern "C" void kernel_launch(void* const* d_in, const int* in_sizes, int n_in, void* d_out, int out_size, void* d_ws, size_t ws_size, hipStream_t stream) {
  (void)hipFuncSetAttribute((const void*)mega_kernel, hipFuncAttributeMaxDynamicSharedMemorySize, 163840);
  mega_kernel<<<256, 256, 163840, stream>>>((const u16*)d_in[0], (const u16*)d_in[1], (const u16*)d_in[2], (const u16*)d_in[3], (const u16*)d_in[4], (const u16*)d_in[5],
      (const u16*)d_in[7], (const u16*)d_in[8], (const u16*)d_in[9], (const u16*)d_in[10], (u16*)d_out, (char*)d_ws);
}
```
